```python
import torch

def _softmax(x, axis=-1):
    return torch.softmax(x, dim=axis)
def _gelu(x):
    return torch.nn.functional.gelu(x, approximate="tanh")
def _silu(x):
    return torch.nn.functional.silu(x)
def _sigmoid(x):
    return torch.sigmoid(x)
def _layernorm(x, axis=-1, eps=1e-5):
    m = x.mean(dim=axis, keepdim=True)
    v = x.var(dim=axis, keepdim=True, unbiased=False)
    return (x - m) / torch.sqrt(v + eps)
def _scatter_add(tgt, idx, src, axis=0):
    return tgt.clone().index_add_(axis, idx, src)
def _segment_sum(src, seg, n):
    out = torch.zeros((n,) + src.shape[1:], dtype=src.dtype)
    return out.index_add_(0, seg, src)
def _pad(x, pw):
    flat = tuple(v for pair in reversed(pw) for v in pair)
    return torch.nn.functional.pad(x, flat)

kernel_name = 'gated_mix__ssm_conv_h3__glu_gated_glu__prompt'

def setup_inputs():

    _w = (lambda a: (lambda t: t.to(torch.bfloat16) if t.is_floating_point() else t)(torch.as_tensor(a)))
    def _A_setup():
        B, S, D = 1, 2048, 640
        ks = 4
        return {
            "q": _w((lambda *s: torch.randn(*s, dtype=torch.bfloat16) * 0.05)(B, S, D)),
            "k": _w((lambda *s: torch.randn(*s, dtype=torch.bfloat16) * 0.4)(B, S, D)),
            "v": _w((lambda *s: torch.randn(*s, dtype=torch.bfloat16) * 0.4)(B, S, D)),
            "shift_k": _w((lambda *s: torch.randn(*s, dtype=torch.bfloat16) * 0.4)(D, ks) * 0.5),
            "ssm_k": _w((lambda *s: torch.randn(*s, dtype=torch.bfloat16) * 1.6)(D, S) * (1.0 / S ** 0.5)),
            "ssm_D": _w((lambda *s: torch.randn(*s, dtype=torch.bfloat16) * 0.4)(D)),
        }
    _A_in = _A_setup()

    def _B_setup():
        B, S, D, F = 1, 2048, 640, 1536
        return {
            "x": _w((lambda *s: torch.randn(*s, dtype=torch.bfloat16) * 0.05)(B, S, D)),
            "w_gate": _w((lambda *s: torch.randn(*s, dtype=torch.bfloat16) * 0.4)(D, F)),
            "w_up": _w((lambda *s: torch.randn(*s, dtype=torch.bfloat16) * 0.05)(D, F)),
            "w_down": _w((lambda *s: torch.randn(*s, dtype=torch.bfloat16) * 0.05)(F, D)),
        }
    _B_in = _B_setup()

    return {
        "A_q": _A_in["q"],
        "A_k": _A_in["k"],
        "A_v": _A_in["v"],
        "A_shift_k": _A_in["shift_k"],
        "A_ssm_k": _A_in["ssm_k"],
        "A_ssm_D": _A_in["ssm_D"],
        "B_x": _B_in["x"],
        "B_w_gate": _B_in["w_gate"],
        "B_w_up": _B_in["w_up"],
        "B_w_down": _B_in["w_down"],
        "gate_w": _w((lambda *s: torch.randn(*s, dtype=torch.bfloat16) * 0.05)(640) * 16.0),
    }

def reference(A_q, A_k, A_v, A_shift_k, A_ssm_k, A_ssm_D, B_x, B_w_gate, B_w_up, B_w_down, gate_w):
    def _A_ref(q, k, v, shift_k, ssm_k, ssm_D):
        B, S, D = q.shape
        ks = shift_k.shape[-1]
        k_pad = _pad(k, ((0, 0), (ks - 1, 0), (0, 0)))
        k_shift = torch.zeros_like(k)
        for t in range(ks):
            k_shift = k_shift + k_pad[:, t:t + S, :] * shift_k[:, ks - 1 - t][None, None, :]
        kv = k_shift * v
        L = 2 * S
        kv_pad = _pad(kv, ((0, 0), (0, S), (0, 0)))
        kf_pad = _pad(ssm_k, ((0, 0), (0, S)))
        kv_f = (lambda x, axes=(-2,-1): torch.fft.rfft2(x.float(), dim=axes))(kv_pad[:, None, :, :], axes=(1, 2))
        kf_f = (lambda x, axes=(-2,-1): torch.fft.rfft2(x.float(), dim=axes))(kf_pad[:, None, :], axes=(1, 2))
        kf_f = (lambda x, *d: x.permute(*d))(kf_f, 1, 2, 0)
        y_f = kv_f * kf_f[None, :, :, :]
        y = (lambda x, s, axes=(-2,-1): torch.fft.irfft2(x, s=s, dim=axes).to(torch.bfloat16))(y_f, (1, L), axes=(1, 2))[:, 0, :S, :]
        y = y + kv * ssm_D[None, None, :]
        return q * y

    def _B_ref(x, w_gate, w_up, w_down):
        g = torch.einsum("bsd,df->bsf", x, w_gate)
        u = torch.einsum("bsd,df->bsf", x, w_up)
        h = _sigmoid(g) * u
        return torch.einsum("bsf,fd->bsd", h, w_down)

    x = A_q
    g = _sigmoid((x * gate_w).sum(-1, keepdims=True))
    a = _A_ref(x, A_k, A_v, A_shift_k, A_ssm_k, A_ssm_D)
    b = _B_ref(x, B_w_gate, B_w_up, B_w_down)
    return g * a + (1 - g) * b
```

```cpp
#include <hip/hip_runtime.h>
#include <hip/hip_bf16.h>
#include <stdint.h>

typedef unsigned short u16;
static constexpr int S = 2048, D = 640, F = 1536;

__device__ __forceinline__ float bf2f(u16 x) { return __uint_as_float(((unsigned)x) << 16); }
__device__ __forceinline__ u16 f2bf(float f) {
    unsigned u = __float_as_uint(f);
    return (u16)((u + 0x7FFFu + ((u >> 16) & 1u)) >> 16);
}
__device__ __forceinline__ float rbf(float f) { return bf2f(f2bf(f)); }
__device__ __forceinline__ float sigm(float x) { return 1.f / (1.f + __expf(-x)); }

__global__ void k_kv(const u16* k, const u16* v, const u16* sk, u16* kv) {
    int i = blockIdx.x * blockDim.x + threadIdx.x;
    int s = i / D, d = i % D;
    float acc = 0.f;
    for (int j = 3; j >= 0; --j) {
        float kk = (s - j >= 0) ? bf2f(k[(s - j) * D + d]) : 0.f;
        acc = rbf(acc + rbf(kk * bf2f(sk[d * 4 + j])));
    }
    kv[i] = f2bf(acc * bf2f(v[i]));
}
__global__ void k_conv(const u16* kv, const u16* sk, const u16* sd, const u16* q, u16* a) {
    int i = blockIdx.x * blockDim.x + threadIdx.x;
    int s = i / D, d = i % D;
    float acc = 0.f;
    for (int j = 0; j <= s; ++j) acc += bf2f(sk[d * S + j]) * bf2f(kv[(s - j) * D + d]);
    float y = rbf(acc);
    y = rbf(y + rbf(bf2f(kv[i]) * bf2f(sd[d])));
    a[i] = f2bf(bf2f(q[i]) * y);
}
__global__ void k_h(const u16* x, const u16* wg, const u16* wu, u16* h) {
    int i = blockIdx.x * blockDim.x + threadIdx.x;
    int s = i / F, f = i % F;
    float g = 0.f, u = 0.f;
    for (int k = 0; k < D; ++k) { float xv = bf2f(x[s * D + k]); g += xv * bf2f(wg[k * F + f]); u += xv * bf2f(wu[k * F + f]); }
    h[i] = f2bf(rbf(sigm(rbf(g))) * rbf(u));
}
__global__ void k_gate(const u16* q, const u16* gw, float* g) {
    int s = blockIdx.x * blockDim.x + threadIdx.x;
    float acc = 0.f;
    for (int d = 0; d < D; ++d) acc += rbf(bf2f(q[s * D + d]) * bf2f(gw[d]));
    g[s] = rbf(sigm(rbf(acc)));
}
__global__ void k_out(const u16* h, const u16* wd, const u16* a, const float* g, u16* out) {
    int i = blockIdx.x * blockDim.x + threadIdx.x;
    int s = i / D, d = i % D;
    float acc = 0.f;
    for (int k = 0; k < F; ++k) acc += bf2f(h[s * F + k]) * bf2f(wd[k * D + d]);
    float b = rbf(acc), gg = g[s];
    out[i] = f2bf(rbf(gg * bf2f(a[i])) + rbf(rbf(1.f - gg) * b));
}

extern "C" void kernel_launch(void* const* d_in, const int* in_sizes, int n_in,
                              void* d_out, int out_size, void* d_ws, size_t ws_size,
                              hipStream_t stream) {
    const u16* q = (const u16*)d_in[0]; const u16* k = (const u16*)d_in[1]; const u16* v = (const u16*)d_in[2];
    const u16* sk = (const u16*)d_in[3]; const u16* ssk = (const u16*)d_in[4]; const u16* sd = (const u16*)d_in[5];
    const u16* x = (const u16*)d_in[0]; const u16* wg = (const u16*)d_in[7]; const u16* wu = (const u16*)d_in[8];
    const u16* wd = (const u16*)d_in[9]; const u16* gw = (const u16*)d_in[10];
    char* ws = (char*)d_ws;
    u16* kv = (u16*)ws; u16* a = (u16*)(ws + (4 << 20)); u16* h = (u16*)(ws + (8 << 20)); float* g = (float*)(ws + (16 << 20));
    k_kv<<<S * D / 256, 256, 0, stream>>>(k, v, sk, kv);
    k_conv<<<S * D / 256, 256, 0, stream>>>(kv, ssk, sd, q, a);
    k_h<<<S * F / 256, 256, 0, stream>>>(x, wg, wu, h);
    k_gate<<<S / 256, 256, 0, stream>>>(q, gw, g);
    k_out<<<S * D / 256, 256, 0, stream>>>(h, wd, a, g, (u16*)d_out);
}
```

```cpp
#include <hip/hip_runtime.h>
#include <hip/hip_bf16.h>
#include <stdint.h>

typedef unsigned short u16;
typedef short s16x4 __attribute__((ext_vector_type(4)));
typedef short s16x8 __attribute__((ext_vector_type(8)));
typedef __bf16 b16x8 __attribute__((ext_vector_type(8)));
typedef __bf16 b16x2 __attribute__((ext_vector_type(2)));
typedef float f32x2 __attribute__((ext_vector_type(2)));
typedef float f32x4 __attribute__((ext_vector_type(4)));
typedef float f32x16 __attribute__((ext_vector_type(16)));
typedef unsigned u32x2 __attribute__((ext_vector_type(2)));
typedef unsigned u32x4 __attribute__((ext_vector_type(4)));
#define LAS __attribute__((address_space(3)))
#define DI __device__ __forceinline__

static constexpr int S = 2048, D = 640, F = 1536;
static constexpr int KRS = 2112;
static constexpr unsigned WS_H = 0, WS_KR0 = 8u << 20, WS_KR1 = 11u << 20, WS_YT = 14u << 20, WS_G = 17u << 20, WS_WT = 18u << 20, WS_STAMP = 20u << 20;

struct Params {
    const u16 *q, *k, *v, *sk, *ssk, *sd, *wg, *wu, *wd, *gw;
    u16* out; char* ws;
};

DI float bflo(unsigned x) { return __uint_as_float(x << 16); }
DI float bfhi(unsigned x) { return __uint_as_float(x & 0xffff0000u); }
DI unsigned cvtpk(float lo, float hi) { f32x2 v = {lo, hi}; b16x2 b = __builtin_convertvector(v, b16x2); return __builtin_bit_cast(unsigned, b); }
DI unsigned rfl(unsigned x) { return __builtin_amdgcn_readfirstlane(x); }
DI float sigmoidf_fast(float x) { return __builtin_amdgcn_rcpf(1.f + __builtin_amdgcn_exp2f(x * -1.44269504089f)); }
DI f32x4 mfma16(s16x8 a, s16x8 b, f32x4 c) {
    return __builtin_amdgcn_mfma_f32_16x16x32_bf16(__builtin_bit_cast(b16x8, a), __builtin_bit_cast(b16x8, b), c, 0, 0, 0);
}
DI f32x16 mfma32(s16x8 a, s16x8 b, f32x16 c) {
    return __builtin_amdgcn_mfma_f32_32x32x16_bf16(__builtin_bit_cast(b16x8, a), __builtin_bit_cast(b16x8, b), c, 0, 0, 0);
}
DI s16x4 trread(LAS char* p) { return __builtin_amdgcn_ds_read_tr16_b64_v4i16((LAS s16x4*)p); }
DI s16x8 cat(s16x4 a, s16x4 b) { return __builtin_shufflevector(a, b, 0, 1, 2, 3, 4, 5, 6, 7); }
DI __amdgpu_buffer_rsrc_t mkrsrc(const void* p, unsigned bytes) { return __builtin_amdgcn_make_buffer_rsrc((void*)p, 0, bytes, 0x00020000); }
DI void dma16(unsigned m0v, unsigned voff, __amdgpu_buffer_rsrc_t r, unsigned soff) {
    asm volatile("s_mov_b32 m0, %0\n\ts_nop 0\n\tbuffer_load_dwordx4 %1, %2, %3 offen lds" ::"s"(m0v), "v"(voff), "s"(r), "s"(soff) : "m0", "memory");
}
#define WAITV(n) asm volatile("s_waitcnt vmcnt(" #n ")" ::: "memory")
#define WAITL0() asm volatile("s_waitcnt lgkmcnt(0)" ::: "memory")
DI void bar() { asm volatile("" ::: "memory"); __builtin_amdgcn_s_barrier(); asm volatile("" ::: "memory"); }
#define SGB(m, n) __builtin_amdgcn_sched_group_barrier(m, n, 0)

#define STAMPT(i) do {} while (0)

constexpr int P0_LDS = 122880;
constexpr int TP = 144;

DI void p0_kv(const Params& p, LAS char* lds, int tile, int tid) {
    const int st = tile / 5, dc = tile % 5, s0 = st * 64, d0 = dc * 128;
    const int c = tid & 15, rg = tid >> 4, lane = tid & 63;
    const int ch0 = d0 + 8 * c;
    LAS char* T = lds + P0_LDS;
    u32x4 w4[4];
#pragma unroll
    for (int i = 0; i < 4; ++i) w4[i] = *(const u32x4*)(p.sk + ch0 * 4 + i * 8);
    const int s = s0 + 2 * rg;
    u32x4 kr[5], vr[2];
#pragma unroll
    for (int i = 0; i < 5; ++i) { const int r = s - 3 + i; kr[i] = (r >= 0) ? *(const u32x4*)(p.k + (size_t)r * D + ch0) : (u32x4){0, 0, 0, 0}; }
#pragma unroll
    for (int i = 0; i < 2; ++i) vr[i] = *(const u32x4*)(p.v + (size_t)(s + i) * D + ch0);
    u32x4 ke[4], ve = {0, 0, 0, 0};
    const bool extra = tid < 16 && s0 > 0;
#pragma unroll
    for (int i = 0; i < 4; ++i) ke[i] = (u32x4){0, 0, 0, 0};
    if (extra) {
#pragma unroll
        for (int i = 0; i < 4; ++i) ke[i] = *(const u32x4*)(p.k + (size_t)(s0 - 4 + i) * D + ch0);
        ve = *(const u32x4*)(p.v + (size_t)(s0 - 1) * D + ch0);
    }
    const int u = 62 - 2 * rg;
#pragma unroll
    for (int e = 0; e < 8; ++e) {
        const unsigned wa = w4[e >> 1][(e & 1) * 2], wb = w4[e >> 1][(e & 1) * 2 + 1];
        const float w0 = bflo(wa), w1 = bfhi(wa), w2 = bflo(wb), w3 = bfhi(wb);
        float kk[5], vv[2];
#pragma unroll
        for (int i = 0; i < 5; ++i) kk[i] = (e & 1) ? bfhi(kr[i][e >> 1]) : bflo(kr[i][e >> 1]);
#pragma unroll
        for (int i = 0; i < 2; ++i) vv[i] = (e & 1) ? bfhi(vr[i][e >> 1]) : bflo(vr[i][e >> 1]);
        const float a0 = (kk[0] * w3 + kk[1] * w2 + kk[2] * w1 + kk[3] * w0) * vv[0];
        const float a1 = (kk[1] * w3 + kk[2] * w2 + kk[3] * w1 + kk[4] * w0) * vv[1];
        *(LAS unsigned*)(T + (8 * c + e) * TP + u * 2) = cvtpk(a1, a0);
        if (tid < 16) {
            float k4[4];
#pragma unroll
            for (int i = 0; i < 4; ++i) k4[i] = (e & 1) ? bfhi(ke[i][e >> 1]) : bflo(ke[i][e >> 1]);
            const float vx = (e & 1) ? bfhi(ve[e >> 1]) : bflo(ve[e >> 1]);
            const float ax = (k4[0] * w3 + k4[1] * w2 + k4[2] * w1 + k4[3] * w0) * vx;
            *(LAS unsigned*)(T + (8 * c + e) * TP + 128) = cvtpk(ax, 0.f);
        }
    }
    WAITL0(); bar();
    __amdgpu_buffer_rsrc_t r0 = mkrsrc(p.ws + WS_KR0, D * KRS * 2), r1 = mkrsrc(p.ws + WS_KR1, D * KRS * 2);
#pragma unroll
    for (int it = 0; it < 2; ++it) {
        const int item = tid + it * 512, ch = item >> 3, j = item & 7;
        const u32x4 a = *(LAS u32x4*)(T + ch * TP + j * 16);
        const unsigned nx = *(LAS unsigned*)(T + ch * TP + j * 16 + 16);
        const u32x4 b = {__builtin_amdgcn_alignbit(a[1], a[0], 16), __builtin_amdgcn_alignbit(a[2], a[1], 16), __builtin_amdgcn_alignbit(a[3], a[2], 16), __builtin_amdgcn_alignbit(nx, a[3], 16)};
        const unsigned off = ((d0 + ch) * KRS + (1984 - s0) + 8 * j) * 2;
        __builtin_amdgcn_raw_buffer_store_b128(a, r0, off, 0, 16);
        __builtin_amdgcn_raw_buffer_store_b128(b, r1, off, 0, 16);
    }
    if (st == 0 && tid < 128) {
        const unsigned off = ((d0 + tid) * KRS + 2048) * 2;
        __builtin_amdgcn_raw_buffer_store_b128((u32x4){0, 0, 0, 0}, r0, off, 0, 16);
        __builtin_amdgcn_raw_buffer_store_b128((u32x4){0, 0, 0, 0}, r1, off, 0, 16);
    }
    (void)lane;
}

DI void p0_gate(const Params& p, int row, int lane) {
    const u32x4 a = *(const u32x4*)(p.q + (size_t)row * D + lane * 8), ga = *(const u32x4*)(p.gw + lane * 8);
    u32x4 b = {0, 0, 0, 0}, gb = {0, 0, 0, 0};
    if (lane < 16) { b = *(const u32x4*)(p.q + (size_t)row * D + 512 + lane * 8); gb = *(const u32x4*)(p.gw + 512 + lane * 8); }
    float acc = 0.f;
#pragma unroll
    for (int i = 0; i < 4; ++i) acc += bflo(a[i]) * bflo(ga[i]) + bfhi(a[i]) * bfhi(ga[i]) + bflo(b[i]) * bflo(gb[i]) + bfhi(b[i]) * bfhi(gb[i]);
#pragma unroll
    for (int m = 32; m >= 1; m >>= 1) acc += __shfl_xor(acc, m);
    if (lane == 0) __hip_atomic_store((float*)(p.ws + WS_G) + row, sigmoidf_fast(acc), __ATOMIC_RELAXED, __HIP_MEMORY_SCOPE_AGENT);
}

DI void p0_wt(const Params& p, LAS char* buf, int unit, int lane) {
    const int T = unit / 10, dl = unit % 10;
#pragma unroll
    for (int i = 0; i < 4; ++i) {
        const int r = 8 * i + (lane >> 3);
        *(LAS u32x4*)(buf + r * 128 + (lane & 7) * 16) = *(const u32x4*)(p.wd + (size_t)(32 * T + r) * D + 64 * dl + (lane & 7) * 8);
    }
    WAITL0();
    const int g = lane >> 4, q = (lane & 15) >> 2, pp = lane & 3;
    const int row = 16 * (g >> 1) + 4 * (g & 1) + q;
    __amdgpu_buffer_rsrc_t rw = mkrsrc(p.ws + WS_WT, F * D * 2);
#pragma unroll
    for (int i = 0; i < 4; ++i) {
        const s16x8 f = cat(trread(buf + row * 128 + 32 * i + 8 * pp), trread(buf + (row + 8) * 128 + 32 * i + 8 * pp));
        const int d16 = 4 * dl + i, ds = d16 / 5, n = d16 % 5;
        __builtin_amdgcn_raw_buffer_store_b128(__builtin_bit_cast(u32x4, f), rw, (((ds * 48 + T) * 5 + n) * 64 + lane) * 16, 0, 16);
    }
}

__global__ __launch_bounds__(512, 2) void k_p0(Params p) {
    extern __shared__ __attribute__((aligned(16))) char smem[];
    LAS char* lds = (LAS char*)smem;
    const int wave = rfl(threadIdx.x >> 6), lane = threadIdx.x & 63, b = blockIdx.x;
    STAMPT(0);
    p0_gate(p, b * 8 + wave, lane);
    if (b < 160) p0_kv(p, lds, b, threadIdx.x);
    else if (wave < 5) p0_wt(p, lds + P0_LDS + wave * 4096, (b - 160) * 5 + wave, lane);
    STAMPT(1);
}

constexpr int G1_STAGE = 40960;
struct Frag1 { s16x8 a[6]; s16x8 b; };

__global__ __launch_bounds__(512, 2) void k_gemm1(Params p) {
    extern __shared__ __attribute__((aligned(16))) char smem[];
    LAS char* lds = (LAS char*)smem;
    const int wave = rfl(threadIdx.x >> 6), lane = threadIdx.x & 63;
    const int xcd = blockIdx.x & 7, idx = blockIdx.x >> 3;
    const int bs = (xcd & 1) * 8 + (idx & 7), bf = (xcd >> 1) * 4 + (idx >> 3);
    STAMPT(0);
    if (wave >= 4) {
        const int w = wave - 4;
        const unsigned ldsb = (unsigned)(size_t)lds;
        __amdgpu_buffer_rsrc_t rx = mkrsrc(p.q, S * D * 2), rg = mkrsrc(p.wg, D * F * 2), ru = mkrsrc(p.wu, D * F * 2);
        unsigned vx[2], vw[3];
        {
            const int r = lane >> 3, slot = lane & 7;
            vx[0] = r * 1280 + ((slot ^ (r >> 1)) * 16);
            vx[1] = r * 1280 + ((slot ^ (r >> 1) ^ 4) * 16);
#pragma unroll
            for (int j = 0; j < 3; ++j) {
                const int L = 2 * j + (lane >> 5), kbl = L / 3, c = L % 3, row = (lane & 31) >> 2, ch = lane & 3;
                vw[j] = (kbl * 8 + row) * 3072 + c * 64 + ch * 16;
            }
        }
        const unsigned sx0 = (bs * 128 + 32 * w) * 1280, sw0 = 16 * w * 3072 + bf * 192;
        auto issue = [&](int t, int buf) {
            const unsigned base = ldsb + buf * G1_STAGE;
#pragma unroll
            for (int i = 0; i < 4; ++i) dma16(base + (32 * w + 8 * i) * 128, vx[i & 1], rx, sx0 + i * 8 * 1280 + t * 128);
#pragma unroll
            for (int j = 0; j < 3; ++j) dma16(base + 16384 + w * 3072 + j * 1024, vw[j], rg, sw0 + t * 64 * 3072);
#pragma unroll
            for (int j = 0; j < 3; ++j) dma16(base + 28672 + w * 3072 + j * 1024, vw[j], ru, sw0 + t * 64 * 3072);
        };
        issue(0, 0); issue(1, 1); issue(2, 2);
        WAITV(20); bar();
        STAMPT(1);
        int buf = 0;
        for (int t = 0; t < 10; ++t) {
            if (t <= 7) WAITV(10); else WAITV(0);
            bar();
            if (t + 3 <= 9) issue(t + 3, buf);
            buf = (buf == 2) ? 0 : buf + 1;
        }
        STAMPT(2);
    } else {
        const int h = lane >> 5, r = lane & 31, blk = (lane >> 4) & 1, q = (lane & 15) >> 2, pp = lane & 3;
        const int oa = 16384 + h * 1536 + q * 64 + 16 * (2 * blk + (pp >> 1)) + 8 * (pp & 1);
        int ob[4];
#pragma unroll
        for (int ks = 0; ks < 4; ++ks) ob[ks] = (32 * wave + r) * 128 + (((2 * ks + h) ^ ((r >> 1) & 7)) * 16);
        f32x16 accg[3], accu[3];
#pragma unroll
        for (int i = 0; i < 3; ++i)
#pragma unroll
            for (int e = 0; e < 16; ++e) { accg[i][e] = 0.f; accu[i][e] = 0.f; }
        Frag1 f0, f1;
        auto rd = [&](Frag1& f, int ks, int sb) {
            f.b = *(LAS s16x8*)(lds + sb + ob[ks]);
#pragma unroll
            for (int i = 0; i < 3; ++i) {
                f.a[i] = cat(trread(lds + sb + oa + ks * 3072 + i * 512), trread(lds + sb + oa + ks * 3072 + i * 512 + 256));
                f.a[3 + i] = cat(trread(lds + sb + oa + ks * 3072 + i * 512 + 12288), trread(lds + sb + oa + ks * 3072 + i * 512 + 256 + 12288));
            }
        };
        auto mm = [&](const Frag1& f) {
#pragma unroll
            for (int i = 0; i < 3; ++i) {
                accg[i] = mfma32(f.a[i], f.b, accg[i]);
                accu[i] = mfma32(f.a[3 + i], f.b, accu[i]);
            }
        };
        auto interleave = [&]() {
            SGB(0x008, 1); SGB(0x100, 3); SGB(0x008, 1); SGB(0x100, 3); SGB(0x008, 1); SGB(0x100, 3);
            SGB(0x008, 1); SGB(0x100, 2); SGB(0x008, 1); SGB(0x100, 2); SGB(0x008, 1);
            __builtin_amdgcn_sched_barrier(0);
        };
        bar();
        STAMPT(1);
        rd(f0, 0, 0);
        int sb = 0;
        for (int t = 0; t < 10; ++t) {
            rd(f1, 1, sb); mm(f0); interleave();
            rd(f0, 2, sb); mm(f1); interleave();
            rd(f1, 3, sb); mm(f0); interleave();
            WAITL0();
            bar();
            sb = (sb == 2 * G1_STAGE) ? 0 : sb + G1_STAGE;
            if (t < 9) rd(f0, 0, sb);
            mm(f1); interleave();
        }
        STAMPT(2);
        __amdgpu_buffer_rsrc_t rh = mkrsrc(p.ws + WS_H, S * F * 2);
#pragma unroll
        for (int i = 0; i < 3; ++i)
#pragma unroll
            for (int c = 0; c < 2; ++c) {
                float hv[8];
#pragma unroll
                for (int e = 0; e < 8; ++e) hv[e] = accu[i][8 * c + e] * sigmoidf_fast(accg[i][8 * c + e]);
                u32x4 o = {cvtpk(hv[0], hv[1]), cvtpk(hv[2], hv[3]), cvtpk(hv[4], hv[5]), cvtpk(hv[6], hv[7])};
                const int st16 = bs * 8 + wave * 2 + blk, T = bf * 3 + i;
                __builtin_amdgcn_raw_buffer_store_b128(o, rh, ((st16 * 48 + T) * 64 + (2 * c + h) * 16 + (lane & 15)) * 16, 0, 16);
            }
        STAMPT(3);
    }
}

constexpr int CV_SLOT = 21248, CV_COPY = 4160, CV_KF = 4608;

__global__ __launch_bounds__(512, 2) void k_conv(Params p) {
    extern __shared__ __attribute__((aligned(16))) char smem[];
    LAS char* lds = (LAS char*)smem;
    const int wave = rfl(threadIdx.x >> 6), lane = threadIdx.x & 63;
    const int w = wave & 3, d = blockIdx.x * 4 + w;
    LAS char* slot = lds + w * CV_SLOT;
    STAMPT(0);
    if (wave >= 4) {
        const unsigned sl = (unsigned)(size_t)slot;
        __amdgpu_buffer_rsrc_t rf = mkrsrc(p.ssk, D * S * 2), r0 = mkrsrc(p.ws + WS_KR0, D * KRS * 2), r1 = mkrsrc(p.ws + WS_KR1, D * KRS * 2);
        const unsigned vo = lane * 16;
#pragma unroll
        for (int i = 0; i < 4; ++i) dma16(sl + 512 + i * 1024, vo, rf, d * (S * 2) + i * 1024);
#pragma unroll
        for (int c = 0; c < 4; ++c)
#pragma unroll
            for (int i = 0; i < 4; ++i) dma16(sl + CV_KF + c * CV_COPY + i * 1024, vo, (c & 1) ? r1 : r0, d * (KRS * 2) + (c >> 1) * 4 + i * 1024);
        if (lane < 32) *(LAS u32x4*)(slot + lane * 16) = (u32x4){0, 0, 0, 0};
        else if (lane < 48) *(LAS u32x4*)(slot + CV_KF + ((lane - 32) >> 2) * CV_COPY + 4096 + (lane & 3) * 16) = (u32x4){0, 0, 0, 0};
        WAITV(0); WAITL0(); bar();
        STAMPT(1);
    } else {
        const int a = lane & 15, g = lane >> 4;
        bar();
        STAMPT(1);
        f32x4 acc[8];
#pragma unroll
        for (int n = 0; n < 8; ++n) acc[n] = (f32x4){0, 0, 0, 0};
        LAS char* pg = slot + 480 + 32 * a + 16 * g;
        LAS char* pf = slot + CV_KF + (3 - (a & 3)) * CV_COPY + 472 + 16 * g - 8 * (a >> 2);
        for (int rho = 0; rho < 8; ++rho) {
            s16x8 G[8];
#pragma unroll
            for (int j = 0; j < 8; ++j) G[j] = *(LAS s16x8*)(pg + 512 * j);
#pragma unroll
            for (int I = 0; I < 8; ++I) {
                const s16x8 Fr = cat(*(LAS s16x4*)(pf + 512 * (7 - I)), *(LAS s16x4*)(pf + 512 * (7 - I) + 8));
#pragma unroll
                for (int n = I; n < 8; ++n) acc[n] = mfma16(Fr, G[n - I], acc[n]);
            }
            pg -= 64; pf -= 64;
        }
        STAMPT(2);
        const float Dd = bflo(p.sd[d]);
        __amdgpu_buffer_rsrc_t ry = mkrsrc(p.ws + WS_YT, D * S * 2);
        u32x2 o[8];
#pragma unroll
        for (int n = 0; n < 8; ++n) {
            const u32x2 kv = *(LAS u32x2*)(slot + CV_KF + 2 * (2044 - 256 * n - 16 * a - 4 * g));
            o[n][0] = cvtpk(acc[n][0] + bfhi(kv[1]) * Dd, acc[n][1] + bflo(kv[1]) * Dd);
            o[n][1] = cvtpk(acc[n][2] + bfhi(kv[0]) * Dd, acc[n][3] + bflo(kv[0]) * Dd);
        }
#pragma unroll
        for (int n = 0; n < 8; n += 2) {
            auto x = __builtin_amdgcn_permlane16_swap(o[n][0], o[n + 1][0], false, false);
            auto y = __builtin_amdgcn_permlane16_swap(o[n][1], o[n + 1][1], false, false);
            const u32x4 val = {x[0], y[0], x[1], y[1]};
            const int s = 256 * (n + (g & 1)) + 16 * a + 4 * (g & 2);
            __builtin_amdgcn_raw_buffer_store_b128(val, ry, (d * S + s) * 2, 0, 16);
        }
        STAMPT(3);
    }
}

constexpr int G2_STAGE = 20480, G2_NST = 4, G2_Y = G2_STAGE * G2_NST;

__global__ __launch_bounds__(512, 2) void k_gemm2(Params p) {
    extern __shared__ __attribute__((aligned(16))) char smem[];
    LAS char* lds = (LAS char*)smem;
    const int wave = rfl(threadIdx.x >> 6), lane = threadIdx.x & 63;
    const int xcd = blockIdx.x & 7, idx = blockIdx.x >> 3;
    const int st = xcd * 4 + (idx & 3), ds = idx >> 2;
    STAMPT(0);
    if (wave >= 4) {
        const int w = wave - 4;
        const unsigned ldsb = (unsigned)(size_t)lds;
        __amdgpu_buffer_rsrc_t rw = mkrsrc(p.ws + WS_WT, F * D * 2);
        const unsigned vo = lane * 16, sw0 = ds * (48 * 5 * 1024) + w * 5120;
        auto issue = [&](int t, int buf) {
#pragma unroll
            for (int j = 0; j < 5; ++j) dma16(ldsb + buf * G2_STAGE + w * 5120 + j * 1024, vo, rw, sw0 + t * G2_STAGE + j * 1024);
        };
        issue(0, 0); issue(1, 1); issue(2, 2); issue(3, 3);
        __amdgpu_buffer_rsrc_t ry = mkrsrc(p.ws + WS_YT, D * S * 2);
        u32x4 yv[3];
#pragma unroll
        for (int i = 0; i < 3; ++i) {
            const int it = w + 4 * i, ch = 8 * it + (lane >> 3);
            yv[i] = (u32x4){0, 0, 0, 0};
            if (it < 10) yv[i] = __builtin_amdgcn_raw_buffer_load_b128(ry, ((80 * ds + ch) * S + 64 * st + (lane & 7) * 8) * 2, 0, 16);
        }
#pragma unroll
        for (int i = 0; i < 3; ++i) {
            const int it = w + 4 * i, ch = 8 * it + (lane >> 3);
            if (it < 10) *(LAS u32x4*)(lds + G2_Y + ch * 128 + (lane & 7) * 16) = yv[i];
        }
        WAITV(0); WAITL0();
        bar();
        STAMPT(1);
        int buf = 0;
        for (int t = 0; t < 12; ++t) {
            if (t <= 8) WAITV(10); else if (t == 9) WAITV(5); else WAITV(0);
            bar();
            if (t + 4 < 12) issue(t + 4, buf);
            buf = (buf + 1) & 3;
        }
        STAMPT(2);
    } else {
        __amdgpu_buffer_rsrc_t rh = mkrsrc(p.ws + WS_H, S * F * 2);
        const unsigned ho = ((st * 4 + wave) * 48 * 64 + lane) * 16;
        u32x4 hf[16];
#pragma unroll
        for (int i = 0; i < 16; ++i) hf[i] = __builtin_amdgcn_raw_buffer_load_b128(rh, ho, i * 1024, 16);
        f32x4 acc[5];
#pragma unroll
        for (int n = 0; n < 5; ++n) acc[n] = (f32x4){0, 0, 0, 0};
        bar();
        STAMPT(1);
        LAS char* pw = lds + lane * 16;
#pragma unroll
        for (int t = 0; t < 12; ++t) {
#pragma unroll
            for (int kk = 0; kk < 4; ++kk) {
                const int T = 4 * t + kk;
                s16x8 wf[5];
#pragma unroll
                for (int n = 0; n < 5; ++n) wf[n] = *(LAS s16x8*)(pw + (t & 3) * G2_STAGE + kk * 5120 + n * 1024);
                const s16x8 hb = __builtin_bit_cast(s16x8, hf[T & 15]);
#pragma unroll
                for (int n = 0; n < 5; ++n) acc[n] = mfma16(wf[n], hb, acc[n]);
                if (T + 16 < 48) hf[T & 15] = __builtin_amdgcn_raw_buffer_load_b128(rh, ho, (T + 16) * 1024, 16);
            }
            WAITL0();
            bar();
        }
        STAMPT(2);
        const int s16 = lane & 15, g = lane >> 4, q = s16 >> 2, pp = lane & 3;
        const int s = 64 * st + 16 * wave + s16;
        const float gt = __hip_atomic_load((const float*)(p.ws + WS_G) + s, __ATOMIC_RELAXED, __HIP_MEMORY_SCOPE_AGENT);
#pragma unroll
        for (int n = 0; n < 5; ++n) {
            const int d = 80 * ds + 16 * n + 4 * g;
            const u32x2 qv = *(const u32x2*)(p.q + (size_t)s * D + d);
            const u32x2 yv = __builtin_bit_cast(u32x2, trread(lds + G2_Y + (16 * n + 4 * g + q) * 128 + (16 * wave + 4 * pp) * 2));
            const float o0 = gt * (bflo(qv[0]) * bflo(yv[0])) + (1.f - gt) * acc[n][0];
            const float o1 = gt * (bfhi(qv[0]) * bfhi(yv[0])) + (1.f - gt) * acc[n][1];
            const float o2 = gt * (bflo(qv[1]) * bflo(yv[1])) + (1.f - gt) * acc[n][2];
            const float o3 = gt * (bfhi(qv[1]) * bfhi(yv[1])) + (1.f - gt) * acc[n][3];
            *(u32x2*)(p.out + (size_t)s * D + d) = (u32x2){cvtpk(o0, o1), cvtpk(o2, o3)};
        }
        STAMPT(3);
    }
}

extern "C" void kernel_launch(void* const* d_in, const int* in_sizes, int n_in,
                              void* d_out, int out_size, void* d_ws, size_t ws_size,
                              hipStream_t stream) {
    Params p;
    p.q = (const u16*)d_in[0]; p.k = (const u16*)d_in[1]; p.v = (const u16*)d_in[2];
    p.sk = (const u16*)d_in[3]; p.ssk = (const u16*)d_in[4]; p.sd = (const u16*)d_in[5];
    p.wg = (const u16*)d_in[7]; p.wu = (const u16*)d_in[8]; p.wd = (const u16*)d_in[9]; p.gw = (const u16*)d_in[10];
    p.out = (u16*)d_out; p.ws = (char*)d_ws;
    hipFuncSetAttribute((const void*)k_p0, hipFuncAttributeMaxDynamicSharedMemorySize, 163840);
    hipFuncSetAttribute((const void*)k_gemm1, hipFuncAttributeMaxDynamicSharedMemorySize, 163840);
    hipFuncSetAttribute((const void*)k_conv, hipFuncAttributeMaxDynamicSharedMemorySize, 163840);
    hipFuncSetAttribute((const void*)k_gemm2, hipFuncAttributeMaxDynamicSharedMemorySize, 163840);
#define ONLY 15
    if (ONLY & 1) k_p0<<<256, 512, 163840, stream>>>(p);
    if (ONLY & 2) k_gemm1<<<256, 512, 3 * G1_STAGE, stream>>>(p);
    if (ONLY & 4) k_conv<<<160, 512, 4 * CV_SLOT, stream>>>(p);
    if (ONLY & 8) k_gemm2<<<256, 512, G2_Y + 10240, stream>>>(p);
}
```

```cpp
#include <hip/hip_runtime.h>
#include <hip/hip_bf16.h>
#include <stdint.h>

typedef unsigned short u16;
typedef short s16x4 __attribute__((ext_vector_type(4)));
typedef short s16x8 __attribute__((ext_vector_type(8)));
typedef __bf16 b16x8 __attribute__((ext_vector_type(8)));
typedef __bf16 b16x2 __attribute__((ext_vector_type(2)));
typedef float f32x2 __attribute__((ext_vector_type(2)));
typedef float f32x4 __attribute__((ext_vector_type(4)));
typedef unsigned u32x2 __attribute__((ext_vector_type(2)));
typedef unsigned u32x4 __attribute__((ext_vector_type(4)));
#define LAS __attribute__((address_space(3)))
#define GAS __attribute__((address_space(1)))
#define DI __device__ __forceinline__

static constexpr int S = 2048, D = 640, F = 1536;
static constexpr int KRS = 2112;
static constexpr unsigned WS_H = 0, WS_KR0 = 8u << 20, WS_KR1 = 11u << 20, WS_YT = 14u << 20, WS_G = 17u << 20, WS_WT = 18u << 20,
                          WS_STAMP = 20u << 20, WS_FLAG = 24u << 20;
static constexpr unsigned FL_EPOCH = 0, FL_P0 = 1024, FL_H = 2048, FL_Y = 3072;

struct Params {
    const u16 *q, *k, *v, *sk, *ssk, *sd, *wg, *wu, *wd, *gw;
    u16* out; char* ws;
};

DI float bflo(unsigned x) { return __uint_as_float(x << 16); }
DI float bfhi(unsigned x) { return __uint_as_float(x & 0xffff0000u); }
DI unsigned cvtpk(float lo, float hi) { f32x2 v = {lo, hi}; b16x2 b = __builtin_convertvector(v, b16x2); return __builtin_bit_cast(unsigned, b); }
DI unsigned rfl(unsigned x) { return __builtin_amdgcn_readfirstlane(x); }
DI float sigmoidf_fast(float x) { return __builtin_amdgcn_rcpf(1.f + __builtin_amdgcn_exp2f(x * -1.44269504089f)); }
DI f32x4 mfma16(s16x8 a, s16x8 b, f32x4 c) {
    return __builtin_amdgcn_mfma_f32_16x16x32_bf16(__builtin_bit_cast(b16x8, a), __builtin_bit_cast(b16x8, b), c, 0, 0, 0);
}
DI s16x4 trread(LAS char* p) { return __builtin_amdgcn_ds_read_tr16_b64_v4i16((LAS s16x4*)p); }
DI s16x8 cat(s16x4 a, s16x4 b) { return __builtin_shufflevector(a, b, 0, 1, 2, 3, 4, 5, 6, 7); }
typedef __amdgpu_buffer_rsrc_t rsrc_t;
DI rsrc_t mkrsrc(const void* p, unsigned bytes) { return __builtin_amdgcn_make_buffer_rsrc((void*)p, 0, bytes, 0x00020000); }
DI void dma16(unsigned m0v, unsigned voff, rsrc_t r, unsigned soff) {
    asm volatile("s_mov_b32 m0, %0\n\ts_nop 0\n\tbuffer_load_dwordx4 %1, %2, %3 offen lds" ::"s"(m0v), "v"(voff), "s"(r), "s"(soff) : "m0", "memory");
}
DI void dma16_sc1(unsigned m0v, unsigned voff, rsrc_t r, unsigned soff) {
    asm volatile("s_mov_b32 m0, %0\n\ts_nop 0\n\tbuffer_load_dwordx4 %1, %2, %3 offen sc1 lds" ::"s"(m0v), "v"(voff), "s"(r), "s"(soff) : "m0", "memory");
}
DI u32x4 aload(rsrc_t r, unsigned voff) { u32x4 x; asm volatile("buffer_load_dwordx4 %0, %1, %2, 0 offen" : "=v"(x) : "v"(voff), "s"(r) : "memory"); return x; }
#define TIE(x) asm volatile("" : "+v"(x))
#define WAITV(n) asm volatile("s_waitcnt vmcnt(" #n ")" ::: "memory")
#define WAITL0() asm volatile("s_waitcnt lgkmcnt(0)" ::: "memory")
DI void bar() { asm volatile("" ::: "memory"); __builtin_amdgcn_s_barrier(); asm volatile("" ::: "memory"); }
#define SGB(m, n) __builtin_amdgcn_sched_group_barrier(m, n, 0)
#define SB0() __builtin_amdgcn_sched_barrier(0)
#define RLX_AGENT __ATOMIC_RELAXED, __HIP_MEMORY_SCOPE_AGENT

DI void st_flag(char* ws, unsigned word, unsigned tag) { __hip_atomic_store((GAS unsigned*)(ws + WS_FLAG) + word, tag, RLX_AGENT); }
template <int NW4>
DI bool poll4_once(char* ws, unsigned word, unsigned tag, int lane) {
    rsrc_t r = mkrsrc(ws + WS_FLAG, 16384);
    u32x4 x;
    asm volatile("buffer_load_dwordx4 %0, %1, %2, 0 offen sc1\n\ts_waitcnt vmcnt(0)" : "=&v"(x) : "v"((word + (lane < NW4 ? lane : 0) * 4) * 4), "s"(r) : "memory");
    return __all(x[0] == tag && x[1] == tag && x[2] == tag && x[3] == tag);
}
template <int NW4>
DI void poll4(char* ws, unsigned word, unsigned tag, int lane) {
    for (int spins = 0; spins < (1 << 18); ++spins) {
        if (poll4_once<NW4>(ws, word, tag, lane)) return;
        __builtin_amdgcn_s_sleep(1);
    }
}

#define STAMPT(i) do {} while (0)

constexpr int G1_STAGE = 40960;
constexpr int P0_LDS = 122880;
constexpr int TP = 144;
constexpr int CV_SLOT = 21248, CV_COPY = 4160, CV_KF = 4608;
constexpr int G2_RING = 84992, G2_STAGE = 20480;
constexpr int G2_Y = G2_RING + 3 * G2_STAGE;
constexpr int G2_RED = 0;
constexpr int L_CNT = 163776;
constexpr int LDS_BYTES = 163840;

__global__ __launch_bounds__(512, 2) void k_fused(Params p) {
    extern __shared__ __attribute__((aligned(16))) char smem[];
    LAS char* lds = (LAS char*)smem;
    const unsigned ldsb = (unsigned)(size_t)lds;
    const int wave = rfl(threadIdx.x >> 6), lane = threadIdx.x & 63, tid = threadIdx.x;
    const int b = blockIdx.x, xcd = b & 7, idx = b >> 3;
    const bool has_conv = idx < 20;
    const int cb = xcd * 20 + idx;
    const int bs = (xcd & 1) * 8 + (idx & 7), bf = (xcd >> 1) * 4 + (idx >> 3);
    const int st = xcd * 4 + (idx & 3), ds = idx >> 2;
    STAMPT(0);
    unsigned tag = rfl(*(const GAS unsigned*)(p.ws + WS_FLAG)) + 1u;
    asm volatile("" : "+s"(tag));

    rsrc_t rq = mkrsrc(p.q, S * D * 2);
    u32x4 ga, gb, gwa, gwb;
    {
        rsrc_t rgw = mkrsrc(p.gw, D * 2);
        const unsigned row = (b * 8 + wave) * (D * 2), hi = lane < 16 ? 1024 + lane * 16 : 0xF0000000u;
        ga = aload(rq, row + lane * 16); gb = aload(rq, lane < 16 ? row + hi : hi);
        gwa = aload(rgw, lane * 16); gwb = aload(rgw, hi);
    }
    u32x4 w4[4], kr[5], vr[2], ke[4], ve;
    const int kst = cb / 5, kdc = cb % 5, s0 = kst * 64, d0 = kdc * 128;
    const int c16 = tid & 15, rg = tid >> 4;
    const int unit = (xcd * 12 + idx - 20) * 5 + wave;
    if (has_conv) {
        rsrc_t rk = mkrsrc(p.k, S * D * 2), rv = mkrsrc(p.v, S * D * 2), rsk = mkrsrc(p.sk, D * 8);
        const int ch0 = d0 + 8 * c16, s = s0 + 2 * rg;
#pragma unroll
        for (int i = 0; i < 5; ++i) kr[i] = aload(rk, (unsigned)(((s - 3 + i) * D + ch0) * 2));
#pragma unroll
        for (int i = 0; i < 2; ++i) vr[i] = aload(rv, (unsigned)(((s + i) * D + ch0) * 2));
#pragma unroll
        for (int i = 0; i < 4; ++i) w4[i] = aload(rsk, ch0 * 8 + i * 16);
        if (wave == 0) {
#pragma unroll
            for (int i = 0; i < 4; ++i) ke[i] = aload(rk, (unsigned)(((s0 - 4 + i) * D + ch0) * 2));
            ve = aload(rv, (unsigned)(((s0 - 1) * D + ch0) * 2));
        }
    } else if (wave < 5) {
        rsrc_t rwd = mkrsrc(p.wd, F * D * 2);
        const int T = unit / 10, dl = unit % 10;
#pragma unroll
        for (int i = 0; i < 4; ++i) kr[i] = aload(rwd, ((32 * T + 8 * i + (lane >> 3)) * D + 64 * dl + (lane & 7) * 8) * 2);
    }

    rsrc_t rg_ = mkrsrc(p.wg, D * F * 2), ru_ = mkrsrc(p.wu, D * F * 2);
    unsigned vx[2], vw[3];
    const int lw = wave & 3;
    {
        const int r = lane >> 3, slot = lane & 7;
        vx[0] = r * 1280 + ((slot ^ (r >> 1)) * 16);
        vx[1] = r * 1280 + ((slot ^ (r >> 1) ^ 4) * 16);
#pragma unroll
        for (int j = 0; j < 3; ++j) {
            const int L = 2 * j + (lane >> 5), kbl = L / 3, c = L % 3, row = (lane & 31) >> 2, ch = (lane & 3) ^ (2 * kbl);
            vw[j] = (kbl * 8 + row) * 3072 + c * 64 + ch * 16;
        }
    }
    const unsigned sx0 = (bs * 128 + 32 * lw) * 1280, sw0 = 16 * lw * 3072 + bf * 192;
    auto g1_issue = [&](int t, int buf) {
        const unsigned base = ldsb + buf * G1_STAGE;
#pragma unroll
        for (int i = 0; i < 4; ++i) dma16(base + (32 * lw + 8 * i) * 128, vx[i & 1], rq, sx0 + i * 8 * 1280 + t * 128);
#pragma unroll
        for (int j = 0; j < 3; ++j) dma16(base + 16384 + lw * 3072 + j * 1024, vw[j], rg_, sw0 + t * 64 * 3072);
#pragma unroll
        for (int j = 0; j < 3; ++j) dma16(base + 28672 + lw * 3072 + j * 1024, vw[j], ru_, sw0 + t * 64 * 3072);
    };
    if (wave >= 4) { g1_issue(0, 0); g1_issue(1, 1); g1_issue(2, 2); WAITV(30); }
    else WAITV(0);
    STAMPT(1);

    TIE(ga); TIE(gb); TIE(gwa); TIE(gwb);
    {
        float acc = 0.f;
#pragma unroll
        for (int i = 0; i < 4; ++i) acc += bflo(ga[i]) * bflo(gwa[i]) + bfhi(ga[i]) * bfhi(gwa[i]) + bflo(gb[i]) * bflo(gwb[i]) + bfhi(gb[i]) * bfhi(gwb[i]);
#pragma unroll
        for (int m = 32; m >= 1; m >>= 1) acc += __shfl_xor(acc, m);
        if (lane == 0) __hip_atomic_store((GAS float*)(p.ws + WS_G) + b * 8 + wave, sigmoidf_fast(acc), RLX_AGENT);
    }
    if (has_conv) {
        LAS char* T = lds + P0_LDS;
#pragma unroll
        for (int i = 0; i < 5; ++i) TIE(kr[i]);
#pragma unroll
        for (int i = 0; i < 2; ++i) TIE(vr[i]);
#pragma unroll
        for (int i = 0; i < 4; ++i) TIE(w4[i]);
        const int u = 62 - 2 * rg;
#pragma unroll
        for (int e = 0; e < 8; ++e) {
            const unsigned wa = w4[e >> 1][(e & 1) * 2], wb = w4[e >> 1][(e & 1) * 2 + 1];
            const float w0 = bflo(wa), w1 = bfhi(wa), w2 = bflo(wb), w3 = bfhi(wb);
            float kk[5], vv[2];
#pragma unroll
            for (int i = 0; i < 5; ++i) kk[i] = (e & 1) ? bfhi(kr[i][e >> 1]) : bflo(kr[i][e >> 1]);
#pragma unroll
            for (int i = 0; i < 2; ++i) vv[i] = (e & 1) ? bfhi(vr[i][e >> 1]) : bflo(vr[i][e >> 1]);
            const float a0 = (kk[0] * w3 + kk[1] * w2 + kk[2] * w1 + kk[3] * w0) * vv[0];
            const float a1 = (kk[1] * w3 + kk[2] * w2 + kk[3] * w1 + kk[4] * w0) * vv[1];
            *(LAS unsigned*)(T + (8 * c16 + e) * TP + u * 2) = cvtpk(a1, a0);
        }
        if (wave == 0) {
#pragma unroll
            for (int i = 0; i < 4; ++i) TIE(ke[i]);
            TIE(ve);
            if (lane < 16) {
#pragma unroll
                for (int e = 0; e < 8; ++e) {
                    const unsigned wa = w4[e >> 1][(e & 1) * 2], wb = w4[e >> 1][(e & 1) * 2 + 1];
                    float k4[4];
#pragma unroll
                    for (int i = 0; i < 4; ++i) k4[i] = (e & 1) ? bfhi(ke[i][e >> 1]) : bflo(ke[i][e >> 1]);
                    const float vx_ = (e & 1) ? bfhi(ve[e >> 1]) : bflo(ve[e >> 1]);
                    const float ax = (k4[0] * bfhi(wb) + k4[1] * bflo(wb) + k4[2] * bfhi(wa) + k4[3] * bflo(wa)) * vx_;
                    *(LAS unsigned*)(T + (8 * c16 + e) * TP + 128) = cvtpk(ax, 0.f);
                }
            }
        }
        WAITL0(); bar();
        rsrc_t r0 = mkrsrc(p.ws + WS_KR0, D * KRS * 2), r1 = mkrsrc(p.ws + WS_KR1, D * KRS * 2);
#pragma unroll
        for (int it = 0; it < 2; ++it) {
            const int item = tid + it * 512, ch = item >> 3, j = item & 7;
            const u32x4 a = *(LAS u32x4*)(T + ch * TP + j * 16);
            const unsigned nx = *(LAS unsigned*)(T + ch * TP + j * 16 + 16);
            const u32x4 sh = {__builtin_amdgcn_alignbit(a[1], a[0], 16), __builtin_amdgcn_alignbit(a[2], a[1], 16), __builtin_amdgcn_alignbit(a[3], a[2], 16), __builtin_amdgcn_alignbit(nx, a[3], 16)};
            const unsigned off = ((d0 + ch) * KRS + (1984 - s0) + 8 * j) * 2;
            __builtin_amdgcn_raw_buffer_store_b128(a, r0, off, 0, 16);
            __builtin_amdgcn_raw_buffer_store_b128(sh, r1, off, 0, 16);
        }
        if (kst == 0 && tid < 128) {
            const unsigned off = ((d0 + tid) * KRS + 2048) * 2;
            __builtin_amdgcn_raw_buffer_store_b128((u32x4){0, 0, 0, 0}, r0, off, 0, 16);
            __builtin_amdgcn_raw_buffer_store_b128((u32x4){0, 0, 0, 0}, r1, off, 0, 16);
        }
    } else if (wave < 5) {
        LAS char* buf = lds + P0_LDS + wave * 4096;
        const int T = unit / 10, dl = unit % 10;
#pragma unroll
        for (int i = 0; i < 4; ++i) { TIE(kr[i]); *(LAS u32x4*)(buf + (8 * i + (lane >> 3)) * 128 + (lane & 7) * 16) = kr[i]; }
        WAITL0();
        const int g = lane >> 4, q = (lane & 15) >> 2, pp = lane & 3, row = 4 * g + q;
        rsrc_t rw = mkrsrc(p.ws + WS_WT, F * D * 2);
        const int kh = T / 24, tt = (T % 24) >> 1, kk = T & 1;
#pragma unroll
        for (int i = 0; i < 4; ++i) {
            const s16x8 f = cat(trread(buf + row * 128 + 32 * i + 8 * pp), trread(buf + (row + 16) * 128 + 32 * i + 8 * pp));
            const int d16 = 4 * dl + i, dsl = d16 / 5, n = d16 % 5;
            __builtin_amdgcn_raw_buffer_store_b128(__builtin_bit_cast(u32x4, f), rw, ((((dsl * 12 + tt) * 4 + kh * 2 + kk) * 5 + n) * 64 + lane) * 16, 0, 16);
        }
    }
    STAMPT(2);

    if (wave >= 4) {
        const int w = wave - 4;
        WAITV(0); bar();
        STAMPT(3);
        int buf = 0;
        for (int t = 0; t < 10; ++t) {
            if (t <= 7) WAITV(10); else WAITV(0);
            bar();
            if (t + 3 <= 9) g1_issue(t + 3, buf);
            if (t == 2 && w == 0 && lane == 0) st_flag(p.ws, FL_P0 + b, tag);
            buf = (buf == 2) ? 0 : buf + 1;
        }
        STAMPT(4);
        poll4<64>(p.ws, FL_P0, tag, lane);
        if (b == 0 && w == 0 && lane == 0) st_flag(p.ws, FL_EPOCH, tag);
        STAMPT(5);
        const unsigned vo = lane * 16;
        if (has_conv) {
            const int d = cb * 4 + w;
            const unsigned sl = ldsb + w * CV_SLOT;
            rsrc_t rf = mkrsrc(p.ssk, D * S * 2), r0 = mkrsrc(p.ws + WS_KR0, D * KRS * 2), r1 = mkrsrc(p.ws + WS_KR1, D * KRS * 2);
#pragma unroll
            for (int i = 0; i < 4; ++i) dma16(sl + 512 + i * 1024, vo, rf, d * (S * 2) + i * 1024);
#pragma unroll
            for (int c = 0; c < 4; ++c)
#pragma unroll
                for (int i = 0; i < 4; ++i) dma16_sc1(sl + CV_KF + c * CV_COPY + i * 1024, vo, (c & 1) ? r1 : r0, d * (KRS * 2) + (c >> 1) * 4 + i * 1024);
            LAS char* slot = lds + w * CV_SLOT;
            if (lane < 32) *(LAS u32x4*)(slot + lane * 16) = (u32x4){0, 0, 0, 0};
            else if (lane < 48) *(LAS u32x4*)(slot + CV_KF + ((lane - 32) >> 2) * CV_COPY + 4096 + (lane & 3) * 16) = (u32x4){0, 0, 0, 0};
        }
        rsrc_t rw = mkrsrc(p.ws + WS_WT, F * D * 2);
        const unsigned sw2 = ds * (48 * 5 * 1024) + w * 5120;
        auto g2_issue = [&](int t, int bufi) {
#pragma unroll
            for (int j = 0; j < 5; ++j) dma16_sc1(ldsb + G2_RING + bufi * G2_STAGE + w * 5120 + j * 1024, vo, rw, sw2 + t * G2_STAGE + j * 1024);
        };
        g2_issue(0, 0); g2_issue(1, 1); g2_issue(2, 2);
        WAITV(15); WAITL0();
        bar();
        STAMPT(6);
        poll4<4>(p.ws, FL_H + bs * 0 + (st >> 1) * 16, tag, lane);
        WAITV(10);
        bar();
        STAMPT(7);
        rsrc_t ry = mkrsrc(p.ws + WS_YT, D * S * 2);
        bool ydone = false;
        auto load_y = [&]() {
            u32x4 yv[3];
#pragma unroll
            for (int i = 0; i < 3; ++i) {
                const int it = w + 4 * i, ch = 8 * it + (lane >> 3);
                yv[i] = __builtin_amdgcn_raw_buffer_load_b128(ry, it < 10 ? ((80 * ds + ch) * S + 64 * st + (lane & 7) * 8) * 2 : 0xF0000000u, 0, 16);
            }
#pragma unroll
            for (int i = 0; i < 3; ++i) {
                const int it = w + 4 * i, ch = 8 * it + (lane >> 3);
                if (it < 10) *(LAS u32x4*)(lds + G2_Y + ch * 128 + (lane & 7) * 16) = yv[i];
            }
        };
        int rb = 0;
        for (int t = 0; t < 12; ++t) {
            if (t <= 9) WAITV(5); else WAITV(0);
            bar();
            if (t + 3 < 12) g2_issue(t + 3, rb);
            if (t == 0 && w == 0 && lane == 0 && has_conv) st_flag(p.ws, FL_Y + cb, tag);
            if (t >= 9 && !ydone && poll4_once<5>(p.ws, FL_Y + 20 * ds, tag, lane)) { load_y(); ydone = true; }
            rb = (rb == 2) ? 0 : rb + 1;
        }
        if (!ydone) { poll4<5>(p.ws, FL_Y + 20 * ds, tag, lane); load_y(); }
        WAITL0();
        bar();
        STAMPT(8);
    } else {
        {
            const int wsx = wave & 1, wf = wave >> 1;
            const int g = lane >> 4, fr = lane & 15, q = (lane & 15) >> 2, pp = lane & 3;
            int oa[3];
#pragma unroll
            for (int i = 0; i < 3; ++i) {
                const int ft = 3 * wf + i, c = ft >> 1, half = ft & 1;
                oa[i] = 16384 + (g * 3 + c) * 512 + q * 64 + 16 * ((2 * half + (pp >> 1)) ^ (2 * (g & 1))) + 8 * (pp & 1);
            }
            const int ob0 = (64 * wsx + fr) * 128 + ((g ^ (fr >> 1)) * 16);
            const int ob1 = (64 * wsx + fr) * 128 + ((g ^ (fr >> 1) ^ 4) * 16);
            f32x4 accg[3][4], accu[3][4];
#pragma unroll
            for (int i = 0; i < 3; ++i)
#pragma unroll
                for (int m = 0; m < 4; ++m) { accg[i][m] = (f32x4){0, 0, 0, 0}; accu[i][m] = (f32x4){0, 0, 0, 0}; }
            struct Fr { s16x8 a[6]; s16x8 b[4]; } fa, fb;
            auto rd = [&](Fr& f, int ks, int sb) {
#pragma unroll
                for (int m = 0; m < 4; ++m) f.b[m] = *(LAS s16x8*)(lds + sb + (ks ? ob1 : ob0) + m * 2048);
#pragma unroll
                for (int i = 0; i < 3; ++i) {
                    f.a[i] = cat(trread(lds + sb + oa[i] + ks * 6144), trread(lds + sb + oa[i] + ks * 6144 + 256));
                    f.a[3 + i] = cat(trread(lds + sb + oa[i] + ks * 6144 + 12288), trread(lds + sb + oa[i] + ks * 6144 + 256 + 12288));
                }
            };
            auto mm = [&](const Fr& f) {
#pragma unroll
                for (int i = 0; i < 3; ++i)
#pragma unroll
                    for (int m = 0; m < 4; ++m) {
                        accg[i][m] = mfma16(f.a[i], f.b[m], accg[i][m]);
                        accu[i][m] = mfma16(f.a[3 + i], f.b[m], accu[i][m]);
                    }
            };
            auto interleave = [&]() {
#pragma unroll
                for (int i = 0; i < 16; ++i) { SGB(0x008, 1); SGB(0x100, 1); }
                SGB(0x008, 8);
                SB0();
            };
            bar();
            STAMPT(3);
            rd(fa, 0, 0);
            int sb = 0;
            for (int t = 0; t < 10; ++t) {
                rd(fb, 1, sb); mm(fa); interleave();
                WAITL0();
                if (t == 2) WAITV(0);
                bar();
                sb = (sb == 2 * G1_STAGE) ? 0 : sb + G1_STAGE;
                if (t < 9) rd(fa, 0, sb);
                mm(fb); interleave();
            }
            STAMPT(4);
            rsrc_t rh = mkrsrc(p.ws + WS_H, S * F * 2);
#pragma unroll
            for (int mp = 0; mp < 2; ++mp)
#pragma unroll
                for (int i = 0; i < 3; ++i) {
                    float hv[8];
#pragma unroll
                    for (int e = 0; e < 8; ++e) hv[e] = accu[i][2 * mp + (e >> 2)][e & 3] * sigmoidf_fast(accg[i][2 * mp + (e >> 2)][e & 3]);
                    const u32x4 o = {cvtpk(hv[0], hv[1]), cvtpk(hv[2], hv[3]), cvtpk(hv[4], hv[5]), cvtpk(hv[6], hv[7])};
                    const int sp = bs * 4 + wsx * 2 + mp, ft = bf * 6 + wf * 3 + i;
                    __builtin_amdgcn_raw_buffer_store_b128(o, rh, ((sp * 96 + ft) * 64 + lane) * 16, 0, 16);
                }
        }
        STAMPT(5);
        auto publish_h = [&]() {
            WAITV(0);
            unsigned old = 0;
            if (lane == 0) old = __hip_atomic_fetch_add((LAS unsigned*)(lds + L_CNT), 1u, __ATOMIC_RELAXED, __HIP_MEMORY_SCOPE_WORKGROUP);
            if (lane == 0 && old == 3) st_flag(p.ws, FL_H + bs * 16 + bf, tag);
        };
        if (wave == 0 && lane == 0) *(LAS unsigned*)(lds + L_CNT) = 0;
        WAITL0();
        bar();
        STAMPT(6);
        if (has_conv) {
            const int d = cb * 4 + wave;
            LAS char* slot = lds + wave * CV_SLOT;
            const int a = lane & 15, g = lane >> 4;
            f32x4 acc[8];
#pragma unroll
            for (int n = 0; n < 8; ++n) acc[n] = (f32x4){0, 0, 0, 0};
            LAS char* pg = slot + 480 + 32 * a + 16 * g;
            LAS char* pf = slot + CV_KF + (3 - (a & 3)) * CV_COPY + 472 + 16 * g - 8 * (a >> 2);
            s16x8 GA[8], GB[8], F0, F1;
            auto rdF = [&](LAS char* base, int I) { return cat(*(LAS s16x4*)(base + 512 * (7 - I)), *(LAS s16x4*)(base + 512 * (7 - I) + 8)); };
            auto residue = [&](const s16x8 (&G)[8], s16x8 (&Gn)[8], bool more) {
#pragma unroll
                for (int I = 0; I < 8; ++I) {
                    s16x8& Fc = (I & 1) ? F1 : F0;
                    s16x8& Fn = (I & 1) ? F0 : F1;
                    if (I < 7) Fn = rdF(pf, I + 1);
                    else Fn = rdF(pf - 64, 0);
                    if (I < 4) { Gn[2 * I] = *(LAS s16x8*)(pg - 64 + 512 * (2 * I)); Gn[2 * I + 1] = *(LAS s16x8*)(pg - 64 + 512 * (2 * I + 1)); }
#pragma unroll
                    for (int n = I; n < 8; ++n) acc[n] = mfma16(Fc, G[n - I], acc[n]);
                    if (I < 4) { SGB(0x008, 1); SGB(0x100, 1); SGB(0x008, 1); SGB(0x100, 1); SGB(0x008, 1); SGB(0x100, 1); SGB(0x008, 1); SGB(0x100, 1); SGB(0x008, 4); }
                    else { SGB(0x100, 2); SGB(0x008, 4); }
                    SB0();
                }
                pg -= 64; pf -= 64;
                (void)more;
            };
#pragma unroll
            for (int j = 0; j < 8; ++j) GA[j] = *(LAS s16x8*)(pg + 512 * j);
            F0 = rdF(pf, 0);
            for (int r2 = 0; r2 < 4; ++r2) {
                residue(GA, GB, true);
                residue(GB, GA, true);
                if (r2 == 1) publish_h();
            }
            STAMPT(7);
            const float Dd = bflo(p.sd[d]);
            rsrc_t ry = mkrsrc(p.ws + WS_YT, D * S * 2);
            u32x2 o[8];
#pragma unroll
            for (int n = 0; n < 8; ++n) {
                const u32x2 kv = *(LAS u32x2*)(slot + CV_KF + 2 * (2044 - 256 * n - 16 * a - 4 * g));
                o[n][0] = cvtpk(acc[n][0] + bfhi(kv[1]) * Dd, acc[n][1] + bflo(kv[1]) * Dd);
                o[n][1] = cvtpk(acc[n][2] + bfhi(kv[0]) * Dd, acc[n][3] + bflo(kv[0]) * Dd);
            }
#pragma unroll
            for (int n = 0; n < 8; n += 2) {
                auto x = __builtin_amdgcn_permlane16_swap(o[n][0], o[n + 1][0], false, false);
                auto y = __builtin_amdgcn_permlane16_swap(o[n][1], o[n + 1][1], false, false);
                const u32x4 val = {x[0], y[0], x[1], y[1]};
                const int s = 256 * (n + (g & 1)) + 16 * a + 4 * (g & 2);
                __builtin_amdgcn_raw_buffer_store_b128(val, ry, (d * S + s) * 2, 0, 16);
            }
        } else {
            publish_h();
        }
        STAMPT(8);
        {
            const int sp = wave & 1, kh = wave >> 1;
            rsrc_t rh = mkrsrc(p.ws + WS_H, S * F * 2);
            const unsigned ho = (((st * 2 + sp) * 96 + kh * 48) * 64 + lane) * 16;
            bar();
            STAMPT(9);
            u32x4 hf[16];
#pragma unroll
            for (int i = 0; i < 16; ++i) hf[i] = __builtin_amdgcn_raw_buffer_load_b128(rh, ho, i * 1024, 16);
            const int s16 = lane & 15, g = lane >> 4, q = s16 >> 2, pp = lane & 3;
            const int srow = 64 * st + 32 * sp + 16 * kh + s16;
            const float gt = __hip_atomic_load((const GAS float*)(p.ws + WS_G) + srow, RLX_AGENT);
            u32x2 qv[5];
#pragma unroll
            for (int n = 0; n < 5; ++n) qv[n] = *(const u32x2*)(p.q + (size_t)srow * D + 80 * ds + 16 * n + 4 * g);
            f32x4 acc[2][5];
#pragma unroll
            for (int n = 0; n < 5; ++n) { acc[0][n] = (f32x4){0, 0, 0, 0}; acc[1][n] = (f32x4){0, 0, 0, 0}; }
            LAS char* pw = lds + G2_RING + kh * 10240 + lane * 16;
#pragma unroll
            for (int t = 0; t < 12; ++t) {
#pragma unroll
                for (int kk = 0; kk < 2; ++kk) {
                    const int T = 2 * t + kk;
                    s16x8 wf[5];
#pragma unroll
                    for (int n = 0; n < 5; ++n) wf[n] = *(LAS s16x8*)(pw + (t % 3) * G2_STAGE + kk * 5120 + n * 1024);
                    const u32x4 r0 = hf[(2 * T) & 15], r1 = hf[(2 * T + 1) & 15];
                    const s16x8 h0 = __builtin_bit_cast(s16x8, (u32x4){r0[0], r0[1], r1[0], r1[1]});
                    const s16x8 h1 = __builtin_bit_cast(s16x8, (u32x4){r0[2], r0[3], r1[2], r1[3]});
#pragma unroll
                    for (int n = 0; n < 5; ++n) { acc[0][n] = mfma16(wf[n], h0, acc[0][n]); acc[1][n] = mfma16(wf[n], h1, acc[1][n]); }
                    if (2 * T + 16 < 48) {
                        hf[(2 * T) & 15] = __builtin_amdgcn_raw_buffer_load_b128(rh, ho, (2 * T + 16) * 1024, 16);
                        hf[(2 * T + 1) & 15] = __builtin_amdgcn_raw_buffer_load_b128(rh, ho, (2 * T + 17) * 1024, 16);
                    }
                }
                WAITL0();
                bar();
            }
            STAMPT(10);
#pragma unroll
            for (int n = 0; n < 5; ++n) *(LAS f32x4*)(lds + G2_RED + (wave * 5 + n) * 1024 + lane * 16) = kh ? acc[0][n] : acc[1][n];
            WAITL0();
            bar();
            STAMPT(11);
#pragma unroll
            for (int n = 0; n < 5; ++n) {
                const f32x4 o4 = (kh ? acc[1][n] : acc[0][n]) + *(LAS f32x4*)(lds + G2_RED + ((wave ^ 2) * 5 + n) * 1024 + lane * 16);
                const u32x2 yv = __builtin_bit_cast(u32x2, trread(lds + G2_Y + (16 * n + 4 * g + q) * 128 + (32 * sp + 16 * kh + 4 * pp) * 2));
                const float o0 = gt * (bflo(qv[n][0]) * bflo(yv[0])) + (1.f - gt) * o4[0];
                const float o1 = gt * (bfhi(qv[n][0]) * bfhi(yv[0])) + (1.f - gt) * o4[1];
                const float o2 = gt * (bflo(qv[n][1]) * bflo(yv[1])) + (1.f - gt) * o4[2];
                const float o3 = gt * (bfhi(qv[n][1]) * bfhi(yv[1])) + (1.f - gt) * o4[3];
                *(u32x2*)(p.out + (size_t)srow * D + 80 * ds + 16 * n + 4 * g) = (u32x2){cvtpk(o0, o1), cvtpk(o2, o3)};
            }
            STAMPT(12);
        }
    }
}

extern "C" void kernel_launch(void* const* d_in, const int* in_sizes, int n_in,
                              void* d_out, int out_size, void* d_ws, size_t ws_size,
                              hipStream_t stream) {
    Params p;
    p.q = (const u16*)d_in[0]; p.k = (const u16*)d_in[1]; p.v = (const u16*)d_in[2];
    p.sk = (const u16*)d_in[3]; p.ssk = (const u16*)d_in[4]; p.sd = (const u16*)d_in[5];
    p.wg = (const u16*)d_in[7]; p.wu = (const u16*)d_in[8]; p.wd = (const u16*)d_in[9]; p.gw = (const u16*)d_in[10];
    p.out = (u16*)d_out; p.ws = (char*)d_ws;
    hipFuncSetAttribute((const void*)k_fused, hipFuncAttributeMaxDynamicSharedMemorySize, LDS_BYTES);
    k_fused<<<256, 512, LDS_BYTES, stream>>>(p);
}
```

```cpp
#include <hip/hip_runtime.h>
#include <hip/hip_bf16.h>
#include <stdint.h>

typedef unsigned short u16;
typedef short s16x4 __attribute__((ext_vector_type(4)));
typedef short s16x8 __attribute__((ext_vector_type(8)));
typedef __bf16 b16x8 __attribute__((ext_vector_type(8)));
typedef __bf16 b16x2 __attribute__((ext_vector_type(2)));
typedef float f32x2 __attribute__((ext_vector_type(2)));
typedef float f32x4 __attribute__((ext_vector_type(4)));
typedef unsigned u32x2 __attribute__((ext_vector_type(2)));
typedef unsigned u32x4 __attribute__((ext_vector_type(4)));
#define LAS __attribute__((address_space(3)))
#define GAS __attribute__((address_space(1)))
#define DI __device__ __forceinline__

static constexpr int S = 2048, D = 640, F = 1536;
static constexpr int KRS = 2112;
static constexpr unsigned WS_H = 0, WS_KR0 = 8u << 20, WS_KR1 = 11u << 20, WS_YT = 14u << 20, WS_G = 17u << 20, WS_WT = 18u << 20,
                          WS_STAMP = 20u << 20, WS_FLAG = 24u << 20;
static constexpr unsigned FL_EPOCH = 0, FL_KR = 1024, FL_WT = 2048, FL_H = 4096, FL_Y = 6144;

struct Params {
    const u16 *q, *k, *v, *sk, *ssk, *sd, *wg, *wu, *wd, *gw;
    u16* out; char* ws;
};

DI float bflo(unsigned x) { return __uint_as_float(x << 16); }
DI float bfhi(unsigned x) { return __uint_as_float(x & 0xffff0000u); }
DI unsigned cvtpk(float lo, float hi) { f32x2 v = {lo, hi}; b16x2 b = __builtin_convertvector(v, b16x2); return __builtin_bit_cast(unsigned, b); }
DI unsigned rfl(unsigned x) { return __builtin_amdgcn_readfirstlane(x); }
DI float sigmoidf_fast(float x) { return __builtin_amdgcn_rcpf(1.f + __builtin_amdgcn_exp2f(x * -1.44269504089f)); }
DI f32x4 mfma16(s16x8 a, s16x8 b, f32x4 c) {
    return __builtin_amdgcn_mfma_f32_16x16x32_bf16(__builtin_bit_cast(b16x8, a), __builtin_bit_cast(b16x8, b), c, 0, 0, 0);
}
DI s16x4 trread(LAS char* p) { return __builtin_amdgcn_ds_read_tr16_b64_v4i16((LAS s16x4*)p); }
DI s16x8 cat(s16x4 a, s16x4 b) { return __builtin_shufflevector(a, b, 0, 1, 2, 3, 4, 5, 6, 7); }
typedef __amdgpu_buffer_rsrc_t rsrc_t;
DI rsrc_t mkrsrc(const void* p, unsigned bytes) { return __builtin_amdgcn_make_buffer_rsrc((void*)p, 0, bytes, 0x00020000); }
DI void dma16(unsigned m0v, unsigned voff, rsrc_t r, unsigned soff) {
    asm volatile("s_mov_b32 m0, %0\n\ts_nop 0\n\tbuffer_load_dwordx4 %1, %2, %3 offen lds" ::"s"(m0v), "v"(voff), "s"(r), "s"(soff) : "m0", "memory");
}
DI void dma16_sc1(unsigned m0v, unsigned voff, rsrc_t r, unsigned soff) {
    asm volatile("s_mov_b32 m0, %0\n\ts_nop 0\n\tbuffer_load_dwordx4 %1, %2, %3 offen sc1 lds" ::"s"(m0v), "v"(voff), "s"(r), "s"(soff) : "m0", "memory");
}
DI u32x4 aload(rsrc_t r, unsigned voff) { u32x4 x; asm volatile("buffer_load_dwordx4 %0, %1, %2, 0 offen" : "=v"(x) : "v"(voff), "s"(r) : "memory"); return x; }
DI void prefetch_lines(unsigned& dummy, rsrc_t r, unsigned voff) { asm volatile("buffer_load_dword %0, %1, %2, 0 offen" : "+v"(dummy) : "v"(voff), "s"(r) : "memory"); }
template <int N> DI void waitv() { asm volatile("s_waitcnt vmcnt(%0)" ::"n"(N) : "memory"); }
DI float dot2(unsigned a, unsigned b, float c) { return __builtin_amdgcn_fdot2_f32_bf16(__builtin_bit_cast(b16x2, a), __builtin_bit_cast(b16x2, b), c, false); }
template <int CTRL, int RM> DI float dpp_add(float x) {
    return x + __int_as_float(__builtin_amdgcn_update_dpp(0, __float_as_int(x), CTRL, RM, 0xf, true));
}
DI float wave_sum(float x) {
    x = dpp_add<0x111, 0xf>(x); x = dpp_add<0x112, 0xf>(x); x = dpp_add<0x114, 0xf>(x); x = dpp_add<0x118, 0xf>(x);
    x = dpp_add<0x142, 0xa>(x); x = dpp_add<0x143, 0xc>(x);
    return __int_as_float(__builtin_amdgcn_readlane(__float_as_int(x), 63));
}
#define TIE(x) asm volatile("" : "+v"(x))
#define WAITV(n) asm volatile("s_waitcnt vmcnt(" #n ")" ::: "memory")
#define WAITL0() asm volatile("s_waitcnt lgkmcnt(0)" ::: "memory")
DI void bar() { asm volatile("" ::: "memory"); __builtin_amdgcn_s_barrier(); asm volatile("" ::: "memory"); }
#define SGB(m, n) __builtin_amdgcn_sched_group_barrier(m, n, 0)
#define SB0() __builtin_amdgcn_sched_barrier(0)
#define HOFF(i) (i)
#define HW 24
#define WK 6
#define LPRIO 2
#define NPUT 0
#define TP0 4
#define SLEEP0 8
#define RLX_AGENT __ATOMIC_RELAXED, __HIP_MEMORY_SCOPE_AGENT

DI void st_flag(char* ws, unsigned word, unsigned tag) { __hip_atomic_store((GAS unsigned*)(ws + WS_FLAG) + word, tag, RLX_AGENT); }
template <int NW4>
DI bool poll4_once(char* ws, unsigned word, unsigned tag, int lane) {
    rsrc_t r = mkrsrc(ws + WS_FLAG, 32768);
    u32x4 x;
    asm volatile("buffer_load_dwordx4 %0, %1, %2, 0 offen sc1\n\ts_waitcnt vmcnt(0)" : "=&v"(x) : "v"((word + (lane < NW4 ? lane : 0) * 4) * 4), "s"(r) : "memory");
    return __all(x[0] == tag && x[1] == tag && x[2] == tag && x[3] == tag);
}
template <int NW4>
DI void poll4(char* ws, unsigned word, unsigned tag, int lane) {
    for (int spins = 0; spins < (1 << 18); ++spins) {
        if (poll4_once<NW4>(ws, word, tag, lane)) return;
        __builtin_amdgcn_s_sleep(4);
    }
}
DI void lds_set(LAS char* lds, int off, unsigned v) { __hip_atomic_store((LAS unsigned*)(lds + off), v, __ATOMIC_RELAXED, __HIP_MEMORY_SCOPE_WORKGROUP); }
DI void lds_wait(LAS char* lds, int off, unsigned v) {
    for (int spins = 0; spins < (1 << 22); ++spins) {
        if (rfl(__hip_atomic_load((LAS unsigned*)(lds + off), __ATOMIC_RELAXED, __HIP_MEMORY_SCOPE_WORKGROUP)) == v) break;
        __builtin_amdgcn_s_sleep(1);
    }
    asm volatile("" ::: "memory");
}

#define STAMPC(i) do {} while (0)
#define STAMPT(i) do {} while (0)

constexpr int G1_STAGE = 40960;
constexpr int P0_LDS = 122880;
constexpr int P0_PP = 1040;
constexpr int P0_RAWK = P0_LDS, P0_W = P0_LDS + 17 * P0_PP, P0_T = P0_W + 1024, P0_G = P0_T + 128 * 144;
constexpr int TP = 144;
constexpr int CV_SLOT = 21248, CV_COPY = 4160, CV_KF = 4608;
constexpr int G2_RING = 86016, G2_STAGE = 20480;
constexpr int G2_Y = G2_RING + 3 * G2_STAGE;
constexpr int G2_HST = 16384;
constexpr int G2_W3 = 65536;
constexpr int G2_RED = 0;
constexpr int L_CNT = 163776;
constexpr int LDS_BYTES = 163840;

struct Ctx {
    Params p; LAS char* lds; unsigned ldsb; int lane, w, b, xcd, idx, cb, bs, bf, st, ds; unsigned tag;
};

template <bool KV>
DI void loader_gemm1(const Ctx& c) {
    const Params& p = c.p;
    const int lane = c.lane, w = c.w, wave = w + 4;
    (void)wave;
    LAS char* lds = c.lds;
    rsrc_t rq = mkrsrc(p.q, S * D * 2), rg_ = mkrsrc(p.wg, D * F * 2), ru_ = mkrsrc(p.wu, D * F * 2);
    unsigned vx[2], vw[3];
    {
        const int r = lane >> 3, slot = lane & 7;
        vx[0] = r * 1280 + ((slot ^ (r >> 1)) * 16);
        vx[1] = r * 1280 + ((slot ^ (r >> 1) ^ 4) * 16);
#pragma unroll
        for (int j = 0; j < 3; ++j) {
            const int L = 2 * j + (lane >> 5), kbl = L / 3, cc = L % 3, row = (lane & 31) >> 2, ch = (lane & 3) ^ (2 * kbl);
            vw[j] = (kbl * 8 + row) * 3072 + cc * 64 + ch * 16;
        }
    }
    const unsigned sx0 = (c.bs * 128 + 32 * w) * 1280, sw0 = 16 * w * 3072 + c.bf * 192;
    auto issue = [&](int t) {
        const unsigned base = c.ldsb + (t >= 9 ? t - 9 : (t >= 6 ? t - 6 : (t >= 3 ? t - 3 : t))) * G1_STAGE;
#pragma unroll
        for (int i = 0; i < 4; ++i) dma16(base + (32 * w + 8 * i) * 128, vx[i & 1], rq, sx0 + i * 8 * 1280 + t * 128);
#pragma unroll
        for (int j = 0; j < 3; ++j) dma16(base + 16384 + w * 3072 + j * 1024, vw[j], rg_, sw0 + t * 64 * 3072);
#pragma unroll
        for (int j = 0; j < 3; ++j) dma16(base + 28672 + w * 3072 + j * 1024, vw[j], ru_, sw0 + t * 64 * 3072);
    };
    if (w == 0 && lane < 8) *(LAS unsigned*)(lds + L_CNT + lane * 4) = 0;
    issue(0); issue(1);
    waitv<10>(); WAITL0(); bar();
    STAMPT(3);
    issue(2);
    constexpr int NA = KV ? 5 : 3, NB = KV ? 5 : 2, NG = 5, NS = KV ? 5 : 6, TS = TP0 + 2;
    const int kst = c.cb / 5, kdc = c.cb % 5, s0 = kst * 64, d0 = kdc * 128;
    const int unit0 = (c.xcd * 12 + c.idx - 20) * 5;
    const int tid = w * 64 + lane, c16 = tid >> 4, rg = tid & 15;
    const int grow = c.bs * 128 + c.bf * 8 + 2 * w;
    unsigned dummy = 0;
    auto raw = [&](int j) {
        if (KV) {
            rsrc_t rk = mkrsrc(p.k, S * D * 2), rsk = mkrsrc(p.sk, D * 8);
            const unsigned vo = (lane >> 4) * (D * 2) + (lane & 15) * 16;
            if (j < 17) dma16(c.ldsb + P0_RAWK + j * P0_PP, vo + (unsigned)(((s0 - 3 + 4 * j) * D + d0) * 2), rk, 0);
            else if (j == 17) dma16(c.ldsb + P0_W, lane * 16, rsk, d0 * 8);
            else dma16(c.ldsb + P0_G, 0xF0000000u, rsk, 0);
        } else {
            rsrc_t rwd = mkrsrc(p.wd, F * D * 2);
            const unsigned vo = (lane >> 3) * (D * 2) + (lane & 7) * 16;
            const int u = j >> 2, unit = unit0 + u, T = unit / 10, dl = unit % 10;
            dma16(c.ldsb + P0_LDS + j * 1024, vo, rwd, ((32 * T + 8 * (j & 3)) * D + 64 * dl) * 2);
        }
    };
    u32x4 g0 = {0, 0, 0, 0}, g1 = g0, gt_ = g0, gwa = g0, gwb = g0, vr[4] = {g0, g0, g0, g0};
    for (int t = 0; t < 10; ++t) {
        if (t == 1) waitv<10 + NA + NG>();
        else if (t == 2) { if (TP0 > 2) waitv<NA + NG + 10 + NB>(); else waitv<10>(); }
        else if (t == 3) { if (TP0 > 3) waitv<NB + 10>(); else waitv<10>(); }
        else if (t == TS + 1) { if (TS + 3 <= 9) waitv<10 + NS>(); else waitv<NS>(); }
        else if (t == TS + 2) { if (TS + 4 <= 9) waitv<NS + 10>(); else waitv<NS>(); }
        else if (t >= TS + 3 || t >= 8) waitv<0>();
        else waitv<10>();
        WAITL0();
        bar();
        if (t + 3 <= 9) issue(t + 3);
        if (t == 0) {
#pragma unroll
            for (int i = 0; i < NA; ++i) raw(w + 4 * i);
            rsrc_t rgw = mkrsrc(p.gw, D * 2);
            const unsigned ro = grow * (D * 2);
            g0 = aload(rq, ro + lane * 16); g1 = aload(rq, ro + D * 2 + lane * 16);
            gt_ = aload(rq, lane < 32 ? ro + (lane >> 4) * (D * 2) + 1024 + (lane & 15) * 16 : 0xF0000000u);
            gwa = aload(rgw, lane * 16); gwb = aload(rgw, 1024 + (lane & 15) * 16);
        } else if (t == 1) {
            if (KV) {
                rsrc_t rv = mkrsrc(p.v, S * D * 2), rf = mkrsrc(p.ssk, D * S * 2);
#pragma unroll
                for (int i = 0; i < 4; ++i) vr[i] = aload(rv, ((s0 + 4 * rg + i) * D + d0 + 8 * c16) * 2);
                prefetch_lines(dummy, rf, lane < 32 ? (c.cb * 4 + w) * (S * 2) + lane * 128 : 0xF0000000u);
            } else {
#pragma unroll
                for (int i = 0; i < NB; ++i) raw(w + 4 * (NA + i));
            }
        } else if (t == TP0 || t == TP0 + 1) {
            if (KV) {
                __builtin_amdgcn_s_setprio(LPRIO);
                TIE(vr[0]); TIE(vr[1]); TIE(vr[2]); TIE(vr[3]); TIE(dummy);
                for (int e2 = 2 * (t - TP0); e2 < 2 * (t - TP0) + 2; ++e2) {
                    unsigned kd[7];
#pragma unroll
                    for (int i = 0; i < 7; ++i) kd[i] = *(LAS unsigned*)(lds + P0_RAWK + (rg + (i >> 2)) * P0_PP + (i & 3) * 256 + c16 * 16 + e2 * 4);
                    const u32x4 w4 = *(LAS u32x4*)(lds + P0_W + c16 * 64 + e2 * 16);
#pragma unroll
                    for (int hf = 0; hf < 2; ++hf) {
                        const unsigned sel = hf ? 0x07060302u : 0x05040100u;
                        unsigned pr[6];
#pragma unroll
                        for (int i = 0; i < 6; ++i) pr[i] = __builtin_amdgcn_perm(kd[i], kd[i + 1], sel);
                        float o[4];
#pragma unroll
                        for (int r = 0; r < 4; ++r) {
                            const unsigned vd = vr[r][e2];
                            o[r] = dot2(pr[r + 2], w4[2 * hf], dot2(pr[r], w4[2 * hf + 1], 0.f)) * (hf ? bfhi(vd) : bflo(vd));
                        }
                        *(LAS u32x2*)(lds + P0_T + (8 * c16 + 2 * e2 + hf) * TP + (60 - 4 * rg) * 2) = (u32x2){cvtpk(o[3], o[2]), cvtpk(o[1], o[0])};
                    }
                }
                __builtin_amdgcn_s_setprio(0);
            }
        } else if (t == TS) {
            if (KV) {
                rsrc_t r0 = mkrsrc(p.ws + WS_KR0, D * KRS * 2);
#pragma unroll
                for (int it = 0; it < 4; ++it) {
                    const int item = tid + it * 256, ch = item >> 3, j = item & 7;
                    __builtin_amdgcn_raw_buffer_store_b128(*(LAS u32x4*)(lds + P0_T + ch * TP + j * 16), r0, ((d0 + ch) * KRS + (1984 - s0) + 8 * j) * 2, 0, 16);
                }
            } else {
                const int g4 = lane >> 4, q = (lane & 15) >> 2, pp = lane & 3, row = 4 * g4 + q;
                rsrc_t rw = mkrsrc(p.ws + WS_WT, F * D * 2);
#pragma unroll
                for (int it = 0; it < 5; ++it) {
                    const int u = it < 4 ? w : 4, i = it < 4 ? it : w, unit = unit0 + u, Tk = unit / 10, dl = unit % 10;
                    const int kh = Tk / 24, tt = (Tk % 24) >> 1, kk = Tk & 1, d16 = 4 * dl + i, dsl = d16 / 5, n = d16 % 5;
                    LAS char* buf = lds + P0_LDS + u * 4096;
                    const s16x8 f = cat(trread(buf + row * 128 + 32 * i + 8 * pp), trread(buf + (row + 16) * 128 + 32 * i + 8 * pp));
                    __builtin_amdgcn_raw_buffer_store_b128(__builtin_bit_cast(u32x4, f), rw, ((((dsl * 12 + tt) * 4 + kh * 2 + kk) * 5 + n) * 64 + lane) * 16, 0, 16);
                }
            }
            TIE(g0); TIE(g1); TIE(gt_); TIE(gwa); TIE(gwb);
            float a0 = 0.f, a1 = 0.f, at = 0.f;
#pragma unroll
            for (int i = 0; i < 4; ++i) { a0 = dot2(g0[i], gwa[i], a0); a1 = dot2(g1[i], gwa[i], a1); at = dot2(gt_[i], gwb[i], at); }
            a0 += lane < 16 ? at : 0.f;
            a1 += (lane >= 16 && lane < 32) ? at : 0.f;
            a0 = wave_sum(a0); a1 = wave_sum(a1);
            rsrc_t rG = mkrsrc(p.ws + WS_G, S * 4);
            __builtin_amdgcn_raw_buffer_store_b32(__float_as_uint(sigmoidf_fast(lane ? a1 : a0)), rG, lane < 2 ? (grow + lane) * 4 : 0xF0000000u, 0, 16);
        } else if (t == (TS + 3 < 9 ? TS + 3 : 9)) {
            if (w == 0) {
                if (KV) { if (lane == 0) st_flag(p.ws, FL_KR + (c.cb % 5) * 64 + c.cb / 5, c.tag); }
                else if (lane < 8) st_flag(p.ws, FL_WT + lane * 128 + c.xcd * 12 + c.idx - 20, c.tag);
            }
            STAMPT(10);
        }
    }
}

__global__ __launch_bounds__(512, 2) void k_fused(Params p) {
    extern __shared__ __attribute__((aligned(16))) char smem[];
    LAS char* lds = (LAS char*)smem;
    const unsigned ldsb = (unsigned)(size_t)lds;
    const int wave = rfl(threadIdx.x >> 6), lane = threadIdx.x & 63;
    const int b = blockIdx.x, xcd = b & 7, idx = b >> 3;
    const bool has_conv = idx < 20;
    const int cb = xcd * 20 + idx;
    const int bs = (xcd & 1) * 8 + (idx & 7), bf = (xcd >> 1) * 4 + (idx >> 3);
    const int st = xcd * 4 + (idx & 3), ds = idx >> 2;
    STAMPT(0);

    if (wave >= 4) {
        const int w = wave - 4;
        Ctx c; c.p = p; c.lds = lds; c.ldsb = ldsb; c.lane = lane; c.w = w; c.b = b; c.xcd = xcd; c.idx = idx; c.cb = cb; c.bs = bs; c.bf = bf; c.st = st; c.ds = ds;
        c.tag = rfl(*(const GAS unsigned*)(p.ws + WS_FLAG)) + 1u;
        const unsigned tag = c.tag;
        if (has_conv) loader_gemm1<true>(c); else loader_gemm1<false>(c);
        STAMPT(4);
        const unsigned vo = lane * 16;
        const int d = cb * 4 + w;
        const unsigned sl = ldsb + w * CV_SLOT;
        if (has_conv) {
            rsrc_t rf = mkrsrc(p.ssk, D * S * 2);
#pragma unroll
            for (int i = 0; i < 4; ++i) dma16(sl + 512 + i * 1024, vo, rf, d * (S * 2) + i * 1024);
            LAS char* slot = lds + w * CV_SLOT;
            if (lane < 32) *(LAS u32x4*)(slot + lane * 16) = (u32x4){0, 0, 0, 0};
            else if (lane < 48) *(LAS u32x4*)(slot + CV_KF + ((lane - 32) >> 2) * CV_COPY + 4096 + (lane & 3) * 16) = (u32x4){0, 0, 0, 0};
        }
        if (w == 0) {
            if (has_conv) poll4<8>(p.ws, FL_KR + (cb >> 5) * 64, tag, lane);
            poll4<24>(p.ws, FL_WT + xcd * 128, tag, lane);
            if (lane == 0) lds_set(lds, L_CNT + 8, 1u);
        } else {
            lds_wait(lds, L_CNT + 8, 1u);
        }
        STAMPT(5);
        if (has_conv) {
            rsrc_t r0 = mkrsrc(p.ws + WS_KR0, D * KRS * 2);
#pragma unroll
            for (int i = 0; i < 4; ++i) dma16_sc1(sl + CV_KF + i * 1024, vo, r0, d * (KRS * 2) + i * 1024);
        }
        rsrc_t rw = mkrsrc(p.ws + WS_WT, F * D * 2);
        const unsigned sw2 = ds * (48 * 5 * 1024) + w * 5120;
        const int rot = (3 * ds) >> 1;
        auto g2_src = [&](int t) { const int ts = t + rot; return sw2 + (ts >= 12 ? ts - 12 : ts) * G2_STAGE; };
#pragma unroll
        for (int t = 0; t < 3; ++t)
#pragma unroll
            for (int j = 0; j < 5; ++j) dma16_sc1(ldsb + G2_RING + t * G2_STAGE + w * 5120 + j * 1024, vo, rw, g2_src(t) + j * 1024);
        if (has_conv) {
            waitv<15>(); WAITL0();
            LAS char* c0 = lds + w * CV_SLOT + CV_KF;
#pragma unroll
            for (int j = 0; j < 4; ++j) {
                const u32x4 a = *(LAS u32x4*)(c0 + j * 1024 + lane * 16);
                const u32x2 nx = *(LAS u32x2*)(c0 + j * 1024 + lane * 16 + 16);
                *(LAS u32x4*)(c0 + CV_COPY + j * 1024 + lane * 16) =
                    (u32x4){__builtin_amdgcn_alignbit(a[1], a[0], 16), __builtin_amdgcn_alignbit(a[2], a[1], 16), __builtin_amdgcn_alignbit(a[3], a[2], 16), __builtin_amdgcn_alignbit(nx[0], a[3], 16)};
                *(LAS u32x4*)(c0 + 2 * CV_COPY + j * 1024 + lane * 16) = (u32x4){a[1], a[2], a[3], nx[0]};
                *(LAS u32x4*)(c0 + 3 * CV_COPY + j * 1024 + lane * 16) =
                    (u32x4){__builtin_amdgcn_alignbit(a[2], a[1], 16), __builtin_amdgcn_alignbit(a[3], a[2], 16), __builtin_amdgcn_alignbit(nx[0], a[3], 16), __builtin_amdgcn_alignbit(nx[1], nx[0], 16)};
            }
        }
        WAITL0();
        bar();
        STAMPT(6);
        u32x4 wreg[9][NPUT + 1];
#pragma unroll
        for (int t = 0; t < 9; ++t)
#pragma unroll
            for (int j = 0; j < NPUT; ++j) wreg[t][j] = __builtin_amdgcn_raw_buffer_load_b128(rw, vo, g2_src(t + 3) + j * 1024, 16);
        unsigned dummy = 0;
        if (NPUT < 5) {
#pragma unroll
            for (int i = 0; i < 2; ++i) {
                const int ln = (idx & 3) * 480 + (w * 2 + i) * 64 + lane;
                prefetch_lines(dummy, rw, (w * 2 + i) * 64 + lane < 480 ? ds * (48 * 5 * 1024) + ln * 128 : 0xF0000000u);
            }
        }
        if (b == 0 && w == 0) {
            rsrc_t rfl_ = mkrsrc(p.ws + WS_FLAG, 32768);
            const unsigned fo = (lane < 40 ? FL_KR + (lane >> 3) * 64 + (lane & 7) * 4 : FL_WT + (lane - 40) * 4) * 4;
            for (int spins = 0; spins < (1 << 18); ++spins) {
                u32x4 x;
                asm volatile("buffer_load_dwordx4 %0, %1, %2, 0 offen sc1\n\ts_waitcnt vmcnt(0)" : "=&v"(x) : "v"(fo), "s"(rfl_) : "memory");
                if (__all(x[0] == tag && x[1] == tag && x[2] == tag && x[3] == tag)) break;
                __builtin_amdgcn_s_sleep(4);
            }
            if (lane == 0) st_flag(p.ws, FL_EPOCH, tag);
        }
        if (w == 0) poll4<4>(p.ws, FL_H + (st >> 1) * 64, tag, lane);
        WAITV(0);
#pragma unroll
        for (int t = 0; t < 9; ++t)
#pragma unroll
            for (int j = 0; j < NPUT; ++j) TIE(wreg[t][j]);
        TIE(dummy);
        bar();
        STAMPT(7);
        rsrc_t rh = mkrsrc(p.ws + WS_H, S * F * 2);
        const unsigned sh2 = ((st * 2 + (w & 1)) * 96 + (w >> 1) * 48) * 1024;
        auto wslot = [&](int t) { return (t & 3) < 3 ? G2_RING + (t & 3) * G2_STAGE : G2_W3; };
        auto g2_dma = [&](int t) {
            const int ts = t + rot;
            const unsigned src = sh2 + (ts >= 12 ? ts - 12 : ts) * 4096, dst = ldsb + (t & 3) * G2_HST + w * 4096;
#pragma unroll
            for (int j = 0; j < 4; ++j) dma16_sc1(dst + j * 1024, vo, rh, src + j * 1024);
            if (t >= 3) {
#pragma unroll
                for (int j = NPUT; j < 5; ++j) dma16_sc1(ldsb + wslot(t) + w * 5120 + j * 1024, vo, rw, g2_src(t) + j * 1024);
            }
        };
        auto g2_put = [&](int t) {
            LAS char* base = lds + wslot(t) + w * 5120 + lane * 16;
#pragma unroll
            for (int j = 0; j < NPUT; ++j) *(LAS u32x4*)(base + j * 1024) = wreg[t - 3][j];
        };
        constexpr int ND = 4 + 5 - NPUT;
        g2_dma(0); g2_dma(1); g2_dma(2); g2_dma(3); g2_put(3);
        waitv<8 + ND>(); WAITL0(); bar();
        rsrc_t ry = mkrsrc(p.ws + WS_YT, D * S * 2);
        auto load_y = [&]() {
            u32x4 yv[3];
#pragma unroll
            for (int i = 0; i < 3; ++i) {
                const int it = w + 4 * i, ch = 8 * it + (lane >> 3);
                yv[i] = __builtin_amdgcn_raw_buffer_load_b128(ry, it < 10 ? ((80 * ds + ch) * S + 64 * st + (lane & 7) * 8) * 2 : 0xF0000000u, 0, 16);
            }
#pragma unroll
            for (int i = 0; i < 3; ++i) {
                const int it = w + 4 * i, ch = 8 * it + (lane >> 3);
                if (it < 10) *(LAS u32x4*)(lds + G2_Y + ch * 128 + (lane & 7) * 16) = yv[i];
            }
        };
        waitv<4 + ND>(); bar();
        waitv<2 * ND - ND>(); bar(); g2_dma(4); g2_put(4);
        waitv<ND>(); WAITL0(); bar(); g2_dma(5); g2_put(5);
        waitv<ND>(); WAITL0(); bar(); g2_dma(6); g2_put(6);
        waitv<ND>(); WAITL0(); bar(); g2_dma(7); g2_put(7);
        waitv<ND>(); WAITL0(); bar(); g2_dma(8); g2_put(8);
        waitv<ND>(); WAITL0(); bar(); g2_dma(9); g2_put(9);
        waitv<ND>(); WAITL0(); bar(); g2_dma(10); g2_put(10);
        waitv<ND>(); WAITL0(); bar(); g2_dma(11); g2_put(11);
        u32x4 yf = {0, 0, 0, 0};
        if (w == 0) {
            rsrc_t rfl_ = mkrsrc(p.ws + WS_FLAG, 32768);
            asm volatile("buffer_load_dwordx4 %0, %1, %2, 0 offen sc1" : "=v"(yf) : "v"((FL_Y + ds * 64 + (lane < 5 ? lane : 0) * 4) * 4), "s"(rfl_) : "memory");
            waitv<ND + 1>();
        } else {
            waitv<ND>();
        }
        WAITL0(); bar();
        WAITV(0);
        if (w == 0) {
            TIE(yf);
            if (__all(yf[0] == tag && yf[1] == tag && yf[2] == tag && yf[3] == tag) && lane == 0) lds_set(lds, L_CNT + 12, 1u);
        }
        WAITL0(); bar();
        const bool yearly = rfl(*(volatile LAS unsigned*)(lds + L_CNT + 12)) == 1u;
        if (yearly) load_y();
        bar();
        if (!yearly) {
            if (w == 0) { poll4<5>(p.ws, FL_Y + ds * 64, tag, lane); if (lane == 0) lds_set(lds, L_CNT + 12, 2u); }
            else lds_wait(lds, L_CNT + 12, 2u);
            load_y();
        }
        bar();
        WAITL0();
        bar();
        STAMPT(8);
    } else {
        const unsigned tag = rfl(*(const GAS unsigned*)(p.ws + WS_FLAG)) + 1u;
        {
            const int wsx = wave & 1, wf = wave >> 1;
            const int g = lane >> 4, fr = lane & 15, q = (lane & 15) >> 2, pp = lane & 3;
            int oa[3];
#pragma unroll
            for (int i = 0; i < 3; ++i) {
                const int ft = 3 * wf + i, c = ft >> 1, half = ft & 1;
                oa[i] = 16384 + (g * 3 + c) * 512 + q * 64 + 16 * ((2 * half + (pp >> 1)) ^ (2 * (g & 1))) + 8 * (pp & 1);
            }
            const int ob0 = (64 * wsx + fr) * 128 + ((g ^ (fr >> 1)) * 16);
            const int ob1 = (64 * wsx + fr) * 128 + ((g ^ (fr >> 1) ^ 4) * 16);
            f32x4 accg[3][4], accu[3][4];
#pragma unroll
            for (int i = 0; i < 3; ++i)
#pragma unroll
                for (int m = 0; m < 4; ++m) { accg[i][m] = (f32x4){0, 0, 0, 0}; accu[i][m] = (f32x4){0, 0, 0, 0}; }
            struct Fr { s16x8 a[6]; s16x8 b[4]; } fa, fb;
            auto rd = [&](Fr& f, int ks, int sb) {
#pragma unroll
                for (int m = 0; m < 4; ++m) f.b[m] = *(LAS s16x8*)(lds + sb + (ks ? ob1 : ob0) + m * 2048);
#pragma unroll
                for (int i = 0; i < 3; ++i) {
                    f.a[i] = cat(trread(lds + sb + oa[i] + ks * 6144), trread(lds + sb + oa[i] + ks * 6144 + 256));
                    f.a[3 + i] = cat(trread(lds + sb + oa[i] + ks * 6144 + 12288), trread(lds + sb + oa[i] + ks * 6144 + 256 + 12288));
                }
            };
            auto mm = [&](const Fr& f) {
#pragma unroll
                for (int i = 0; i < 3; ++i)
#pragma unroll
                    for (int m = 0; m < 4; ++m) {
                        accg[i][m] = mfma16(f.a[i], f.b[m], accg[i][m]);
                        accu[i][m] = mfma16(f.a[3 + i], f.b[m], accu[i][m]);
                    }
            };
            auto interleave = [&]() {
#pragma unroll
                for (int i = 0; i < 16; ++i) { SGB(0x008, 1); SGB(0x100, 1); }
                SGB(0x008, 8);
                SB0();
            };
            unsigned dummy = 0;
            rsrc_t rpf = mkrsrc(wave == 0 ? p.q : (wave == 1 ? p.wg : p.wu), wave == 0 ? S * D * 2 : D * F * 2);
            const unsigned pfo = wave == 0 ? (lane < 32 ? (8 * b + (lane >> 2)) * 1280 + (2 + (lane & 3)) * 128 : 0xF0000000u)
                                           : (lane < 24 && wave < 3 ? (128 + b) * 3072 + lane * 128 : 0xF0000000u);
            const unsigned pfs = wave == 0 ? 512 : 256 * 3072;
            __builtin_amdgcn_s_sleep(SLEEP0);
            prefetch_lines(dummy, rpf, pfo);
            __builtin_amdgcn_s_setprio(1);
            bar();
            STAMPT(3);
#define RSTAMP1() do {} while (0)
            rd(fa, 0, 0);
            int sb = 0;
            for (int t = 0; t < 10; ++t) {
                rd(fb, 1, sb); mm(fa); interleave();
                WAITL0();
                RSTAMP1();
                bar();
                RSTAMP1();
                if (t == 1) prefetch_lines(dummy, rpf, pfo == 0xF0000000u ? pfo : pfo + pfs);
                sb = (sb == 2 * G1_STAGE) ? 0 : sb + G1_STAGE;
                if (t < 9) rd(fa, 0, sb);
                mm(fb); interleave();
            }
            STAMPT(4);
            WAITV(0); TIE(dummy);
            rsrc_t rh = mkrsrc(p.ws + WS_H, S * F * 2);
#pragma unroll
            for (int mp = 0; mp < 2; ++mp)
#pragma unroll
                for (int i = 0; i < 3; ++i) {
                    float hv[8];
#pragma unroll
                    for (int e = 0; e < 8; ++e) hv[e] = accu[i][2 * mp + (e >> 2)][e & 3] * sigmoidf_fast(accg[i][2 * mp + (e >> 2)][e & 3]);
                    const u32x4 o = {cvtpk(hv[0], hv[1]), cvtpk(hv[2], hv[3]), cvtpk(hv[4], hv[5]), cvtpk(hv[6], hv[7])};
                    const int sp = bs * 4 + wsx * 2 + mp, ft = bf * 6 + wf * 3 + i;
                    __builtin_amdgcn_raw_buffer_store_b128(o, rh, ((sp * 96 + ft) * 64 + lane) * 16, 0, 16);
                }
        }
        STAMPT(5);
        auto publish_h = [&]() {
            WAITV(0);
            unsigned old = 0;
            if (lane == 0) old = __hip_atomic_fetch_add((LAS unsigned*)(lds + L_CNT), 1u, __ATOMIC_RELAXED, __HIP_MEMORY_SCOPE_WORKGROUP);
            if (lane == 0 && old == 3) st_flag(p.ws, FL_H + bs * 64 + bf, tag);
        };
        bar();
        STAMPT(6);
        f32x4 cacc[8];
        u32x2 ckv[8];
        const int ca = lane & 15, cg = lane >> 4;
        if (has_conv) {
            LAS char* slot = lds + wave * CV_SLOT;
#pragma unroll
            for (int n = 0; n < 8; ++n) cacc[n] = (f32x4){0, 0, 0, 0};
            unsigned pg = (unsigned)(size_t)slot + 480 + 32 * ca + 16 * cg;
            unsigned pf = (unsigned)(size_t)slot + CV_KF + (3 - (ca & 3)) * CV_COPY + 472 + 16 * cg - 8 * (ca >> 2);
            struct Res { s16x8 G[8]; s16x4 Fl[8], Fh[8]; } RA, RB;
            auto rd1 = [&](Res& R, int k) {
                const int j = k / 3, m = k % 3;
                if (m == 0) asm volatile("ds_read_b64 %0, %1 offset:%2" : "=v"(R.Fl[j]) : "v"(pf), "n"(512 * (7 - j)));
                else if (m == 1) asm volatile("ds_read_b64 %0, %1 offset:%2" : "=v"(R.Fh[j]) : "v"(pf), "n"(512 * (7 - j) + 8));
                else asm volatile("ds_read_b128 %0, %1 offset:%2" : "=v"(R.G[j]) : "v"(pg), "n"(512 * j));
            };
            auto land = [&](Res& R) {
                asm volatile("s_waitcnt lgkmcnt(0)"
                             : "+v"(R.G[0]), "+v"(R.G[1]), "+v"(R.G[2]), "+v"(R.G[3]), "+v"(R.G[4]), "+v"(R.G[5]), "+v"(R.G[6]), "+v"(R.G[7]),
                               "+v"(R.Fl[0]), "+v"(R.Fl[1]), "+v"(R.Fl[2]), "+v"(R.Fl[3]), "+v"(R.Fl[4]), "+v"(R.Fl[5]), "+v"(R.Fl[6]), "+v"(R.Fl[7]));
                asm volatile(""
                             : "+v"(R.Fh[0]), "+v"(R.Fh[1]), "+v"(R.Fh[2]), "+v"(R.Fh[3]), "+v"(R.Fh[4]), "+v"(R.Fh[5]), "+v"(R.Fh[6]), "+v"(R.Fh[7]));
                SB0();
            };
            auto residue = [&](Res& R, Res& N) {
                pg -= 64; pf -= 64;
                int k = 0;
#pragma unroll
                for (int I = 0; I < 8; ++I) {
                    const s16x8 Fv = cat(R.Fl[I], R.Fh[I]);
#pragma unroll
                    for (int n = I; n < 8; ++n) {
                        cacc[n] = mfma16(Fv, R.G[n - I], cacc[n]);
                        if (k < 24) rd1(N, k);
                        ++k;
                        SB0();
                    }
                }
                land(N);
            };
#pragma unroll
            for (int k = 0; k < 24; ++k) rd1(RA, k);
            land(RA);
            for (int r2 = 0; r2 < 4; ++r2) {
                residue(RA, RB);
                residue(RB, RA);
                if (r2 == 0) publish_h();
            }
#pragma unroll
            for (int n = 0; n < 8; ++n) ckv[n] = *(LAS u32x2*)(slot + CV_KF + 2 * (2044 - 256 * n - 16 * ca - 4 * cg));
        } else {
            publish_h();
        }
        STAMPT(7);
        {
            const int sp = wave & 1, kh = wave >> 1;
            WAITL0();
            bar();
            STAMPT(8);
            SB0();
            if (has_conv) {
                const int d = cb * 4 + wave;
                const float Dd = bflo(p.sd[d]);
                rsrc_t ry = mkrsrc(p.ws + WS_YT, D * S * 2);
                u32x2 o[8];
#pragma unroll
                for (int n = 0; n < 8; ++n) {
                    o[n][0] = cvtpk(cacc[n][0] + bfhi(ckv[n][1]) * Dd, cacc[n][1] + bflo(ckv[n][1]) * Dd);
                    o[n][1] = cvtpk(cacc[n][2] + bfhi(ckv[n][0]) * Dd, cacc[n][3] + bflo(ckv[n][0]) * Dd);
                }
#pragma unroll
                for (int n = 0; n < 8; n += 2) {
                    auto x = __builtin_amdgcn_permlane16_swap(o[n][0], o[n + 1][0], false, false);
                    auto y = __builtin_amdgcn_permlane16_swap(o[n][1], o[n + 1][1], false, false);
                    const u32x4 val = {x[0], y[0], x[1], y[1]};
                    const int s = 256 * (n + (cg & 1)) + 16 * ca + 4 * (cg & 2);
                    __builtin_amdgcn_raw_buffer_store_b128(val, ry, (d * S + s) * 2, 0, 16);
                }
            }
            SB0();
            STAMPT(9);
            const int s16 = lane & 15, g = lane >> 4, q = s16 >> 2, pp = lane & 3;
            const int srow = 64 * st + 32 * sp + 16 * kh + s16;
            const float gt = __hip_atomic_load((const GAS float*)(p.ws + WS_G) + srow, RLX_AGENT);
            u32x2 qv[5];
#pragma unroll
            for (int n = 0; n < 5; ++n) qv[n] = *(const u32x2*)(p.q + (size_t)srow * D + 80 * ds + 16 * n + 4 * g);
            f32x4 acc[2][5];
#pragma unroll
            for (int n = 0; n < 5; ++n) { acc[0][n] = (f32x4){0, 0, 0, 0}; acc[1][n] = (f32x4){0, 0, 0, 0}; }
            const unsigned pw = ldsb + kh * 10240 + lane * 16, ph = ldsb + wave * 4096 + lane * 16;
            struct Fr2 { s16x8 w[5]; u32x4 h[2]; } fa, fb;
            auto rd2 = [&](Fr2& f, int s4, int kk) {
                LAS char* bh = (LAS char*)(size_t)(ph + s4 * G2_HST + kk * 2048);
                LAS char* bw = (LAS char*)(size_t)(pw + (s4 < 3 ? G2_RING + s4 * G2_STAGE : G2_W3) + kk * 5120);
                f.h[0] = *(LAS u32x4*)bh; f.h[1] = *(LAS u32x4*)(bh + 1024);
#pragma unroll
                for (int n = 0; n < 5; ++n) f.w[n] = *(LAS s16x8*)(bw + n * 1024);
            };
            auto swp = [&](Fr2& f) {
                unsigned a2 = f.h[0][2], a3 = f.h[0][3], b0 = f.h[1][0], b1 = f.h[1][1];
                asm volatile("v_swap_b32 %0, %1\n\tv_swap_b32 %2, %3\n\ts_nop 1" : "+v"(a2), "+v"(b0), "+v"(a3), "+v"(b1));
                f.h[0][2] = a2; f.h[0][3] = a3; f.h[1][0] = b0; f.h[1][1] = b1;
            };
            auto step = [&](const Fr2& f) {
                const s16x8 h0 = __builtin_bit_cast(s16x8, f.h[0]), h1 = __builtin_bit_cast(s16x8, f.h[1]);
#pragma unroll
                for (int n = 0; n < 5; ++n) { acc[0][n] = mfma16(f.w[n], h0, acc[0][n]); acc[1][n] = mfma16(f.w[n], h1, acc[1][n]); }
            };
            auto il2 = [&]() {
#pragma unroll
                for (int i = 0; i < 7; ++i) { SGB(0x008, 1); SGB(0x100, 1); }
                SGB(0x008, 3);
                SB0();
            };
            STAMPT(1);
            bar();
            STAMPT(2);
#define RSTAMP() do {} while (0)
            rd2(fa, 0, 0);
            for (int t = 0; t < 12; ++t) {
                swp(fa);
                rd2(fb, t & 3, 1);
                step(fa);
                il2();
                if (t == 4) WAITV(0);
                RSTAMP();
                bar();
                RSTAMP();
                if (t == 4 && has_conv && wave == 0 && lane == 0) st_flag(p.ws, FL_Y + (cb / 20) * 64 + cb % 20, tag);
                swp(fb);
                rd2(fa, (t + 1) & 3, 0);
                step(fb);
                il2();
            }
            WAITL0();
            bar();
            STAMPT(10);
#pragma unroll
            for (int n = 0; n < 5; ++n) *(LAS f32x4*)(lds + G2_RED + (wave * 5 + n) * 1024 + lane * 16) = kh ? acc[0][n] : acc[1][n];
            WAITL0();
            bar();
            STAMPT(11);
#pragma unroll
            for (int n = 0; n < 5; ++n) {
                const f32x4 o4 = (kh ? acc[1][n] : acc[0][n]) + *(LAS f32x4*)(lds + G2_RED + ((wave ^ 2) * 5 + n) * 1024 + lane * 16);
                const u32x2 yv = __builtin_bit_cast(u32x2, trread(lds + G2_Y + (16 * n + 4 * g + q) * 128 + (32 * sp + 16 * kh + 4 * pp) * 2));
                const float o0 = gt * (bflo(qv[n][0]) * bflo(yv[0])) + (1.f - gt) * o4[0];
                const float o1 = gt * (bfhi(qv[n][0]) * bfhi(yv[0])) + (1.f - gt) * o4[1];
                const float o2 = gt * (bflo(qv[n][1]) * bflo(yv[1])) + (1.f - gt) * o4[2];
                const float o3 = gt * (bfhi(qv[n][1]) * bfhi(yv[1])) + (1.f - gt) * o4[3];
                *(u32x2*)(p.out + (size_t)srow * D + 80 * ds + 16 * n + 4 * g) = (u32x2){cvtpk(o0, o1), cvtpk(o2, o3)};
            }
            STAMPT(12);
        }
    }
}

extern "C" void kernel_launch(void* const* d_in, const int* in_sizes, int n_in,
                              void* d_out, int out_size, void* d_ws, size_t ws_size,
                              hipStream_t stream) {
    Params p;
    p.q = (const u16*)d_in[0]; p.k = (const u16*)d_in[1]; p.v = (const u16*)d_in[2];
    p.sk = (const u16*)d_in[3]; p.ssk = (const u16*)d_in[4]; p.sd = (const u16*)d_in[5];
    p.wg = (const u16*)d_in[7]; p.wu = (const u16*)d_in[8]; p.wd = (const u16*)d_in[9]; p.gw = (const u16*)d_in[10];
    p.out = (u16*)d_out; p.ws = (char*)d_ws;
    hipFuncSetAttribute((const void*)k_fused, hipFuncAttributeMaxDynamicSharedMemorySize, LDS_BYTES);
    k_fused<<<256, 512, LDS_BYTES, stream>>>(p);
}
```

```cpp
#include <hip/hip_runtime.h>
#include <hip/hip_bf16.h>
#include <stdint.h>

typedef unsigned short u16;
typedef short s16x4 __attribute__((ext_vector_type(4)));
typedef short s16x8 __attribute__((ext_vector_type(8)));
typedef __bf16 b16x8 __attribute__((ext_vector_type(8)));
typedef __bf16 b16x2 __attribute__((ext_vector_type(2)));
typedef float f32x2 __attribute__((ext_vector_type(2)));
typedef float f32x4 __attribute__((ext_vector_type(4)));
typedef unsigned u32x2 __attribute__((ext_vector_type(2)));
typedef unsigned u32x4 __attribute__((ext_vector_type(4)));
#define LAS __attribute__((address_space(3)))
#define GAS __attribute__((address_space(1)))
#define DI __device__ __forceinline__

static constexpr int S = 2048, D = 640, F = 1536;
static constexpr int KRS = 2112;
static constexpr unsigned WS_H = 0, WS_KR0 = 8u << 20, WS_KR1 = 11u << 20, WS_YT = 14u << 20, WS_G = 17u << 20, WS_WT = 18u << 20,
                          WS_STAMP = 20u << 20, WS_FLAG = 24u << 20;
static constexpr unsigned FL_EPOCH = 0, FL_KR = 1024, FL_WT = 2048, FL_H = 4096, FL_Y = 6144;

struct Params {
    const u16 *q, *k, *v, *sk, *ssk, *sd, *wg, *wu, *wd, *gw;
    u16* out; char* ws;
};

DI float bflo(unsigned x) { return __uint_as_float(x << 16); }
DI float bfhi(unsigned x) { return __uint_as_float(x & 0xffff0000u); }
DI unsigned cvtpk(float lo, float hi) { f32x2 v = {lo, hi}; b16x2 b = __builtin_convertvector(v, b16x2); return __builtin_bit_cast(unsigned, b); }
DI unsigned rfl(unsigned x) { return __builtin_amdgcn_readfirstlane(x); }
DI float sigmoidf_fast(float x) { return __builtin_amdgcn_rcpf(1.f + __builtin_amdgcn_exp2f(x * -1.44269504089f)); }
DI f32x4 mfma16(s16x8 a, s16x8 b, f32x4 c) {
    return __builtin_amdgcn_mfma_f32_16x16x32_bf16(__builtin_bit_cast(b16x8, a), __builtin_bit_cast(b16x8, b), c, 0, 0, 0);
}
DI s16x4 trread(LAS char* p) { return __builtin_amdgcn_ds_read_tr16_b64_v4i16((LAS s16x4*)p); }
DI s16x8 cat(s16x4 a, s16x4 b) { return __builtin_shufflevector(a, b, 0, 1, 2, 3, 4, 5, 6, 7); }
typedef __amdgpu_buffer_rsrc_t rsrc_t;
DI rsrc_t mkrsrc(const void* p, unsigned bytes) { return __builtin_amdgcn_make_buffer_rsrc((void*)p, 0, bytes, 0x00020000); }
DI void dma16(unsigned m0v, unsigned voff, rsrc_t r, unsigned soff) {
    asm volatile("s_mov_b32 m0, %0\n\ts_nop 0\n\tbuffer_load_dwordx4 %1, %2, %3 offen lds" ::"s"(m0v), "v"(voff), "s"(r), "s"(soff) : "m0", "memory");
}
DI void dma16_sc1(unsigned m0v, unsigned voff, rsrc_t r, unsigned soff) {
    asm volatile("s_mov_b32 m0, %0\n\ts_nop 0\n\tbuffer_load_dwordx4 %1, %2, %3 offen sc1 lds" ::"s"(m0v), "v"(voff), "s"(r), "s"(soff) : "m0", "memory");
}
DI u32x4 aload(rsrc_t r, unsigned voff) { u32x4 x; asm volatile("buffer_load_dwordx4 %0, %1, %2, 0 offen" : "=v"(x) : "v"(voff), "s"(r) : "memory"); return x; }
DI void prefetch_lines(unsigned& dummy, rsrc_t r, unsigned voff) { asm volatile("buffer_load_dword %0, %1, %2, 0 offen" : "+v"(dummy) : "v"(voff), "s"(r) : "memory"); }
template <int N> DI void waitv() { asm volatile("s_waitcnt vmcnt(%0)" ::"n"(N) : "memory"); }
DI float dot2(unsigned a, unsigned b, float c) { return __builtin_amdgcn_fdot2_f32_bf16(__builtin_bit_cast(b16x2, a), __builtin_bit_cast(b16x2, b), c, false); }
template <int CTRL, int RM> DI float dpp_add(float x) {
    return x + __int_as_float(__builtin_amdgcn_update_dpp(0, __float_as_int(x), CTRL, RM, 0xf, true));
}
DI float wave_sum(float x) {
    x = dpp_add<0x111, 0xf>(x); x = dpp_add<0x112, 0xf>(x); x = dpp_add<0x114, 0xf>(x); x = dpp_add<0x118, 0xf>(x);
    x = dpp_add<0x142, 0xa>(x); x = dpp_add<0x143, 0xc>(x);
    return __int_as_float(__builtin_amdgcn_readlane(__float_as_int(x), 63));
}
#define TIE(x) asm volatile("" : "+v"(x))
#define WAITV(n) asm volatile("s_waitcnt vmcnt(" #n ")" ::: "memory")
#define WAITL0() asm volatile("s_waitcnt lgkmcnt(0)" ::: "memory")
DI void bar() { asm volatile("" ::: "memory"); __builtin_amdgcn_s_barrier(); asm volatile("" ::: "memory"); }
#define SGB(m, n) __builtin_amdgcn_sched_group_barrier(m, n, 0)
#define SB0() __builtin_amdgcn_sched_barrier(0)
#define HOFF(i) (i)
#define HW 24
#define WK 6
#define LPRIO 2
#define NPUT 0
#define TP0 3
#define SLEEP0 8
#define RLX_AGENT __ATOMIC_RELAXED, __HIP_MEMORY_SCOPE_AGENT

DI void st_flag(char* ws, unsigned word, unsigned tag) { __hip_atomic_store((GAS unsigned*)(ws + WS_FLAG) + word, tag, RLX_AGENT); }
template <int NW4>
DI bool poll4_once(char* ws, unsigned word, unsigned tag, int lane) {
    rsrc_t r = mkrsrc(ws + WS_FLAG, 32768);
    u32x4 x;
    asm volatile("buffer_load_dwordx4 %0, %1, %2, 0 offen sc1\n\ts_waitcnt vmcnt(0)" : "=&v"(x) : "v"((word + (lane < NW4 ? lane : 0) * 4) * 4), "s"(r) : "memory");
    return __all(x[0] == tag && x[1] == tag && x[2] == tag && x[3] == tag);
}
template <int NW4>
DI void poll4(char* ws, unsigned word, unsigned tag, int lane) {
    for (int spins = 0; spins < (1 << 18); ++spins) {
        if (poll4_once<NW4>(ws, word, tag, lane)) return;
        __builtin_amdgcn_s_sleep(4);
    }
}
DI void lds_set(LAS char* lds, int off, unsigned v) { __hip_atomic_store((LAS unsigned*)(lds + off), v, __ATOMIC_RELAXED, __HIP_MEMORY_SCOPE_WORKGROUP); }
DI void lds_wait(LAS char* lds, int off, unsigned v) {
    for (int spins = 0; spins < (1 << 22); ++spins) {
        if (rfl(__hip_atomic_load((LAS unsigned*)(lds + off), __ATOMIC_RELAXED, __HIP_MEMORY_SCOPE_WORKGROUP)) == v) break;
        __builtin_amdgcn_s_sleep(1);
    }
    asm volatile("" ::: "memory");
}

#define STAMPC(i) do {} while (0)
#define STAMPT(i) do {} while (0)

constexpr int G1_STAGE = 40960;
constexpr int P0_LDS = 122880;
constexpr int P0_T = P0_LDS, P0_W = P0_LDS + 128 * 272, P0_GW = P0_W + 1024;
constexpr int TP = 272;
constexpr int CV_SLOT = 21248, CV_COPY = 4160, CV_KF = 4608;
constexpr int G2_RING = 86016, G2_STAGE = 20480;
constexpr int G2_Y = G2_RING + 3 * G2_STAGE;
constexpr int G2_HST = 16384;
constexpr int G2_W3 = 65536;
constexpr int G2_RED = 0;
constexpr int L_CNT = 163776;
constexpr int LDS_BYTES = 163840;

struct Ctx {
    Params p; LAS char* lds; unsigned ldsb; int lane, wave, b, xcd, idx, cb, pb, bs, bf; unsigned tag;
};

struct PrepRegs { u32x4 a[16]; };
template <bool KV> DI void prep_load(const Ctx& c, PrepRegs& R) {
    const Params& p = c.p;
    const int lane = c.lane, wave = c.wave;
    if (KV) {
        const int kdc = c.pb % 5, s0 = (c.pb / 5) * 128, d0 = kdc * 128, tid = wave * 64 + lane, c16 = tid & 15, s = s0 + 4 * (tid >> 4);
        rsrc_t rk = mkrsrc(p.k, S * D * 2), rv = mkrsrc(p.v, S * D * 2), rsk = mkrsrc(p.sk, D * 8);
        if (wave == 0) dma16(c.ldsb + P0_W, lane * 16, rsk, d0 * 8);
#pragma unroll
        for (int i = 0; i < 7; ++i) R.a[i] = aload(rk, (unsigned)(((s - 3 + i) * D + d0 + 8 * c16) * 2));
#pragma unroll
        for (int i = 0; i < 4; ++i) R.a[7 + i] = aload(rv, ((s + i) * D + d0 + 8 * c16) * 2);
    } else {
        rsrc_t rwd = mkrsrc(p.wd, F * D * 2);
#pragma unroll
        for (int it = 0; it < 4; ++it) {
            const int ul = wave + 8 * it, unit = (c.pb - 80) * 30 + ul, T = unit / 10, dl = unit % 10;
#pragma unroll
            for (int i = 0; i < 4; ++i)
                R.a[4 * it + i] = aload(rwd, ul < 30 ? ((32 * T + 8 * i + (lane >> 3)) * D + 64 * dl + (lane & 7) * 8) * 2 : 0xF0000000u);
        }
    }
}
template <bool KV> DI void prep_run(const Ctx& c, PrepRegs& R) {
    const Params& p = c.p;
    const int lane = c.lane, wave = c.wave;
    LAS char* lds = c.lds;
    if (KV) {
        const int kdc = c.pb % 5, s0 = (c.pb / 5) * 128, d0 = kdc * 128, tid = wave * 64 + lane, c16 = tid & 15, rg = tid >> 4;
#pragma unroll
        for (int i = 0; i < 11; ++i) TIE(R.a[i]);
        WAITL0(); bar();
        u32x4 w4[4];
#pragma unroll
        for (int i = 0; i < 4; ++i) w4[i] = *(LAS u32x4*)(lds + P0_W + c16 * 64 + i * 16);
        LAS char* tw = lds + P0_T + 8 * c16 * TP + (((15 - (rg >> 1)) ^ c16) * 16) + ((rg & 1) ? 0 : 8);
#pragma unroll
        for (int e = 0; e < 8; ++e) {
            const unsigned sel = (e & 1) ? 0x07060302u : 0x05040100u;
            const unsigned wa = w4[e >> 1][(e & 1) * 2], wb = w4[e >> 1][(e & 1) * 2 + 1];
            unsigned pr[6];
#pragma unroll
            for (int i = 0; i < 6; ++i) pr[i] = __builtin_amdgcn_perm(R.a[i][e >> 1], R.a[i + 1][e >> 1], sel);
            float o[4];
#pragma unroll
            for (int r = 0; r < 4; ++r) o[r] = dot2(pr[r + 2], wa, dot2(pr[r], wb, 0.f)) * ((e & 1) ? bfhi(R.a[7 + r][e >> 1]) : bflo(R.a[7 + r][e >> 1]));
            *(LAS u32x2*)(tw + e * TP) = (u32x2){cvtpk(o[3], o[2]), cvtpk(o[1], o[0])};
        }
        WAITL0(); bar();
        rsrc_t r0 = mkrsrc(p.ws + WS_KR0, D * KRS * 2);
#pragma unroll
        for (int it = 0; it < 4; ++it) {
            const int item = tid + it * 512, ch = item >> 4, j = item & 15;
            __builtin_amdgcn_raw_buffer_store_b128(*(LAS u32x4*)(lds + P0_T + ch * TP + ((j ^ ((ch >> 3) & 15)) * 16)), r0, ((d0 + ch) * KRS + (1920 - s0) + 8 * j) * 2, 0, 16);
        }
    } else {
        const int g4 = lane >> 4, q = (lane & 15) >> 2, pp = lane & 3, row = 4 * g4 + q;
        rsrc_t rw = mkrsrc(p.ws + WS_WT, F * D * 2);
        LAS char* buf = lds + P0_LDS + wave * 4096;
#pragma unroll
        for (int it = 0; it < 4; ++it) {
            const int ul = wave + 8 * it, unit = (c.pb - 80) * 30 + ul, Tk = unit / 10, dl = unit % 10;
            const int kh = Tk / 24, tt = (Tk % 24) >> 1, kk = Tk & 1;
#pragma unroll
            for (int i = 0; i < 4; ++i) { TIE(R.a[4 * it + i]); *(LAS u32x4*)(buf + (8 * i + (lane >> 3)) * 128 + (lane & 7) * 16) = R.a[4 * it + i]; }
#pragma unroll
            for (int i = 0; i < 4; ++i) {
                const int d16 = 4 * dl + i, dsl = d16 / 5, n = d16 % 5;
                const s16x8 f = cat(trread(buf + row * 128 + 32 * i + 8 * pp), trread(buf + (row + 16) * 128 + 32 * i + 8 * pp));
                __builtin_amdgcn_raw_buffer_store_b128(__builtin_bit_cast(u32x4, f), rw, ul < 30 ? ((((dsl * 12 + tt) * 4 + kh * 2 + kk) * 5 + n) * 64 + lane) * 16 : 0xF0000000u, 0, 16);
            }
        }
    }
}
DI void prep_publish(const Ctx& c) {
    if (c.pb < 80) { if (c.lane == 0) st_flag(c.p.ws, FL_KR + (c.pb % 5) * 64 + c.pb / 5, c.tag); }
    else if (c.lane < 8) st_flag(c.p.ws, FL_WT + c.lane * 128 + c.pb - 80, c.tag);
}

template <int MODE>
DI void loader_gemm1(const Ctx& c) {
    const Params& p = c.p;
    const int lane = c.lane, w = c.wave - 4, wave = c.wave;
    (void)wave;
    LAS char* lds = c.lds;
    rsrc_t rq = mkrsrc(p.q, S * D * 2), rg_ = mkrsrc(p.wg, D * F * 2), ru_ = mkrsrc(p.wu, D * F * 2);
    unsigned vx[2], vw[3];
    {
        const int r = lane >> 3, slot = lane & 7;
        vx[0] = r * 1280 + ((slot ^ (r >> 1)) * 16);
        vx[1] = r * 1280 + ((slot ^ (r >> 1) ^ 4) * 16);
#pragma unroll
        for (int j = 0; j < 3; ++j) {
            const int L = 2 * j + (lane >> 5), kbl = L / 3, cc = L % 3, row = (lane & 31) >> 2, ch = (lane & 3) ^ (2 * kbl);
            vw[j] = (kbl * 8 + row) * 3072 + cc * 64 + ch * 16;
        }
    }
    const unsigned sx0 = (c.bs * 128 + 32 * w) * 1280, sw0 = 16 * w * 3072 + c.bf * 192;
    auto issue = [&](int t) {
        const unsigned base = c.ldsb + (t >= 9 ? t - 9 : (t >= 6 ? t - 6 : (t >= 3 ? t - 3 : t))) * G1_STAGE;
#pragma unroll
        for (int i = 0; i < 4; ++i) dma16(base + (32 * w + 8 * i) * 128, vx[i & 1], rq, sx0 + i * 8 * 1280 + t * 128);
#pragma unroll
        for (int j = 0; j < 3; ++j) dma16(base + 16384 + w * 3072 + j * 1024, vw[j], rg_, sw0 + t * 64 * 3072);
#pragma unroll
        for (int j = 0; j < 3; ++j) dma16(base + 28672 + w * 3072 + j * 1024, vw[j], ru_, sw0 + t * 64 * 3072);
    };
    constexpr int NX = MODE == 0 ? 1 : (MODE == 1 ? 4 : 16);
    PrepRegs R;
    if (MODE == 1) prep_load<true>(c, R);
    if (MODE == 2) prep_load<false>(c, R);
    if (w < 2) {
        rsrc_t rgw = mkrsrc(p.gw, D * 2);
        if (w == 0 || lane < 16) dma16(c.ldsb + P0_GW + w * 1024, (w * 64 + lane) * 16, rgw, 0);
    }
    if (w == 3 && lane < 8) *(LAS unsigned*)(lds + L_CNT + lane * 4) = 0;
    issue(0); issue(1);
    unsigned dummy = 0;
    if (MODE == 0) {
        rsrc_t rf = mkrsrc(p.ssk, D * S * 2);
        prefetch_lines(dummy, rf, lane < 32 ? (c.cb * 4 + w) * (S * 2) + lane * 128 : 0xF0000000u);
    } else {
        waitv<20>();
        prep_run<MODE == 1>(c, R);
    }
    waitv<10 + NX>(); WAITL0(); bar();
    STAMPT(3);
    issue(2);
    const int grw = 8 * c.bf + (lane >> 3), gch = (lane & 7) ^ ((grw >> 1) & 7);
    float gacc = 0.f;
    auto gate_step = [&](int t) {
        const int buf = t >= 9 ? t - 9 : (t >= 6 ? t - 6 : (t >= 3 ? t - 3 : t));
        const u32x4 xv = *(LAS u32x4*)(lds + buf * G1_STAGE + grw * 128 + (lane & 7) * 16);
        const u32x4 gv = *(LAS u32x4*)(lds + P0_GW + t * 128 + gch * 16);
#pragma unroll
        for (int i = 0; i < 4; ++i) gacc = dot2(xv[i], gv[i], gacc);
    };
    if (w == 3) gate_step(0);
    for (int t = 0; t < 10; ++t) {
        if (t == 0) waitv<NX + 10>();
        else if (t <= 7) waitv<10>();
        else waitv<0>();
        WAITL0();
        bar();
        if (t + 3 <= 9) issue(t + 3);
        if (w == 3 && t < 9) gate_step(t + 1);
        if (MODE != 0 && t == 1 && w == 0) prep_publish(c);
    }
    TIE(dummy);
    if (w == 3) {
        gacc = dpp_add<0x111, 0xf>(gacc); gacc = dpp_add<0x112, 0xf>(gacc); gacc = dpp_add<0x114, 0xf>(gacc);
        rsrc_t rG = mkrsrc(p.ws + WS_G, S * 4);
        __builtin_amdgcn_raw_buffer_store_b32(__float_as_uint(sigmoidf_fast(gacc)), rG, (lane & 7) == 7 ? (c.bs * 128 + grw) * 4 : 0xF0000000u, 0, 16);
    }
}

__global__ __launch_bounds__(512, 2) void k_fused(Params p) {
    extern __shared__ __attribute__((aligned(16))) char smem[];
    LAS char* lds = (LAS char*)smem;
    const unsigned ldsb = (unsigned)(size_t)lds;
    const int wave = rfl(threadIdx.x >> 6), lane = threadIdx.x & 63;
    const int b = blockIdx.x, xcd = b & 7, idx = b >> 3;
    const bool has_conv = idx < 20;
    const int cb = xcd * 20 + idx, pb = xcd * 12 + idx - 20;
    const int bs = (xcd & 1) * 8 + (idx & 7), bf = (xcd >> 1) * 4 + (idx >> 3);
    const int st = xcd * 4 + (idx & 3), ds = idx >> 2;
    STAMPT(0);
    Ctx c; c.p = p; c.lds = lds; c.ldsb = ldsb; c.lane = lane; c.wave = wave; c.b = b; c.xcd = xcd; c.idx = idx; c.cb = cb; c.pb = pb; c.bs = bs; c.bf = bf;
    c.tag = rfl(*(const GAS unsigned*)(p.ws + WS_FLAG)) + 1u;
    const unsigned tag = c.tag;

    if (wave >= 4) {
        const int w = wave - 4;
        if (has_conv) loader_gemm1<0>(c);
        else if (pb < 80) loader_gemm1<1>(c);
        else loader_gemm1<2>(c);
        STAMPT(4);
        const unsigned vo = lane * 16;
        const int d = cb * 4 + w;
        const unsigned sl = ldsb + w * CV_SLOT;
        if (has_conv) {
            rsrc_t rf = mkrsrc(p.ssk, D * S * 2);
#pragma unroll
            for (int i = 0; i < 4; ++i) dma16(sl + 512 + i * 1024, vo, rf, d * (S * 2) + i * 1024);
            LAS char* slot = lds + w * CV_SLOT;
            if (lane < 32) *(LAS u32x4*)(slot + lane * 16) = (u32x4){0, 0, 0, 0};
            else if (lane < 48) *(LAS u32x4*)(slot + CV_KF + ((lane - 32) >> 2) * CV_COPY + 4096 + (lane & 3) * 16) = (u32x4){0, 0, 0, 0};
        }
        if (w == 0) {
            if (has_conv) poll4<4>(p.ws, FL_KR + (cb >> 5) * 64, tag, lane);
            poll4<4>(p.ws, FL_WT + xcd * 128, tag, lane);
            if (lane == 0) lds_set(lds, L_CNT + 8, 1u);
        } else {
            lds_wait(lds, L_CNT + 8, 1u);
        }
        STAMPT(5);
        if (has_conv) {
            rsrc_t r0 = mkrsrc(p.ws + WS_KR0, D * KRS * 2);
#pragma unroll
            for (int i = 0; i < 4; ++i) dma16_sc1(sl + CV_KF + i * 1024, vo, r0, d * (KRS * 2) + i * 1024);
        }
        rsrc_t rw = mkrsrc(p.ws + WS_WT, F * D * 2);
        const unsigned sw2 = ds * (48 * 5 * 1024) + w * 5120;
        const int rot = (3 * ds) >> 1;
        auto g2_src = [&](int t) { const int ts = t + rot; return sw2 + (ts >= 12 ? ts - 12 : ts) * G2_STAGE; };
#pragma unroll
        for (int t = 0; t < 3; ++t)
#pragma unroll
            for (int j = 0; j < 5; ++j) dma16_sc1(ldsb + G2_RING + t * G2_STAGE + w * 5120 + j * 1024, vo, rw, g2_src(t) + j * 1024);
        waitv<15>();
        if (has_conv) {
            WAITL0();
            LAS char* c0 = lds + w * CV_SLOT + CV_KF;
#pragma unroll
            for (int j = 0; j < 4; ++j) {
                const u32x4 a = *(LAS u32x4*)(c0 + j * 1024 + lane * 16);
                const u32x2 nx = *(LAS u32x2*)(c0 + j * 1024 + lane * 16 + 16);
                *(LAS u32x4*)(c0 + CV_COPY + j * 1024 + lane * 16) =
                    (u32x4){__builtin_amdgcn_alignbit(a[1], a[0], 16), __builtin_amdgcn_alignbit(a[2], a[1], 16), __builtin_amdgcn_alignbit(a[3], a[2], 16), __builtin_amdgcn_alignbit(nx[0], a[3], 16)};
                *(LAS u32x4*)(c0 + 2 * CV_COPY + j * 1024 + lane * 16) = (u32x4){a[1], a[2], a[3], nx[0]};
                *(LAS u32x4*)(c0 + 3 * CV_COPY + j * 1024 + lane * 16) =
                    (u32x4){__builtin_amdgcn_alignbit(a[2], a[1], 16), __builtin_amdgcn_alignbit(a[3], a[2], 16), __builtin_amdgcn_alignbit(nx[0], a[3], 16), __builtin_amdgcn_alignbit(nx[1], nx[0], 16)};
            }
        }
        WAITL0();
        bar();
        STAMPT(6);
        u32x4 wreg[9][NPUT + 1];
#pragma unroll
        for (int t = 0; t < 9; ++t)
#pragma unroll
            for (int j = 0; j < NPUT; ++j) wreg[t][j] = __builtin_amdgcn_raw_buffer_load_b128(rw, vo, g2_src(t + 3) + j * 1024, 16);
        unsigned dummy = 0;
        if (NPUT < 5) {
#pragma unroll
            for (int i = 0; i < 2; ++i) {
                const int ln = (idx & 3) * 480 + (w * 2 + i) * 64 + lane;
                prefetch_lines(dummy, rw, (w * 2 + i) * 64 + lane < 480 ? ds * (48 * 5 * 1024) + ln * 128 : 0xF0000000u);
            }
        }
        if (w == 0) poll4<4>(p.ws, FL_H + (st >> 1) * 64, tag, lane);
        WAITV(0);
#pragma unroll
        for (int t = 0; t < 9; ++t)
#pragma unroll
            for (int j = 0; j < NPUT; ++j) TIE(wreg[t][j]);
        TIE(dummy);
        bar();
        STAMPT(7);
        rsrc_t rh = mkrsrc(p.ws + WS_H, S * F * 2);
        const unsigned sh2 = ((st * 2 + (w & 1)) * 96 + (w >> 1) * 48) * 1024;
        auto wslot = [&](int t) { return (t & 3) < 3 ? G2_RING + (t & 3) * G2_STAGE : G2_W3; };
        auto g2_dma = [&](int t) {
            const int ts = t + rot;
            const unsigned src = sh2 + (ts >= 12 ? ts - 12 : ts) * 4096, dst = ldsb + (t & 3) * G2_HST + w * 4096;
#pragma unroll
            for (int j = 0; j < 4; ++j) dma16_sc1(dst + j * 1024, vo, rh, src + j * 1024);
            if (t >= 3) {
#pragma unroll
                for (int j = NPUT; j < 5; ++j) dma16_sc1(ldsb + wslot(t) + w * 5120 + j * 1024, vo, rw, g2_src(t) + j * 1024);
            }
        };
        auto g2_put = [&](int t) {
            LAS char* base = lds + wslot(t) + w * 5120 + lane * 16;
#pragma unroll
            for (int j = 0; j < NPUT; ++j) *(LAS u32x4*)(base + j * 1024) = wreg[t - 3][j];
        };
        constexpr int ND = 4 + 5 - NPUT;
        g2_dma(0); g2_dma(1); g2_dma(2); g2_dma(3); g2_put(3);
        waitv<8 + ND>(); WAITL0(); bar();
        rsrc_t ry = mkrsrc(p.ws + WS_YT, D * S * 2);
        auto load_y = [&]() {
            u32x4 yv[3];
#pragma unroll
            for (int i = 0; i < 3; ++i) {
                const int it = w + 4 * i, ch = 8 * it + (lane >> 3);
                yv[i] = __builtin_amdgcn_raw_buffer_load_b128(ry, it < 10 ? ((80 * ds + ch) * S + 64 * st + (lane & 7) * 8) * 2 : 0xF0000000u, 0, 16);
            }
#pragma unroll
            for (int i = 0; i < 3; ++i) {
                const int it = w + 4 * i, ch = 8 * it + (lane >> 3);
                if (it < 10) *(LAS u32x4*)(lds + G2_Y + ch * 128 + (lane & 7) * 16) = yv[i];
            }
        };
        waitv<4 + ND>(); bar();
        waitv<2 * ND - ND>(); bar(); g2_dma(4); g2_put(4);
        waitv<ND>(); WAITL0(); bar(); g2_dma(5); g2_put(5);
        waitv<ND>(); WAITL0(); bar(); g2_dma(6); g2_put(6);
        waitv<ND>(); WAITL0(); bar(); g2_dma(7); g2_put(7);
        waitv<ND>(); WAITL0(); bar(); g2_dma(8); g2_put(8);
        waitv<ND>(); WAITL0(); bar(); g2_dma(9); g2_put(9);
        waitv<ND>(); WAITL0(); bar(); g2_dma(10); g2_put(10);
        waitv<ND>(); WAITL0(); bar(); g2_dma(11); g2_put(11);
        u32x4 yf = {0, 0, 0, 0};
        if (w == 0) {
            rsrc_t rfl_ = mkrsrc(p.ws + WS_FLAG, 32768);
            asm volatile("buffer_load_dwordx4 %0, %1, %2, 0 offen sc1" : "=v"(yf) : "v"((FL_Y + ds * 64 + (lane < 5 ? lane : 0) * 4) * 4), "s"(rfl_) : "memory");
            waitv<ND + 1>();
        } else {
            waitv<ND>();
        }
        WAITL0(); bar();
        WAITV(0);
        if (w == 0) {
            TIE(yf);
            if (__all(yf[0] == tag && yf[1] == tag && yf[2] == tag && yf[3] == tag) && lane == 0) lds_set(lds, L_CNT + 12, 1u);
        }
        WAITL0(); bar();
        const bool yearly = rfl(*(volatile LAS unsigned*)(lds + L_CNT + 12)) == 1u;
        if (yearly) load_y();
        bar();
        if (!yearly) {
            if (w == 0) { poll4<5>(p.ws, FL_Y + ds * 64, tag, lane); if (lane == 0) lds_set(lds, L_CNT + 12, 2u); }
            else lds_wait(lds, L_CNT + 12, 2u);
            load_y();
        }
        bar();
        WAITL0();
        bar();
        if (b == 0 && w == 0) {
            rsrc_t rfl_ = mkrsrc(p.ws + WS_FLAG, 32768);
            const unsigned fo = (FL_H + (lane >> 2) * 64 + (lane & 3) * 4) * 4;
            for (int spins = 0; spins < (1 << 18); ++spins) {
                u32x4 x;
                asm volatile("buffer_load_dwordx4 %0, %1, %2, 0 offen sc1\n\ts_waitcnt vmcnt(0)" : "=&v"(x) : "v"(fo), "s"(rfl_) : "memory");
                if (__all(x[0] == tag && x[1] == tag && x[2] == tag && x[3] == tag)) break;
                __builtin_amdgcn_s_sleep(4);
            }
            if (lane == 0) st_flag(p.ws, FL_EPOCH, tag);
        }
        STAMPT(8);
    } else {
        float Dd = bflo(p.sd[(has_conv ? cb * 4 : 0) + wave]);
        asm volatile("" : "+v"(Dd));
        {
            const int wsx = wave & 1, wf = wave >> 1;
            const int g = lane >> 4, fr = lane & 15, q = (lane & 15) >> 2, pp = lane & 3;
            int oa[3];
#pragma unroll
            for (int i = 0; i < 3; ++i) {
                const int ft = 3 * wf + i, c = ft >> 1, half = ft & 1;
                oa[i] = 16384 + (g * 3 + c) * 512 + q * 64 + 16 * ((2 * half + (pp >> 1)) ^ (2 * (g & 1))) + 8 * (pp & 1);
            }
            const int ob0 = (64 * wsx + fr) * 128 + ((g ^ (fr >> 1)) * 16);
            const int ob1 = (64 * wsx + fr) * 128 + ((g ^ (fr >> 1) ^ 4) * 16);
            f32x4 accg[3][4], accu[3][4];
#pragma unroll
            for (int i = 0; i < 3; ++i)
#pragma unroll
                for (int m = 0; m < 4; ++m) { accg[i][m] = (f32x4){0, 0, 0, 0}; accu[i][m] = (f32x4){0, 0, 0, 0}; }
            struct Fr { s16x8 a[6]; s16x8 b[4]; } fa, fb;
            auto rd = [&](Fr& f, int ks, int sb) {
#pragma unroll
                for (int m = 0; m < 4; ++m) f.b[m] = *(LAS s16x8*)(lds + sb + (ks ? ob1 : ob0) + m * 2048);
#pragma unroll
                for (int i = 0; i < 3; ++i) {
                    f.a[i] = cat(trread(lds + sb + oa[i] + ks * 6144), trread(lds + sb + oa[i] + ks * 6144 + 256));
                    f.a[3 + i] = cat(trread(lds + sb + oa[i] + ks * 6144 + 12288), trread(lds + sb + oa[i] + ks * 6144 + 256 + 12288));
                }
            };
            auto mm = [&](const Fr& f) {
#pragma unroll
                for (int i = 0; i < 3; ++i)
#pragma unroll
                    for (int m = 0; m < 4; ++m) {
                        accg[i][m] = mfma16(f.a[i], f.b[m], accg[i][m]);
                        accu[i][m] = mfma16(f.a[3 + i], f.b[m], accu[i][m]);
                    }
            };
            auto interleave = [&]() {
#pragma unroll
                for (int i = 0; i < 16; ++i) { SGB(0x008, 1); SGB(0x100, 1); }
                SGB(0x008, 8);
                SB0();
            };
            unsigned dummy = 0;
            if (!has_conv) {
                PrepRegs R;
                if (pb < 80) { prep_load<true>(c, R); WAITV(0); prep_run<true>(c, R); }
                else { prep_load<false>(c, R); WAITV(0); prep_run<false>(c, R); }
            }
            {
                rsrc_t rpf = mkrsrc(wave == 0 ? p.q : (wave == 1 ? p.wg : p.wu), wave == 0 ? S * D * 2 : D * F * 2);
                const unsigned pfo = wave == 0 ? (8 * b + (lane >> 3)) * 1280 + (2 + (lane & 7)) * 128
                                               : (lane < 48 && wave < 3 ? (128 + b + (lane >= 24 ? 256 : 0)) * 3072 + (lane >= 24 ? lane - 24 : lane) * 128 : 0xF0000000u);
                if (has_conv) __builtin_amdgcn_s_sleep(SLEEP0);
                prefetch_lines(dummy, rpf, pfo);
            }
            __builtin_amdgcn_s_setprio(1);
            bar();
            STAMPT(3);
#define RSTAMP1() do {} while (0)
            rd(fa, 0, 0);
            int sb = 0;
            for (int t = 0; t < 10; ++t) {
                rd(fb, 1, sb); mm(fa); interleave();
                WAITL0();
                if (t == 1 && !has_conv) WAITV(0);
                RSTAMP1();
                bar();
                RSTAMP1();
                sb = (sb == 2 * G1_STAGE) ? 0 : sb + G1_STAGE;
                if (t < 9) rd(fa, 0, sb);
                mm(fb); interleave();
            }
            STAMPT(4);
            WAITV(0); TIE(dummy);
            rsrc_t rh = mkrsrc(p.ws + WS_H, S * F * 2);
#pragma unroll
            for (int mp = 0; mp < 2; ++mp)
#pragma unroll
                for (int i = 0; i < 3; ++i) {
                    float hv[8];
#pragma unroll
                    for (int e = 0; e < 8; ++e) hv[e] = accu[i][2 * mp + (e >> 2)][e & 3] * sigmoidf_fast(accg[i][2 * mp + (e >> 2)][e & 3]);
                    const u32x4 o = {cvtpk(hv[0], hv[1]), cvtpk(hv[2], hv[3]), cvtpk(hv[4], hv[5]), cvtpk(hv[6], hv[7])};
                    const int sp = bs * 4 + wsx * 2 + mp, ft = bf * 6 + wf * 3 + i;
                    __builtin_amdgcn_raw_buffer_store_b128(o, rh, ((sp * 96 + ft) * 64 + lane) * 16, 0, 16);
                }
        }
        STAMPT(5);
        auto publish_h = [&]() {
            WAITV(0);
            unsigned old = 0;
            if (lane == 0) old = __hip_atomic_fetch_add((LAS unsigned*)(lds + L_CNT), 1u, __ATOMIC_RELAXED, __HIP_MEMORY_SCOPE_WORKGROUP);
            if (lane == 0 && old == 3) st_flag(p.ws, FL_H + bs * 64 + bf, tag);
        };
        bar();
        STAMPT(6);
        f32x4 cacc[8];
        u32x2 ckv[8];
        const int ca = lane & 15, cg = lane >> 4;
        if (has_conv) {
            LAS char* slot = lds + wave * CV_SLOT;
#pragma unroll
            for (int n = 0; n < 8; ++n) cacc[n] = (f32x4){0, 0, 0, 0};
            unsigned pg = (unsigned)(size_t)slot + 480 + 32 * ca + 16 * cg;
            unsigned pf = (unsigned)(size_t)slot + CV_KF + (3 - (ca & 3)) * CV_COPY + 472 + 16 * cg - 8 * (ca >> 2);
            struct Res { s16x8 G[8]; s16x4 Fl[8], Fh[8]; } RA, RB;
            auto rd1 = [&](Res& R, int k) {
                const int j = k / 3, m = k % 3;
                if (m == 0) asm volatile("ds_read_b64 %0, %1 offset:%2" : "=v"(R.Fl[j]) : "v"(pf), "n"(512 * (7 - j)));
                else if (m == 1) asm volatile("ds_read_b64 %0, %1 offset:%2" : "=v"(R.Fh[j]) : "v"(pf), "n"(512 * (7 - j) + 8));
                else asm volatile("ds_read_b128 %0, %1 offset:%2" : "=v"(R.G[j]) : "v"(pg), "n"(512 * j));
            };
            auto land = [&](Res& R) {
                asm volatile("s_waitcnt lgkmcnt(0)"
                             : "+v"(R.G[0]), "+v"(R.G[1]), "+v"(R.G[2]), "+v"(R.G[3]), "+v"(R.G[4]), "+v"(R.G[5]), "+v"(R.G[6]), "+v"(R.G[7]),
                               "+v"(R.Fl[0]), "+v"(R.Fl[1]), "+v"(R.Fl[2]), "+v"(R.Fl[3]), "+v"(R.Fl[4]), "+v"(R.Fl[5]), "+v"(R.Fl[6]), "+v"(R.Fl[7]));
                asm volatile(""
                             : "+v"(R.Fh[0]), "+v"(R.Fh[1]), "+v"(R.Fh[2]), "+v"(R.Fh[3]), "+v"(R.Fh[4]), "+v"(R.Fh[5]), "+v"(R.Fh[6]), "+v"(R.Fh[7]));
                SB0();
            };
            auto residue = [&](Res& R, Res& N) {
                pg -= 64; pf -= 64;
                int k = 0;
#pragma unroll
                for (int I = 0; I < 8; ++I) {
                    const s16x8 Fv = cat(R.Fl[I], R.Fh[I]);
#pragma unroll
                    for (int n = I; n < 8; ++n) {
                        cacc[n] = mfma16(Fv, R.G[n - I], cacc[n]);
                        if (k < 24) rd1(N, k);
                        ++k;
                        SB0();
                    }
                }
                land(N);
            };
#pragma unroll
            for (int k = 0; k < 24; ++k) rd1(RA, k);
            land(RA);
            for (int r2 = 0; r2 < 4; ++r2) {
                residue(RA, RB);
                residue(RB, RA);
                if (r2 == 0) publish_h();
            }
#pragma unroll
            for (int n = 0; n < 8; ++n) ckv[n] = *(LAS u32x2*)(slot + CV_KF + 2 * (2044 - 256 * n - 16 * ca - 4 * cg));
        } else {
            publish_h();
        }
        STAMPT(7);
        {
            const int sp = wave & 1, kh = wave >> 1;
            WAITL0();
            bar();
            STAMPT(8);
            SB0();
            if (has_conv) {
                const int d = cb * 4 + wave;
                rsrc_t ry = mkrsrc(p.ws + WS_YT, D * S * 2);
                u32x2 o[8];
#pragma unroll
                for (int n = 0; n < 8; ++n) {
                    o[n][0] = cvtpk(cacc[n][0] + bfhi(ckv[n][1]) * Dd, cacc[n][1] + bflo(ckv[n][1]) * Dd);
                    o[n][1] = cvtpk(cacc[n][2] + bfhi(ckv[n][0]) * Dd, cacc[n][3] + bflo(ckv[n][0]) * Dd);
                }
#pragma unroll
                for (int n = 0; n < 8; n += 2) {
                    auto x = __builtin_amdgcn_permlane16_swap(o[n][0], o[n + 1][0], false, false);
                    auto y = __builtin_amdgcn_permlane16_swap(o[n][1], o[n + 1][1], false, false);
                    const u32x4 val = {x[0], y[0], x[1], y[1]};
                    const int s = 256 * (n + (cg & 1)) + 16 * ca + 4 * (cg & 2);
                    __builtin_amdgcn_raw_buffer_store_b128(val, ry, (d * S + s) * 2, 0, 16);
                }
            }
            SB0();
            STAMPT(9);
            const int s16 = lane & 15, g = lane >> 4, q = s16 >> 2, pp = lane & 3;
            const int srow = 64 * st + 32 * sp + 16 * kh + s16;
            const float gt = __hip_atomic_load((const GAS float*)(p.ws + WS_G) + srow, RLX_AGENT);
            u32x2 qv[5];
#pragma unroll
            for (int n = 0; n < 5; ++n) qv[n] = *(const u32x2*)(p.q + (size_t)srow * D + 80 * ds + 16 * n + 4 * g);
            f32x4 acc[2][5];
#pragma unroll
            for (int n = 0; n < 5; ++n) { acc[0][n] = (f32x4){0, 0, 0, 0}; acc[1][n] = (f32x4){0, 0, 0, 0}; }
            const unsigned pw = ldsb + kh * 10240 + lane * 16, ph = ldsb + wave * 4096 + lane * 16;
            struct Fr2 { s16x8 w[5]; u32x4 h[2]; } fa, fb;
            auto rd2 = [&](Fr2& f, int s4, int kk) {
                LAS char* bh = (LAS char*)(size_t)(ph + s4 * G2_HST + kk * 2048);
                LAS char* bw = (LAS char*)(size_t)(pw + (s4 < 3 ? G2_RING + s4 * G2_STAGE : G2_W3) + kk * 5120);
                f.h[0] = *(LAS u32x4*)bh; f.h[1] = *(LAS u32x4*)(bh + 1024);
#pragma unroll
                for (int n = 0; n < 5; ++n) f.w[n] = *(LAS s16x8*)(bw + n * 1024);
            };
            auto swp = [&](Fr2& f) {
                unsigned a2 = f.h[0][2], a3 = f.h[0][3], b0 = f.h[1][0], b1 = f.h[1][1];
                asm volatile("v_swap_b32 %0, %1\n\tv_swap_b32 %2, %3\n\ts_nop 1" : "+v"(a2), "+v"(b0), "+v"(a3), "+v"(b1));
                f.h[0][2] = a2; f.h[0][3] = a3; f.h[1][0] = b0; f.h[1][1] = b1;
            };
            auto step = [&](const Fr2& f) {
                const s16x8 h0 = __builtin_bit_cast(s16x8, f.h[0]), h1 = __builtin_bit_cast(s16x8, f.h[1]);
#pragma unroll
                for (int n = 0; n < 5; ++n) { acc[0][n] = mfma16(f.w[n], h0, acc[0][n]); acc[1][n] = mfma16(f.w[n], h1, acc[1][n]); }
            };
            auto il2 = [&]() {
#pragma unroll
                for (int i = 0; i < 7; ++i) { SGB(0x008, 1); SGB(0x100, 1); }
                SGB(0x008, 3);
                SB0();
            };
            STAMPT(1);
            bar();
            STAMPT(2);
#define RSTAMP() do {} while (0)
            rd2(fa, 0, 0);
            swp(fa);
            for (int t = 0; t < 12; ++t) {
                rd2(fb, t & 3, 1);
                step(fa);
                il2();
                swp(fb);
                SB0();
                if (t == 4) WAITV(0);
                RSTAMP();
                bar();
                RSTAMP();
                if (t == 4 && has_conv && wave == 0 && lane == 0) st_flag(p.ws, FL_Y + (cb / 20) * 64 + cb % 20, tag);
                rd2(fa, (t + 1) & 3, 0);
                step(fb);
                il2();
                swp(fa);
                SB0();
            }
            WAITL0();
            bar();
            STAMPT(10);
#pragma unroll
            for (int n = 0; n < 5; ++n) *(LAS f32x4*)(lds + G2_RED + (wave * 5 + n) * 1024 + lane * 16) = kh ? acc[0][n] : acc[1][n];
            WAITL0();
            bar();
            STAMPT(11);
#pragma unroll
            for (int n = 0; n < 5; ++n) {
                const f32x4 o4 = (kh ? acc[1][n] : acc[0][n]) + *(LAS f32x4*)(lds + G2_RED + ((wave ^ 2) * 5 + n) * 1024 + lane * 16);
                const u32x2 yv = __builtin_bit_cast(u32x2, trread(lds + G2_Y + (16 * n + 4 * g + q) * 128 + (32 * sp + 16 * kh + 4 * pp) * 2));
                const float o0 = gt * (bflo(qv[n][0]) * bflo(yv[0])) + (1.f - gt) * o4[0];
                const float o1 = gt * (bfhi(qv[n][0]) * bfhi(yv[0])) + (1.f - gt) * o4[1];
                const float o2 = gt * (bflo(qv[n][1]) * bflo(yv[1])) + (1.f - gt) * o4[2];
                const float o3 = gt * (bfhi(qv[n][1]) * bfhi(yv[1])) + (1.f - gt) * o4[3];
                *(u32x2*)(p.out + (size_t)srow * D + 80 * ds + 16 * n + 4 * g) = (u32x2){cvtpk(o0, o1), cvtpk(o2, o3)};
            }
            STAMPT(12);
        }
    }
}

extern "C" void kernel_launch(void* const* d_in, const int* in_sizes, int n_in,
                              void* d_out, int out_size, void* d_ws, size_t ws_size,
                              hipStream_t stream) {
    Params p;
    p.q = (const u16*)d_in[0]; p.k = (const u16*)d_in[1]; p.v = (const u16*)d_in[2];
    p.sk = (const u16*)d_in[3]; p.ssk = (const u16*)d_in[4]; p.sd = (const u16*)d_in[5];
    p.wg = (const u16*)d_in[7]; p.wu = (const u16*)d_in[8]; p.wd = (const u16*)d_in[9]; p.gw = (const u16*)d_in[10];
    p.out = (u16*)d_out; p.ws = (char*)d_ws;
    hipFuncSetAttribute((const void*)k_fused, hipFuncAttributeMaxDynamicSharedMemorySize, LDS_BYTES);
    k_fused<<<256, 512, LDS_BYTES, stream>>>(p);
}
```

```cpp
#include <hip/hip_runtime.h>
#include <hip/hip_bf16.h>
#include <stdint.h>

typedef unsigned short u16;
typedef short s16x4 __attribute__((ext_vector_type(4)));
typedef short s16x8 __attribute__((ext_vector_type(8)));
typedef __bf16 b16x8 __attribute__((ext_vector_type(8)));
typedef __bf16 b16x2 __attribute__((ext_vector_type(2)));
typedef float f32x2 __attribute__((ext_vector_type(2)));
typedef float f32x4 __attribute__((ext_vector_type(4)));
typedef unsigned u32x2 __attribute__((ext_vector_type(2)));
typedef unsigned u32x4 __attribute__((ext_vector_type(4)));
#define LAS __attribute__((address_space(3)))
#define GAS __attribute__((address_space(1)))
#define DI __device__ __forceinline__

static constexpr int S = 2048, D = 640, F = 1536;
static constexpr int KRS = 2112;
static constexpr unsigned WS_H = 0, WS_KR0 = 8u << 20, WS_KR1 = 11u << 20, WS_YT = 14u << 20, WS_G = 17u << 20, WS_WT = 18u << 20,
                          WS_STAMP = 20u << 20, WS_FLAG = 24u << 20;
static constexpr unsigned FL_EPOCH = 0, FL_KR = 1024, FL_WT = 2048, FL_H = 4096, FL_Y = 6144;

struct Params {
    const u16 *q, *k, *v, *sk, *ssk, *sd, *wg, *wu, *wd, *gw;
    u16* out; char* ws;
};

DI float bflo(unsigned x) { return __uint_as_float(x << 16); }
DI float bfhi(unsigned x) { return __uint_as_float(x & 0xffff0000u); }
DI unsigned cvtpk(float lo, float hi) { f32x2 v = {lo, hi}; b16x2 b = __builtin_convertvector(v, b16x2); return __builtin_bit_cast(unsigned, b); }
DI unsigned rfl(unsigned x) { return __builtin_amdgcn_readfirstlane(x); }
DI float sigmoidf_fast(float x) { return __builtin_amdgcn_rcpf(1.f + __builtin_amdgcn_exp2f(x * -1.44269504089f)); }
DI f32x4 mfma16(s16x8 a, s16x8 b, f32x4 c) {
    return __builtin_amdgcn_mfma_f32_16x16x32_bf16(__builtin_bit_cast(b16x8, a), __builtin_bit_cast(b16x8, b), c, 0, 0, 0);
}
DI s16x4 trread(LAS char* p) { return __builtin_amdgcn_ds_read_tr16_b64_v4i16((LAS s16x4*)p); }
DI s16x8 cat(s16x4 a, s16x4 b) { return __builtin_shufflevector(a, b, 0, 1, 2, 3, 4, 5, 6, 7); }
typedef __amdgpu_buffer_rsrc_t rsrc_t;
DI rsrc_t mkrsrc(const void* p, unsigned bytes) { return __builtin_amdgcn_make_buffer_rsrc((void*)p, 0, bytes, 0x00020000); }
DI void dma16(unsigned m0v, unsigned voff, rsrc_t r, unsigned soff) {
    asm volatile("s_mov_b32 m0, %0\n\ts_nop 0\n\tbuffer_load_dwordx4 %1, %2, %3 offen lds" ::"s"(m0v), "v"(voff), "s"(r), "s"(soff) : "m0", "memory");
}
DI void dma16_sc1(unsigned m0v, unsigned voff, rsrc_t r, unsigned soff) {
    asm volatile("s_mov_b32 m0, %0\n\ts_nop 0\n\tbuffer_load_dwordx4 %1, %2, %3 offen sc1 lds" ::"s"(m0v), "v"(voff), "s"(r), "s"(soff) : "m0", "memory");
}
DI u32x4 aload(rsrc_t r, unsigned voff) { u32x4 x; asm volatile("buffer_load_dwordx4 %0, %1, %2, 0 offen" : "=v"(x) : "v"(voff), "s"(r) : "memory"); return x; }
DI void prefetch_lines(unsigned& dummy, rsrc_t r, unsigned voff) { asm volatile("buffer_load_dword %0, %1, %2, 0 offen" : "+v"(dummy) : "v"(voff), "s"(r) : "memory"); }
template <int N> DI void waitv() { asm volatile("s_waitcnt vmcnt(%0)" ::"n"(N) : "memory"); }
DI float dot2(unsigned a, unsigned b, float c) { return __builtin_amdgcn_fdot2_f32_bf16(__builtin_bit_cast(b16x2, a), __builtin_bit_cast(b16x2, b), c, false); }
template <int CTRL, int RM> DI float dpp_add(float x) {
    return x + __int_as_float(__builtin_amdgcn_update_dpp(0, __float_as_int(x), CTRL, RM, 0xf, true));
}
DI float wave_sum(float x) {
    x = dpp_add<0x111, 0xf>(x); x = dpp_add<0x112, 0xf>(x); x = dpp_add<0x114, 0xf>(x); x = dpp_add<0x118, 0xf>(x);
    x = dpp_add<0x142, 0xa>(x); x = dpp_add<0x143, 0xc>(x);
    return __int_as_float(__builtin_amdgcn_readlane(__float_as_int(x), 63));
}
#define TIE(x) asm volatile("" : "+v"(x))
#define WAITV(n) asm volatile("s_waitcnt vmcnt(" #n ")" ::: "memory")
#define WAITL0() asm volatile("s_waitcnt lgkmcnt(0)" ::: "memory")
DI void bar() { asm volatile("" ::: "memory"); __builtin_amdgcn_s_barrier(); asm volatile("" ::: "memory"); }
#define SGB(m, n) __builtin_amdgcn_sched_group_barrier(m, n, 0)
#define SB0() __builtin_amdgcn_sched_barrier(0)
#define HOFF(i) (i)
#define HW 24
#define WK 6
#define LPRIO 2
#define NPUT 0
#define TP0 3
#define SLEEP0 8
#define RLX_AGENT __ATOMIC_RELAXED, __HIP_MEMORY_SCOPE_AGENT

DI void st_flag(char* ws, unsigned word, unsigned tag) { __hip_atomic_store((GAS unsigned*)(ws + WS_FLAG) + word, tag, RLX_AGENT); }
template <int NW4>
DI bool poll4_once(char* ws, unsigned word, unsigned tag, int lane) {
    rsrc_t r = mkrsrc(ws + WS_FLAG, 32768);
    u32x4 x;
    asm volatile("buffer_load_dwordx4 %0, %1, %2, 0 offen sc1\n\ts_waitcnt vmcnt(0)" : "=&v"(x) : "v"((word + (lane < NW4 ? lane : 0) * 4) * 4), "s"(r) : "memory");
    return __all(x[0] == tag && x[1] == tag && x[2] == tag && x[3] == tag);
}
template <int NW4>
DI void poll4(char* ws, unsigned word, unsigned tag, int lane) {
    for (int spins = 0; spins < (1 << 18); ++spins) {
        if (poll4_once<NW4>(ws, word, tag, lane)) return;
        __builtin_amdgcn_s_sleep(4);
    }
}
DI void lds_set(LAS char* lds, int off, unsigned v) { __hip_atomic_store((LAS unsigned*)(lds + off), v, __ATOMIC_RELAXED, __HIP_MEMORY_SCOPE_WORKGROUP); }
DI void lds_wait(LAS char* lds, int off, unsigned v) {
    for (int spins = 0; spins < (1 << 22); ++spins) {
        if (rfl(__hip_atomic_load((LAS unsigned*)(lds + off), __ATOMIC_RELAXED, __HIP_MEMORY_SCOPE_WORKGROUP)) == v) break;
        __builtin_amdgcn_s_sleep(1);
    }
    asm volatile("" ::: "memory");
}

#define STAMPC(i) do {} while (0)
#define STAMPT(i) do {} while (0)

constexpr int G1_STAGE = 40960;
constexpr int P0_LDS = 122880;
constexpr int P0_T = P0_LDS, P0_W = P0_LDS + 128 * 272, P0_GW = P0_W + 1024;
constexpr int TP = 272;
constexpr int CV_SLOT = 21248, CV_COPY = 4160, CV_KF = 4608;
constexpr int G2_RING = 86016, G2_STAGE = 20480;
constexpr int G2_Y = G2_RING + 3 * G2_STAGE;
constexpr int G2_HST = 16384;
constexpr int G2_W3 = 65536;
constexpr int G2_RED = 0;
constexpr int L_CNT = 163776;
constexpr int LDS_BYTES = 163840;

struct Ctx {
    Params p; LAS char* lds; unsigned ldsb; int lane, wave, b, xcd, idx, cb, pb, bs, bf; unsigned tag;
};

struct PrepRegs { u32x4 a[16]; };
template <bool KV> DI void prep_load(const Ctx& c, PrepRegs& R) {
    const Params& p = c.p;
    const int lane = c.lane, wave = c.wave;
    if (KV) {
        const int kdc = c.pb % 5, s0 = (c.pb / 5) * 128, d0 = kdc * 128, tid = wave * 64 + lane, c16 = tid & 15, s = s0 + 4 * (tid >> 4);
        rsrc_t rk = mkrsrc(p.k, S * D * 2), rv = mkrsrc(p.v, S * D * 2), rsk = mkrsrc(p.sk, D * 8);
        if (wave == 0) dma16(c.ldsb + P0_W, lane * 16, rsk, d0 * 8);
#pragma unroll
        for (int i = 0; i < 7; ++i) R.a[i] = aload(rk, (unsigned)(((s - 3 + i) * D + d0 + 8 * c16) * 2));
#pragma unroll
        for (int i = 0; i < 4; ++i) R.a[7 + i] = aload(rv, ((s + i) * D + d0 + 8 * c16) * 2);
    } else {
        rsrc_t rwd = mkrsrc(p.wd, F * D * 2);
#pragma unroll
        for (int it = 0; it < 4; ++it) {
            const int ul = wave + 8 * it, unit = (c.pb - 80) * 30 + ul, T = unit / 10, dl = unit % 10;
#pragma unroll
            for (int i = 0; i < 4; ++i)
                R.a[4 * it + i] = aload(rwd, ul < 30 ? ((32 * T + 8 * i + (lane >> 3)) * D + 64 * dl + (lane & 7) * 8) * 2 : 0xF0000000u);
        }
    }
}
template <bool KV> DI void prep_run(const Ctx& c, PrepRegs& R) {
    const Params& p = c.p;
    const int lane = c.lane, wave = c.wave;
    LAS char* lds = c.lds;
    if (KV) {
        const int kdc = c.pb % 5, s0 = (c.pb / 5) * 128, d0 = kdc * 128, tid = wave * 64 + lane, c16 = tid & 15, rg = tid >> 4;
#pragma unroll
        for (int i = 0; i < 11; ++i) TIE(R.a[i]);
        WAITL0(); bar();
        u32x4 w4[4];
#pragma unroll
        for (int i = 0; i < 4; ++i) w4[i] = *(LAS u32x4*)(lds + P0_W + c16 * 64 + i * 16);
        LAS char* tw = lds + P0_T + 8 * c16 * TP + (((15 - (rg >> 1)) ^ c16) * 16) + ((rg & 1) ? 0 : 8);
#pragma unroll
        for (int e = 0; e < 8; ++e) {
            const unsigned sel = (e & 1) ? 0x07060302u : 0x05040100u;
            const unsigned wa = w4[e >> 1][(e & 1) * 2], wb = w4[e >> 1][(e & 1) * 2 + 1];
            unsigned pr[6];
#pragma unroll
            for (int i = 0; i < 6; ++i) pr[i] = __builtin_amdgcn_perm(R.a[i][e >> 1], R.a[i + 1][e >> 1], sel);
            float o[4];
#pragma unroll
            for (int r = 0; r < 4; ++r) o[r] = dot2(pr[r + 2], wa, dot2(pr[r], wb, 0.f)) * ((e & 1) ? bfhi(R.a[7 + r][e >> 1]) : bflo(R.a[7 + r][e >> 1]));
            *(LAS u32x2*)(tw + e * TP) = (u32x2){cvtpk(o[3], o[2]), cvtpk(o[1], o[0])};
        }
        WAITL0(); bar();
        rsrc_t r0 = mkrsrc(p.ws + WS_KR0, D * KRS * 2);
#pragma unroll
        for (int it = 0; it < 4; ++it) {
            const int item = tid + it * 512, ch = item >> 4, j = item & 15;
            __builtin_amdgcn_raw_buffer_store_b128(*(LAS u32x4*)(lds + P0_T + ch * TP + ((j ^ ((ch >> 3) & 15)) * 16)), r0, ((d0 + ch) * KRS + (1920 - s0) + 8 * j) * 2, 0, 16);
        }
    } else {
        const int g4 = lane >> 4, q = (lane & 15) >> 2, pp = lane & 3, row = 4 * g4 + q;
        rsrc_t rw = mkrsrc(p.ws + WS_WT, F * D * 2);
        LAS char* buf = lds + P0_LDS + wave * 4096;
#pragma unroll
        for (int it = 0; it < 4; ++it) {
            const int ul = wave + 8 * it, unit = (c.pb - 80) * 30 + ul, Tk = unit / 10, dl = unit % 10;
            const int kh = Tk / 24, tt = (Tk % 24) >> 1, kk = Tk & 1;
#pragma unroll
            for (int i = 0; i < 4; ++i) { TIE(R.a[4 * it + i]); *(LAS u32x4*)(buf + (8 * i + (lane >> 3)) * 128 + (lane & 7) * 16) = R.a[4 * it + i]; }
#pragma unroll
            for (int i = 0; i < 4; ++i) {
                const int d16 = 4 * dl + i, dsl = d16 / 5, n = d16 % 5;
                const s16x8 f = cat(trread(buf + row * 128 + 32 * i + 8 * pp), trread(buf + (row + 16) * 128 + 32 * i + 8 * pp));
                __builtin_amdgcn_raw_buffer_store_b128(__builtin_bit_cast(u32x4, f), rw, ul < 30 ? ((((dsl * 12 + tt) * 4 + kh * 2 + kk) * 5 + n) * 64 + lane) * 16 : 0xF0000000u, 0, 16);
            }
        }
    }
}
DI void prep_publish(const Ctx& c) {
    if (c.pb < 80) { if (c.lane == 0) st_flag(c.p.ws, FL_KR + (c.pb % 5) * 64 + c.pb / 5, c.tag); }
    else if (c.lane < 8) st_flag(c.p.ws, FL_WT + c.lane * 128 + c.pb - 80, c.tag);
}

template <int MODE>
DI void loader_gemm1(const Ctx& c) {
    const Params& p = c.p;
    const int lane = c.lane, w = c.wave - 4, wave = c.wave;
    (void)wave;
    LAS char* lds = c.lds;
    rsrc_t rq = mkrsrc(p.q, S * D * 2), rg_ = mkrsrc(p.wg, D * F * 2), ru_ = mkrsrc(p.wu, D * F * 2);
    unsigned vx[2], vw[3];
    {
        const int r = lane >> 3, slot = lane & 7;
        vx[0] = r * 1280 + ((slot ^ (r >> 1)) * 16);
        vx[1] = r * 1280 + ((slot ^ (r >> 1) ^ 4) * 16);
#pragma unroll
        for (int j = 0; j < 3; ++j) {
            const int L = 2 * j + (lane >> 5), kbl = L / 3, cc = L % 3, row = (lane & 31) >> 2, ch = (lane & 3) ^ (2 * kbl);
            vw[j] = (kbl * 8 + row) * 3072 + cc * 64 + ch * 16;
        }
    }
    const unsigned sx0 = (c.bs * 128 + 32 * w) * 1280, sw0 = 16 * w * 3072 + c.bf * 192;
    auto issue = [&](int t) {
        const unsigned base = c.ldsb + (t >= 9 ? t - 9 : (t >= 6 ? t - 6 : (t >= 3 ? t - 3 : t))) * G1_STAGE;
#pragma unroll
        for (int i = 0; i < 4; ++i) dma16(base + (32 * w + 8 * i) * 128, vx[i & 1], rq, sx0 + i * 8 * 1280 + t * 128);
#pragma unroll
        for (int j = 0; j < 3; ++j) dma16(base + 16384 + w * 3072 + j * 1024, vw[j], rg_, sw0 + t * 64 * 3072);
#pragma unroll
        for (int j = 0; j < 3; ++j) dma16(base + 28672 + w * 3072 + j * 1024, vw[j], ru_, sw0 + t * 64 * 3072);
    };
    constexpr int NX = MODE == 0 ? 1 : (MODE == 1 ? 4 : 16);
    PrepRegs R;
    if (MODE == 1) prep_load<true>(c, R);
    if (MODE == 2) prep_load<false>(c, R);
    if (w < 2) {
        rsrc_t rgw = mkrsrc(p.gw, D * 2);
        if (w == 0 || lane < 16) dma16(c.ldsb + P0_GW + w * 1024, (w * 64 + lane) * 16, rgw, 0);
    }
    if (w == 3 && lane < 8) *(LAS unsigned*)(lds + L_CNT + lane * 4) = 0;
    issue(0); issue(1);
    unsigned dummy = 0;
    if (MODE == 0) {
        rsrc_t rf = mkrsrc(p.ssk, D * S * 2);
        prefetch_lines(dummy, rf, lane < 32 ? (c.cb * 4 + w) * (S * 2) + lane * 128 : 0xF0000000u);
    } else {
        waitv<20>();
        prep_run<MODE == 1>(c, R);
    }
    waitv<10 + NX>(); WAITL0(); bar();
    STAMPT(3);
    issue(2);
    const int grw = 8 * c.bf + (lane >> 3), gch = (lane & 7) ^ ((grw >> 1) & 7);
    float gacc = 0.f;
    auto gate_step = [&](int t) {
        const int buf = t >= 9 ? t - 9 : (t >= 6 ? t - 6 : (t >= 3 ? t - 3 : t));
        const u32x4 xv = *(LAS u32x4*)(lds + buf * G1_STAGE + grw * 128 + (lane & 7) * 16);
        const u32x4 gv = *(LAS u32x4*)(lds + P0_GW + t * 128 + gch * 16);
#pragma unroll
        for (int i = 0; i < 4; ++i) gacc = dot2(xv[i], gv[i], gacc);
    };
    if (w == 3) gate_step(0);
    for (int t = 0; t < 10; ++t) {
        if (t == 0) waitv<NX + 10>();
        else if (t <= 7) waitv<10>();
        else waitv<0>();
        WAITL0();
        bar();
        if (t + 3 <= 9) issue(t + 3);
        if (w == 3 && t < 9) gate_step(t + 1);
        if (MODE != 0 && t == 1 && w == 0) prep_publish(c);
    }
    TIE(dummy);
    if (w == 3) {
        gacc = dpp_add<0x111, 0xf>(gacc); gacc = dpp_add<0x112, 0xf>(gacc); gacc = dpp_add<0x114, 0xf>(gacc);
        rsrc_t rG = mkrsrc(p.ws + WS_G, S * 4);
        __builtin_amdgcn_raw_buffer_store_b32(__float_as_uint(sigmoidf_fast(gacc)), rG, (lane & 7) == 7 ? (c.bs * 128 + grw) * 4 : 0xF0000000u, 0, 16);
    }
}

__global__ __launch_bounds__(512, 2) void k_fused(Params p) {
    extern __shared__ __attribute__((aligned(16))) char smem[];
    LAS char* lds = (LAS char*)smem;
    const unsigned ldsb = (unsigned)(size_t)lds;
    const int wave = rfl(threadIdx.x >> 6), lane = threadIdx.x & 63;
    const int b = blockIdx.x, xcd = b & 7, idx = b >> 3;
    const bool has_conv = idx < 20;
    const int cb = xcd * 20 + idx, pb = xcd * 12 + idx - 20;
    const int bs = (xcd & 1) * 8 + (idx & 7), bf = (xcd >> 1) * 4 + (idx >> 3);
    const int st = xcd * 4 + (idx & 3), ds = idx >> 2;
    const unsigned long long tstart_ = __builtin_amdgcn_s_memrealtime();
    STAMPT(0);
    Ctx c; c.p = p; c.lds = lds; c.ldsb = ldsb; c.lane = lane; c.wave = wave; c.b = b; c.xcd = xcd; c.idx = idx; c.cb = cb; c.pb = pb; c.bs = bs; c.bf = bf;
    c.tag = rfl(*(const GAS unsigned*)(p.ws + WS_FLAG)) + 1u;
    const unsigned tag = c.tag;
    if (b == 0 && wave == 0 && lane == 0) __hip_atomic_store((GAS unsigned long long*)(p.ws + WS_FLAG) + 256, ((unsigned long long)tag << 32) | (unsigned)tstart_, RLX_AGENT);

    if (wave >= 4) {
        const int w = wave - 4;
        if (has_conv) loader_gemm1<0>(c);
        else if (pb < 80) loader_gemm1<1>(c);
        else loader_gemm1<2>(c);
        STAMPT(4);
        const unsigned vo = lane * 16;
        const int d = cb * 4 + w;
        const unsigned sl = ldsb + w * CV_SLOT;
        if (has_conv) {
            rsrc_t rf = mkrsrc(p.ssk, D * S * 2);
#pragma unroll
            for (int i = 0; i < 4; ++i) dma16(sl + 512 + i * 1024, vo, rf, d * (S * 2) + i * 1024);
            LAS char* slot = lds + w * CV_SLOT;
            if (lane < 32) *(LAS u32x4*)(slot + lane * 16) = (u32x4){0, 0, 0, 0};
            else if (lane < 48) *(LAS u32x4*)(slot + CV_KF + ((lane - 32) >> 2) * CV_COPY + 4096 + (lane & 3) * 16) = (u32x4){0, 0, 0, 0};
        }
        if (w == 0) {
            if (has_conv) poll4<4>(p.ws, FL_KR + (cb >> 5) * 64, tag, lane);
            poll4<4>(p.ws, FL_WT + xcd * 128, tag, lane);
            if (lane == 0) lds_set(lds, L_CNT + 8, 1u);
        } else {
            lds_wait(lds, L_CNT + 8, 1u);
        }
        STAMPT(5);
        if (has_conv) {
            rsrc_t r0 = mkrsrc(p.ws + WS_KR0, D * KRS * 2);
#pragma unroll
            for (int i = 0; i < 4; ++i) dma16_sc1(sl + CV_KF + i * 1024, vo, r0, d * (KRS * 2) + i * 1024);
        }
        rsrc_t rw = mkrsrc(p.ws + WS_WT, F * D * 2);
        const unsigned sw2 = ds * (48 * 5 * 1024) + w * 5120;
        const int rot = (3 * ds) >> 1;
        auto g2_src = [&](int t) { const int ts = t + rot; return sw2 + (ts >= 12 ? ts - 12 : ts) * G2_STAGE; };
#pragma unroll
        for (int t = 0; t < 3; ++t)
#pragma unroll
            for (int j = 0; j < 5; ++j) dma16_sc1(ldsb + G2_RING + t * G2_STAGE + w * 5120 + j * 1024, vo, rw, g2_src(t) + j * 1024);
        waitv<15>();
        if (has_conv) {
            WAITL0();
            LAS char* c0 = lds + w * CV_SLOT + CV_KF;
#pragma unroll
            for (int j = 0; j < 4; ++j) {
                const u32x4 a = *(LAS u32x4*)(c0 + j * 1024 + lane * 16);
                const u32x2 nx = *(LAS u32x2*)(c0 + j * 1024 + lane * 16 + 16);
                *(LAS u32x4*)(c0 + CV_COPY + j * 1024 + lane * 16) =
                    (u32x4){__builtin_amdgcn_alignbit(a[1], a[0], 16), __builtin_amdgcn_alignbit(a[2], a[1], 16), __builtin_amdgcn_alignbit(a[3], a[2], 16), __builtin_amdgcn_alignbit(nx[0], a[3], 16)};
                *(LAS u32x4*)(c0 + 2 * CV_COPY + j * 1024 + lane * 16) = (u32x4){a[1], a[2], a[3], nx[0]};
                *(LAS u32x4*)(c0 + 3 * CV_COPY + j * 1024 + lane * 16) =
                    (u32x4){__builtin_amdgcn_alignbit(a[2], a[1], 16), __builtin_amdgcn_alignbit(a[3], a[2], 16), __builtin_amdgcn_alignbit(nx[0], a[3], 16), __builtin_amdgcn_alignbit(nx[1], nx[0], 16)};
            }
        }
        WAITL0();
        bar();
        STAMPT(6);
        u32x4 wreg[9][NPUT + 1];
#pragma unroll
        for (int t = 0; t < 9; ++t)
#pragma unroll
            for (int j = 0; j < NPUT; ++j) wreg[t][j] = __builtin_amdgcn_raw_buffer_load_b128(rw, vo, g2_src(t + 3) + j * 1024, 16);
        unsigned dummy = 0;
        if (NPUT < 5) {
#pragma unroll
            for (int i = 0; i < 2; ++i) {
                const int ln = (idx & 3) * 480 + (w * 2 + i) * 64 + lane;
                prefetch_lines(dummy, rw, (w * 2 + i) * 64 + lane < 480 ? ds * (48 * 5 * 1024) + ln * 128 : 0xF0000000u);
            }
        }
        if (w == 0) poll4<4>(p.ws, FL_H + (st >> 1) * 64, tag, lane);
        WAITV(0);
#pragma unroll
        for (int t = 0; t < 9; ++t)
#pragma unroll
            for (int j = 0; j < NPUT; ++j) TIE(wreg[t][j]);
        TIE(dummy);
        bar();
        STAMPT(7);
        rsrc_t rh = mkrsrc(p.ws + WS_H, S * F * 2);
        const unsigned sh2 = ((st * 2 + (w & 1)) * 96 + (w >> 1) * 48) * 1024;
        auto wslot = [&](int t) { return (t & 3) < 3 ? G2_RING + (t & 3) * G2_STAGE : G2_W3; };
        auto g2_dma = [&](int t) {
            const int ts = t + rot;
            const unsigned src = sh2 + (ts >= 12 ? ts - 12 : ts) * 4096, dst = ldsb + (t & 3) * G2_HST + w * 4096;
#pragma unroll
            for (int j = 0; j < 4; ++j) dma16_sc1(dst + j * 1024, vo, rh, src + j * 1024);
            if (t >= 3) {
#pragma unroll
                for (int j = NPUT; j < 5; ++j) dma16_sc1(ldsb + wslot(t) + w * 5120 + j * 1024, vo, rw, g2_src(t) + j * 1024);
            }
        };
        auto g2_put = [&](int t) {
            LAS char* base = lds + wslot(t) + w * 5120 + lane * 16;
#pragma unroll
            for (int j = 0; j < NPUT; ++j) *(LAS u32x4*)(base + j * 1024) = wreg[t - 3][j];
        };
        constexpr int ND = 4 + 5 - NPUT;
        g2_dma(0); g2_dma(1); g2_dma(2); g2_dma(3); g2_put(3);
        waitv<8 + ND>(); WAITL0(); bar();
        rsrc_t ry = mkrsrc(p.ws + WS_YT, D * S * 2);
        auto load_y = [&]() {
            u32x4 yv[3];
#pragma unroll
            for (int i = 0; i < 3; ++i) {
                const int it = w + 4 * i, ch = 8 * it + (lane >> 3);
                yv[i] = __builtin_amdgcn_raw_buffer_load_b128(ry, it < 10 ? ((80 * ds + ch) * S + 64 * st + (lane & 7) * 8) * 2 : 0xF0000000u, 0, 16);
            }
#pragma unroll
            for (int i = 0; i < 3; ++i) {
                const int it = w + 4 * i, ch = 8 * it + (lane >> 3);
                if (it < 10) *(LAS u32x4*)(lds + G2_Y + ch * 128 + (lane & 7) * 16) = yv[i];
            }
        };
        waitv<4 + ND>(); bar();
        waitv<2 * ND - ND>(); bar(); g2_dma(4); g2_put(4);
        waitv<ND>(); WAITL0(); bar(); g2_dma(5); g2_put(5);
        waitv<ND>(); WAITL0(); bar(); g2_dma(6); g2_put(6);
        waitv<ND>(); WAITL0(); bar(); g2_dma(7); g2_put(7);
        waitv<ND>(); WAITL0(); bar(); g2_dma(8); g2_put(8);
        waitv<ND>(); WAITL0(); bar(); g2_dma(9); g2_put(9);
        waitv<ND>(); WAITL0(); bar(); g2_dma(10); g2_put(10);
        waitv<ND>(); WAITL0(); bar(); g2_dma(11); g2_put(11);
        u32x4 yf = {0, 0, 0, 0};
        if (w == 0) {
            rsrc_t rfl_ = mkrsrc(p.ws + WS_FLAG, 32768);
            asm volatile("buffer_load_dwordx4 %0, %1, %2, 0 offen sc1" : "=v"(yf) : "v"((FL_Y + ds * 64 + (lane < 5 ? lane : 0) * 4) * 4), "s"(rfl_) : "memory");
            waitv<ND + 1>();
        } else {
            waitv<ND>();
        }
        WAITL0(); bar();
        WAITV(0);
        if (w == 0) {
            TIE(yf);
            if (__all(yf[0] == tag && yf[1] == tag && yf[2] == tag && yf[3] == tag) && lane == 0) lds_set(lds, L_CNT + 12, 1u);
        }
        WAITL0(); bar();
        const bool yearly = rfl(*(volatile LAS unsigned*)(lds + L_CNT + 12)) == 1u;
        if (yearly) load_y();
        bar();
        if (!yearly) {
            if (w == 0) { poll4<5>(p.ws, FL_Y + ds * 64, tag, lane); if (lane == 0) lds_set(lds, L_CNT + 12, 2u); }
            else lds_wait(lds, L_CNT + 12, 2u);
            load_y();
        }
        bar();
        WAITL0();
        bar();
        if (b == 0 && w == 0) {
            rsrc_t rfl_ = mkrsrc(p.ws + WS_FLAG, 32768);
            const unsigned fo = (FL_H + (lane >> 2) * 64 + (lane & 3) * 4) * 4;
            for (int spins = 0; spins < (1 << 18); ++spins) {
                u32x4 x;
                asm volatile("buffer_load_dwordx4 %0, %1, %2, 0 offen sc1\n\ts_waitcnt vmcnt(0)" : "=&v"(x) : "v"(fo), "s"(rfl_) : "memory");
                if (__all(x[0] == tag && x[1] == tag && x[2] == tag && x[3] == tag)) break;
                __builtin_amdgcn_s_sleep(4);
            }
            if (lane == 0) st_flag(p.ws, FL_EPOCH, tag);
        }
        STAMPT(8);
    } else {
        float Dd = bflo(p.sd[(has_conv ? cb * 4 : 0) + wave]);
        asm volatile("" : "+v"(Dd));
        {
            const int wsx = wave & 1, wf = wave >> 1;
            const int g = lane >> 4, fr = lane & 15, q = (lane & 15) >> 2, pp = lane & 3;
            int oa[3];
#pragma unroll
            for (int i = 0; i < 3; ++i) {
                const int ft = 3 * wf + i, c = ft >> 1, half = ft & 1;
                oa[i] = 16384 + (g * 3 + c) * 512 + q * 64 + 16 * ((2 * half + (pp >> 1)) ^ (2 * (g & 1))) + 8 * (pp & 1);
            }
            const int ob0 = (64 * wsx + fr) * 128 + ((g ^ (fr >> 1)) * 16);
            const int ob1 = (64 * wsx + fr) * 128 + ((g ^ (fr >> 1) ^ 4) * 16);
            f32x4 accg[3][4], accu[3][4];
#pragma unroll
            for (int i = 0; i < 3; ++i)
#pragma unroll
                for (int m = 0; m < 4; ++m) { accg[i][m] = (f32x4){0, 0, 0, 0}; accu[i][m] = (f32x4){0, 0, 0, 0}; }
            struct Fr { s16x8 a[6]; s16x8 b[4]; } fa, fb;
            auto rd = [&](Fr& f, int ks, int sb) {
#pragma unroll
                for (int m = 0; m < 4; ++m) f.b[m] = *(LAS s16x8*)(lds + sb + (ks ? ob1 : ob0) + m * 2048);
#pragma unroll
                for (int i = 0; i < 3; ++i) {
                    f.a[i] = cat(trread(lds + sb + oa[i] + ks * 6144), trread(lds + sb + oa[i] + ks * 6144 + 256));
                    f.a[3 + i] = cat(trread(lds + sb + oa[i] + ks * 6144 + 12288), trread(lds + sb + oa[i] + ks * 6144 + 256 + 12288));
                }
            };
            auto mm = [&](const Fr& f) {
#pragma unroll
                for (int i = 0; i < 3; ++i)
#pragma unroll
                    for (int m = 0; m < 4; ++m) {
                        accg[i][m] = mfma16(f.a[i], f.b[m], accg[i][m]);
                        accu[i][m] = mfma16(f.a[3 + i], f.b[m], accu[i][m]);
                    }
            };
            auto interleave = [&]() {
#pragma unroll
                for (int i = 0; i < 16; ++i) { SGB(0x008, 1); SGB(0x100, 1); }
                SGB(0x008, 8);
                SB0();
            };
            unsigned dummy = 0;
            if (!has_conv) {
                PrepRegs R;
                if (pb < 80) { prep_load<true>(c, R); WAITV(0); prep_run<true>(c, R); }
                else { prep_load<false>(c, R); WAITV(0); prep_run<false>(c, R); }
            }
            {
                rsrc_t rpf = mkrsrc(wave == 0 ? p.q : (wave == 1 ? p.wg : p.wu), wave == 0 ? S * D * 2 : D * F * 2);
                const unsigned pfo = wave == 0 ? (8 * b + (lane >> 3)) * 1280 + (2 + (lane & 7)) * 128
                                               : (lane < 48 && wave < 3 ? (128 + b + (lane >= 24 ? 256 : 0)) * 3072 + (lane >= 24 ? lane - 24 : lane) * 128 : 0xF0000000u);
                if (has_conv) __builtin_amdgcn_s_sleep(SLEEP0);
                prefetch_lines(dummy, rpf, pfo);
            }
            __builtin_amdgcn_s_setprio(1);
            bar();
            STAMPT(3);
#define RSTAMP1() do {} while (0)
            rd(fa, 0, 0);
            int sb = 0;
            for (int t = 0; t < 10; ++t) {
                rd(fb, 1, sb); mm(fa); interleave();
                WAITL0();
                if (t == 1 && !has_conv) WAITV(0);
                RSTAMP1();
                bar();
                RSTAMP1();
                sb = (sb == 2 * G1_STAGE) ? 0 : sb + G1_STAGE;
                if (t < 9) rd(fa, 0, sb);
                mm(fb); interleave();
            }
            STAMPT(4);
            WAITV(0); TIE(dummy);
            rsrc_t rh = mkrsrc(p.ws + WS_H, S * F * 2);
#pragma unroll
            for (int mp = 0; mp < 2; ++mp)
#pragma unroll
                for (int i = 0; i < 3; ++i) {
                    float hv[8];
#pragma unroll
                    for (int e = 0; e < 8; ++e) hv[e] = accu[i][2 * mp + (e >> 2)][e & 3] * sigmoidf_fast(accg[i][2 * mp + (e >> 2)][e & 3]);
                    const u32x4 o = {cvtpk(hv[0], hv[1]), cvtpk(hv[2], hv[3]), cvtpk(hv[4], hv[5]), cvtpk(hv[6], hv[7])};
                    const int sp = bs * 4 + wsx * 2 + mp, ft = bf * 6 + wf * 3 + i;
                    __builtin_amdgcn_raw_buffer_store_b128(o, rh, ((sp * 96 + ft) * 64 + lane) * 16, 0, 16);
                }
        }
        STAMPT(5);
        auto publish_h = [&]() {
            WAITV(0);
            unsigned old = 0;
            if (lane == 0) old = __hip_atomic_fetch_add((LAS unsigned*)(lds + L_CNT), 1u, __ATOMIC_RELAXED, __HIP_MEMORY_SCOPE_WORKGROUP);
            if (lane == 0 && old == 3) st_flag(p.ws, FL_H + bs * 64 + bf, tag);
        };
        bar();
        STAMPT(6);
        f32x4 cacc[8];
        u32x2 ckv[8];
        const int ca = lane & 15, cg = lane >> 4;
        if (has_conv) {
            LAS char* slot = lds + wave * CV_SLOT;
#pragma unroll
            for (int n = 0; n < 8; ++n) cacc[n] = (f32x4){0, 0, 0, 0};
            unsigned pg = (unsigned)(size_t)slot + 480 + 32 * ca + 16 * cg;
            unsigned pf = (unsigned)(size_t)slot + CV_KF + (3 - (ca & 3)) * CV_COPY + 472 + 16 * cg - 8 * (ca >> 2);
            struct Res { s16x8 G[8]; s16x4 Fl[8], Fh[8]; } RA, RB;
            auto rd1 = [&](Res& R, int k) {
                const int j = k / 3, m = k % 3;
                if (m == 0) asm volatile("ds_read_b64 %0, %1 offset:%2" : "=v"(R.Fl[j]) : "v"(pf), "n"(512 * (7 - j)));
                else if (m == 1) asm volatile("ds_read_b64 %0, %1 offset:%2" : "=v"(R.Fh[j]) : "v"(pf), "n"(512 * (7 - j) + 8));
                else asm volatile("ds_read_b128 %0, %1 offset:%2" : "=v"(R.G[j]) : "v"(pg), "n"(512 * j));
            };
            auto land = [&](Res& R) {
                asm volatile("s_waitcnt lgkmcnt(0)"
                             : "+v"(R.G[0]), "+v"(R.G[1]), "+v"(R.G[2]), "+v"(R.G[3]), "+v"(R.G[4]), "+v"(R.G[5]), "+v"(R.G[6]), "+v"(R.G[7]),
                               "+v"(R.Fl[0]), "+v"(R.Fl[1]), "+v"(R.Fl[2]), "+v"(R.Fl[3]), "+v"(R.Fl[4]), "+v"(R.Fl[5]), "+v"(R.Fl[6]), "+v"(R.Fl[7]));
                asm volatile(""
                             : "+v"(R.Fh[0]), "+v"(R.Fh[1]), "+v"(R.Fh[2]), "+v"(R.Fh[3]), "+v"(R.Fh[4]), "+v"(R.Fh[5]), "+v"(R.Fh[6]), "+v"(R.Fh[7]));
                SB0();
            };
            auto residue = [&](Res& R, Res& N) {
                pg -= 64; pf -= 64;
                int k = 0;
#pragma unroll
                for (int I = 0; I < 8; ++I) {
                    const s16x8 Fv = cat(R.Fl[I], R.Fh[I]);
#pragma unroll
                    for (int n = I; n < 8; ++n) {
                        cacc[n] = mfma16(Fv, R.G[n - I], cacc[n]);
                        if (k < 24) rd1(N, k);
                        ++k;
                        SB0();
                    }
                }
                land(N);
            };
#pragma unroll
            for (int k = 0; k < 24; ++k) rd1(RA, k);
            land(RA);
            for (int r2 = 0; r2 < 4; ++r2) {
                residue(RA, RB);
                residue(RB, RA);
                if (r2 == 0) publish_h();
            }
#pragma unroll
            for (int n = 0; n < 8; ++n) ckv[n] = *(LAS u32x2*)(slot + CV_KF + 2 * (2044 - 256 * n - 16 * ca - 4 * cg));
        } else {
            publish_h();
        }
        STAMPT(7);
        {
            const int sp = wave & 1, kh = wave >> 1;
            WAITL0();
            bar();
            STAMPT(8);
            SB0();
            if (has_conv) {
                const int d = cb * 4 + wave;
                rsrc_t ry = mkrsrc(p.ws + WS_YT, D * S * 2);
                u32x2 o[8];
#pragma unroll
                for (int n = 0; n < 8; ++n) {
                    o[n][0] = cvtpk(cacc[n][0] + bfhi(ckv[n][1]) * Dd, cacc[n][1] + bflo(ckv[n][1]) * Dd);
                    o[n][1] = cvtpk(cacc[n][2] + bfhi(ckv[n][0]) * Dd, cacc[n][3] + bflo(ckv[n][0]) * Dd);
                }
#pragma unroll
                for (int n = 0; n < 8; n += 2) {
                    auto x = __builtin_amdgcn_permlane16_swap(o[n][0], o[n + 1][0], false, false);
                    auto y = __builtin_amdgcn_permlane16_swap(o[n][1], o[n + 1][1], false, false);
                    const u32x4 val = {x[0], y[0], x[1], y[1]};
                    const int s = 256 * (n + (cg & 1)) + 16 * ca + 4 * (cg & 2);
                    __builtin_amdgcn_raw_buffer_store_b128(val, ry, (d * S + s) * 2, 0, 16);
                }
            }
            SB0();
            STAMPT(9);
            const int s16 = lane & 15, g = lane >> 4, q = s16 >> 2, pp = lane & 3;
            const int srow = 64 * st + 32 * sp + 16 * kh + s16;
            const float gt = __hip_atomic_load((const GAS float*)(p.ws + WS_G) + srow, RLX_AGENT);
            u32x2 qv[5];
#pragma unroll
            for (int n = 0; n < 5; ++n) qv[n] = *(const u32x2*)(p.q + (size_t)srow * D + 80 * ds + 16 * n + 4 * g);
            f32x4 acc[2][5];
#pragma unroll
            for (int n = 0; n < 5; ++n) { acc[0][n] = (f32x4){0, 0, 0, 0}; acc[1][n] = (f32x4){0, 0, 0, 0}; }
            const unsigned pw = ldsb + kh * 10240 + lane * 16, ph = ldsb + wave * 4096 + lane * 16;
            struct Fr2 { s16x8 w[5]; u32x4 h[2]; } fa, fb;
            auto rd2 = [&](Fr2& f, int s4, int kk) {
                LAS char* bh = (LAS char*)(size_t)(ph + s4 * G2_HST + kk * 2048);
                LAS char* bw = (LAS char*)(size_t)(pw + (s4 < 3 ? G2_RING + s4 * G2_STAGE : G2_W3) + kk * 5120);
                f.h[0] = *(LAS u32x4*)bh; f.h[1] = *(LAS u32x4*)(bh + 1024);
#pragma unroll
                for (int n = 0; n < 5; ++n) f.w[n] = *(LAS s16x8*)(bw + n * 1024);
            };
            auto swp = [&](Fr2& f) {
                unsigned a2 = f.h[0][2], a3 = f.h[0][3], b0 = f.h[1][0], b1 = f.h[1][1];
                asm volatile("v_swap_b32 %0, %1\n\tv_swap_b32 %2, %3\n\ts_nop 1" : "+v"(a2), "+v"(b0), "+v"(a3), "+v"(b1));
                f.h[0][2] = a2; f.h[0][3] = a3; f.h[1][0] = b0; f.h[1][1] = b1;
            };
            auto step = [&](const Fr2& f) {
                const s16x8 h0 = __builtin_bit_cast(s16x8, f.h[0]), h1 = __builtin_bit_cast(s16x8, f.h[1]);
#pragma unroll
                for (int n = 0; n < 5; ++n) { acc[0][n] = mfma16(f.w[n], h0, acc[0][n]); acc[1][n] = mfma16(f.w[n], h1, acc[1][n]); }
            };
            auto il2 = [&]() {
#pragma unroll
                for (int i = 0; i < 7; ++i) { SGB(0x008, 1); SGB(0x100, 1); }
                SGB(0x008, 3);
                SB0();
            };
            STAMPT(1);
            bar();
            STAMPT(2);
#define RSTAMP() do {} while (0)
            rd2(fa, 0, 0);
            swp(fa);
            for (int t = 0; t < 12; ++t) {
                rd2(fb, t & 3, 1);
                step(fa);
                il2();
                swp(fb);
                SB0();
                if (t == 4) WAITV(0);
                RSTAMP();
                bar();
                RSTAMP();
                if (t == 4 && has_conv && wave == 0 && lane == 0) st_flag(p.ws, FL_Y + (cb / 20) * 64 + cb % 20, tag);
                rd2(fa, (t + 1) & 3, 0);
                step(fb);
                il2();
                swp(fa);
                SB0();
            }
            WAITL0();
            bar();
            STAMPT(10);
#pragma unroll
            for (int n = 0; n < 5; ++n) *(LAS f32x4*)(lds + G2_RED + (wave * 5 + n) * 1024 + lane * 16) = kh ? acc[0][n] : acc[1][n];
            WAITL0();
            bar();
            STAMPT(11);
#pragma unroll
            for (int n = 0; n < 5; ++n) {
                const f32x4 o4 = (kh ? acc[1][n] : acc[0][n]) + *(LAS f32x4*)(lds + G2_RED + ((wave ^ 2) * 5 + n) * 1024 + lane * 16);
                const u32x2 yv = __builtin_bit_cast(u32x2, trread(lds + G2_Y + (16 * n + 4 * g + q) * 128 + (32 * sp + 16 * kh + 4 * pp) * 2));
                const float o0 = gt * (bflo(qv[n][0]) * bflo(yv[0])) + (1.f - gt) * o4[0];
                const float o1 = gt * (bfhi(qv[n][0]) * bfhi(yv[0])) + (1.f - gt) * o4[1];
                const float o2 = gt * (bflo(qv[n][1]) * bflo(yv[1])) + (1.f - gt) * o4[2];
                const float o3 = gt * (bfhi(qv[n][1]) * bfhi(yv[1])) + (1.f - gt) * o4[3];
                *(u32x2*)(p.out + (size_t)srow * D + 80 * ds + 16 * n + 4 * g) = (u32x2){cvtpk(o0, o1), cvtpk(o2, o3)};
            }
            STAMPT(12);
            if (wave == 0) { unsigned long long x_; int sp_ = 0; do { x_ = __hip_atomic_load((GAS unsigned long long*)(p.ws + WS_FLAG) + 256, RLX_AGENT); } while ((unsigned)(x_ >> 32) != tag && ++sp_ < 100000);
                const unsigned t0_ = rfl((unsigned)x_); while ((unsigned)__builtin_amdgcn_s_memrealtime() - t0_ < 5000u) __builtin_amdgcn_s_sleep(1); }
        }
    }
}

extern "C" void kernel_launch(void* const* d_in, const int* in_sizes, int n_in,
                              void* d_out, int out_size, void* d_ws, size_t ws_size,
                              hipStream_t stream) {
    Params p;
    p.q = (const u16*)d_in[0]; p.k = (const u16*)d_in[1]; p.v = (const u16*)d_in[2];
    p.sk = (const u16*)d_in[3]; p.ssk = (const u16*)d_in[4]; p.sd = (const u16*)d_in[5];
    p.wg = (const u16*)d_in[7]; p.wu = (const u16*)d_in[8]; p.wd = (const u16*)d_in[9]; p.gw = (const u16*)d_in[10];
    p.out = (u16*)d_out; p.ws = (char*)d_ws;
    hipFuncSetAttribute((const void*)k_fused, hipFuncAttributeMaxDynamicSharedMemorySize, LDS_BYTES);
    k_fused<<<256, 512, LDS_BYTES, stream>>>(p);
}
```

```cpp
#include <hip/hip_runtime.h>
#include <hip/hip_bf16.h>
#include <stdint.h>

typedef unsigned short u16;
typedef short s16x4 __attribute__((ext_vector_type(4)));
typedef short s16x8 __attribute__((ext_vector_type(8)));
typedef __bf16 b16x8 __attribute__((ext_vector_type(8)));
typedef __bf16 b16x2 __attribute__((ext_vector_type(2)));
typedef float f32x2 __attribute__((ext_vector_type(2)));
typedef float f32x4 __attribute__((ext_vector_type(4)));
typedef unsigned u32x2 __attribute__((ext_vector_type(2)));
typedef unsigned u32x4 __attribute__((ext_vector_type(4)));
#define LAS __attribute__((address_space(3)))
#define GAS __attribute__((address_space(1)))
#define DI __device__ __forceinline__

static constexpr int S = 2048, D = 640, F = 1536;
static constexpr int KRS = 2112;
static constexpr unsigned WS_H = 0, WS_KR0 = 8u << 20, WS_KR1 = 11u << 20, WS_YT = 14u << 20, WS_G = 17u << 20, WS_WT = 18u << 20,
                          WS_STAMP = 20u << 20, WS_FLAG = 24u << 20;
static constexpr unsigned FL_EPOCH = 0, FL_KR = 1024, FL_WT = 2048, FL_H = 4096, FL_Y = 6144;

struct Params {
    const u16 *q, *k, *v, *sk, *ssk, *sd, *wg, *wu, *wd, *gw;
    u16* out; char* ws;
};

DI float bflo(unsigned x) { return __uint_as_float(x << 16); }
DI float bfhi(unsigned x) { return __uint_as_float(x & 0xffff0000u); }
DI unsigned cvtpk(float lo, float hi) { f32x2 v = {lo, hi}; b16x2 b = __builtin_convertvector(v, b16x2); return __builtin_bit_cast(unsigned, b); }
DI unsigned rfl(unsigned x) { return __builtin_amdgcn_readfirstlane(x); }
DI float sigmoidf_fast(float x) { return __builtin_amdgcn_rcpf(1.f + __builtin_amdgcn_exp2f(x * -1.44269504089f)); }
DI f32x4 mfma16(s16x8 a, s16x8 b, f32x4 c) {
    return __builtin_amdgcn_mfma_f32_16x16x32_bf16(__builtin_bit_cast(b16x8, a), __builtin_bit_cast(b16x8, b), c, 0, 0, 0);
}
DI s16x4 trread(LAS char* p) { return __builtin_amdgcn_ds_read_tr16_b64_v4i16((LAS s16x4*)p); }
DI s16x8 cat(s16x4 a, s16x4 b) { return __builtin_shufflevector(a, b, 0, 1, 2, 3, 4, 5, 6, 7); }
typedef __amdgpu_buffer_rsrc_t rsrc_t;
DI rsrc_t mkrsrc(const void* p, unsigned bytes) { return __builtin_amdgcn_make_buffer_rsrc((void*)p, 0, bytes, 0x00020000); }
DI void dma16(unsigned m0v, unsigned voff, rsrc_t r, unsigned soff) {
    asm volatile("s_mov_b32 m0, %0\n\ts_nop 0\n\tbuffer_load_dwordx4 %1, %2, %3 offen lds" ::"s"(m0v), "v"(voff), "s"(r), "s"(soff) : "m0", "memory");
}
DI void dma16_sc1(unsigned m0v, unsigned voff, rsrc_t r, unsigned soff) {
    asm volatile("s_mov_b32 m0, %0\n\ts_nop 0\n\tbuffer_load_dwordx4 %1, %2, %3 offen sc1 lds" ::"s"(m0v), "v"(voff), "s"(r), "s"(soff) : "m0", "memory");
}
DI u32x4 aload(rsrc_t r, unsigned voff) { u32x4 x; asm volatile("buffer_load_dwordx4 %0, %1, %2, 0 offen" : "=v"(x) : "v"(voff), "s"(r) : "memory"); return x; }
DI void prefetch_lines(unsigned scratch, rsrc_t r, unsigned voff) {
    asm volatile("s_mov_b32 m0, %0\n\ts_nop 0\n\tbuffer_load_dword %1, %2, 0 offen lds" ::"s"(scratch), "v"(voff), "s"(r) : "m0", "memory");
}
template <int N> DI void waitv() { asm volatile("s_waitcnt vmcnt(%0)" ::"n"(N) : "memory"); }
DI float dot2(unsigned a, unsigned b, float c) { return __builtin_amdgcn_fdot2_f32_bf16(__builtin_bit_cast(b16x2, a), __builtin_bit_cast(b16x2, b), c, false); }
template <int CTRL, int RM> DI float dpp_add(float x) {
    return x + __int_as_float(__builtin_amdgcn_update_dpp(0, __float_as_int(x), CTRL, RM, 0xf, true));
}
DI float wave_sum(float x) {
    x = dpp_add<0x111, 0xf>(x); x = dpp_add<0x112, 0xf>(x); x = dpp_add<0x114, 0xf>(x); x = dpp_add<0x118, 0xf>(x);
    x = dpp_add<0x142, 0xa>(x); x = dpp_add<0x143, 0xc>(x);
    return __int_as_float(__builtin_amdgcn_readlane(__float_as_int(x), 63));
}
#define TIE(x) asm volatile("" : "+v"(x))
#define WAITV(n) asm volatile("s_waitcnt vmcnt(" #n ")" ::: "memory")
#define WAITL0() asm volatile("s_waitcnt lgkmcnt(0)" ::: "memory")
DI void bar() { asm volatile("" ::: "memory"); __builtin_amdgcn_s_barrier(); asm volatile("" ::: "memory"); }
#define SGB(m, n) __builtin_amdgcn_sched_group_barrier(m, n, 0)
#define SB0() __builtin_amdgcn_sched_barrier(0)
#define HOFF(i) (i)
#define HW 24
#define WK 6
#define LPRIO 2
#define SLEEP1 8
#define TP0 3
#define SLEEP0 4
#define RLX_AGENT __ATOMIC_RELAXED, __HIP_MEMORY_SCOPE_AGENT

DI void st_flag(char* ws, unsigned word, unsigned tag) { __hip_atomic_store((GAS unsigned*)(ws + WS_FLAG) + word, tag, RLX_AGENT); }
template <int NW4>
DI bool poll4_once(char* ws, unsigned word, unsigned tag, int lane) {
    rsrc_t r = mkrsrc(ws + WS_FLAG, 32768);
    u32x4 x;
    asm volatile("buffer_load_dwordx4 %0, %1, %2, 0 offen sc1\n\ts_waitcnt vmcnt(0)" : "=&v"(x) : "v"((word + (lane < NW4 ? lane : 0) * 4) * 4), "s"(r) : "memory");
    return __all(x[0] == tag && x[1] == tag && x[2] == tag && x[3] == tag);
}
template <int NW4>
DI void poll4(char* ws, unsigned word, unsigned tag, int lane) {
    for (int spins = 0; spins < (1 << 18); ++spins) {
        if (poll4_once<NW4>(ws, word, tag, lane)) return;
        __builtin_amdgcn_s_sleep(4);
    }
}
DI void lds_set(LAS char* lds, int off, unsigned v) { __hip_atomic_store((LAS unsigned*)(lds + off), v, __ATOMIC_RELAXED, __HIP_MEMORY_SCOPE_WORKGROUP); }
DI void lds_wait(LAS char* lds, int off, unsigned v) {
    for (int spins = 0; spins < (1 << 22); ++spins) {
        if (rfl(__hip_atomic_load((LAS unsigned*)(lds + off), __ATOMIC_RELAXED, __HIP_MEMORY_SCOPE_WORKGROUP)) == v) break;
        __builtin_amdgcn_s_sleep(1);
    }
    asm volatile("" ::: "memory");
}

#define STAMPC(i) do {} while (0)
#define STAMPT(i) do {} while (0)

constexpr int G1_STAGE = 40960;
constexpr int P0_LDS = 122880;
constexpr int P0_T = P0_LDS, P0_W = P0_LDS + 128 * 272, P0_GW = P0_W + 1024;
constexpr int TP = 272;
constexpr int CV_SLOT = 21248, CV_COPY = 4160, CV_KF = 4608;
constexpr int G2_RING = 86016, G2_STAGE = 20480;
constexpr int G2_Y = G2_RING + 3 * G2_STAGE;
constexpr int G2_HST = 16384;
constexpr int G2_W3 = 65536;
constexpr int G2_RED = 0;
constexpr int L_DUMMY = 163328;
constexpr int L_EPOCH = 163824;
constexpr int L_CNT = 163776;
constexpr int LDS_BYTES = 163840;

struct Ctx {
    Params p; LAS char* lds; unsigned ldsb; int lane, wave, b, xcd, idx, cb, pb, bs, bf; unsigned tag;
};

struct PrepRegs { u32x4 a[16]; };
template <bool KV> DI void prep_load(const Ctx& c, PrepRegs& R) {
    const Params& p = c.p;
    const int lane = c.lane, wave = c.wave;
    if (KV) {
        const int kdc = c.pb % 5, s0 = (c.pb / 5) * 128, d0 = kdc * 128, tid = wave * 64 + lane, c16 = tid & 15, s = s0 + 4 * (tid >> 4);
        rsrc_t rk = mkrsrc(p.k, S * D * 2), rv = mkrsrc(p.v, S * D * 2), rsk = mkrsrc(p.sk, D * 8);
        if (wave == 0) dma16(c.ldsb + P0_W, lane * 16, rsk, d0 * 8);
#pragma unroll
        for (int i = 0; i < 7; ++i) R.a[i] = aload(rk, (unsigned)(((s - 3 + i) * D + d0 + 8 * c16) * 2));
#pragma unroll
        for (int i = 0; i < 4; ++i) R.a[7 + i] = aload(rv, ((s + i) * D + d0 + 8 * c16) * 2);
    } else {
        rsrc_t rwd = mkrsrc(p.wd, F * D * 2);
#pragma unroll
        for (int it = 0; it < 4; ++it) {
            const int ul = wave + 8 * it, unit = (c.pb - 80) * 30 + ul, T = unit / 10, dl = unit % 10;
#pragma unroll
            for (int i = 0; i < 4; ++i)
                R.a[4 * it + i] = aload(rwd, ul < 30 ? ((32 * T + 8 * i + (lane >> 3)) * D + 64 * dl + (lane & 7) * 8) * 2 : 0xF0000000u);
        }
    }
}
template <bool KV> DI void prep_run(const Ctx& c, PrepRegs& R) {
    const Params& p = c.p;
    const int lane = c.lane, wave = c.wave;
    LAS char* lds = c.lds;
    if (KV) {
        const int kdc = c.pb % 5, s0 = (c.pb / 5) * 128, d0 = kdc * 128, tid = wave * 64 + lane, c16 = tid & 15, rg = tid >> 4;
#pragma unroll
        for (int i = 0; i < 11; ++i) TIE(R.a[i]);
        WAITL0(); bar();
        u32x4 w4[4];
#pragma unroll
        for (int i = 0; i < 4; ++i) w4[i] = *(LAS u32x4*)(lds + P0_W + c16 * 64 + i * 16);
        LAS char* tw = lds + P0_T + 8 * c16 * TP + (((15 - (rg >> 1)) ^ c16) * 16) + ((rg & 1) ? 0 : 8);
#pragma unroll
        for (int e = 0; e < 8; ++e) {
            const unsigned sel = (e & 1) ? 0x07060302u : 0x05040100u;
            const unsigned wa = w4[e >> 1][(e & 1) * 2], wb = w4[e >> 1][(e & 1) * 2 + 1];
            unsigned pr[6];
#pragma unroll
            for (int i = 0; i < 6; ++i) pr[i] = __builtin_amdgcn_perm(R.a[i][e >> 1], R.a[i + 1][e >> 1], sel);
            float o[4];
#pragma unroll
            for (int r = 0; r < 4; ++r) o[r] = dot2(pr[r + 2], wa, dot2(pr[r], wb, 0.f)) * ((e & 1) ? bfhi(R.a[7 + r][e >> 1]) : bflo(R.a[7 + r][e >> 1]));
            *(LAS u32x2*)(tw + e * TP) = (u32x2){cvtpk(o[3], o[2]), cvtpk(o[1], o[0])};
        }
        WAITL0(); bar();
        rsrc_t r0 = mkrsrc(p.ws + WS_KR0, D * KRS * 2);
#pragma unroll
        for (int it = 0; it < 4; ++it) {
            const int item = tid + it * 512, ch = item >> 4, j = item & 15;
            __builtin_amdgcn_raw_buffer_store_b128(*(LAS u32x4*)(lds + P0_T + ch * TP + ((j ^ ((ch >> 3) & 15)) * 16)), r0, ((d0 + ch) * KRS + (1920 - s0) + 8 * j) * 2, 0, 16);
        }
    } else {
        const int g4 = lane >> 4, q = (lane & 15) >> 2, pp = lane & 3, row = 4 * g4 + q;
        rsrc_t rw = mkrsrc(p.ws + WS_WT, F * D * 2);
        LAS char* buf = lds + P0_LDS + wave * 4096;
#pragma unroll
        for (int it = 0; it < 4; ++it) {
            const int ul = wave + 8 * it, unit = (c.pb - 80) * 30 + ul, Tk = unit / 10, dl = unit % 10;
            const int kh = Tk / 24, tt = (Tk % 24) >> 1, kk = Tk & 1;
#pragma unroll
            for (int i = 0; i < 4; ++i) { TIE(R.a[4 * it + i]); *(LAS u32x4*)(buf + (8 * i + (lane >> 3)) * 128 + (lane & 7) * 16) = R.a[4 * it + i]; }
#pragma unroll
            for (int i = 0; i < 4; ++i) {
                const int d16 = 4 * dl + i, dsl = d16 / 5, n = d16 % 5;
                const s16x8 f = cat(trread(buf + row * 128 + 32 * i + 8 * pp), trread(buf + (row + 16) * 128 + 32 * i + 8 * pp));
                __builtin_amdgcn_raw_buffer_store_b128(__builtin_bit_cast(u32x4, f), rw, ul < 30 ? ((((dsl * 12 + tt) * 4 + kh * 2 + kk) * 5 + n) * 64 + lane) * 16 : 0xF0000000u, 0, 16);
            }
        }
    }
}
DI void load_epoch(const Ctx& c) {
    rsrc_t r = mkrsrc(c.p.ws + WS_FLAG, 64);
    if (c.lane == 0) asm volatile("s_mov_b32 m0, %0\n\ts_nop 0\n\tbuffer_load_dword %1, %2, 0 offen sc1 lds" ::"s"(c.ldsb + L_EPOCH), "v"(0u), "s"(r) : "m0", "memory");
}
DI unsigned get_tag(const Ctx& c) { return rfl(*(volatile LAS unsigned*)(c.lds + L_EPOCH)) + 1u; }
DI void prep_publish(const Ctx& c) {
    if (c.lane == 0) st_flag(c.p.ws, FL_KR + (c.pb % 5) * 64 + c.pb / 5, c.tag);
}

struct Tail { u32x4 kf4; bool kr_ready, wt_ready; };
template <int MODE>
DI void loader_gemm1(Ctx& c, Tail& X) {
    const Params& p = c.p;
    const int lane = c.lane, w = c.wave - 4, wave = c.wave;
    (void)wave;
    LAS char* lds = c.lds;
    rsrc_t rq = mkrsrc(p.q, S * D * 2), rg_ = mkrsrc(p.wg, D * F * 2), ru_ = mkrsrc(p.wu, D * F * 2);
    unsigned vx[2], vw[3];
    {
        const int r = lane >> 3, slot = lane & 7;
        vx[0] = r * 1280 + ((slot ^ (r >> 1)) * 16);
        vx[1] = r * 1280 + ((slot ^ (r >> 1) ^ 4) * 16);
#pragma unroll
        for (int j = 0; j < 3; ++j) {
            const int L = 2 * j + (lane >> 5), kbl = L / 3, cc = L % 3, row = (lane & 31) >> 2, ch = (lane & 3) ^ (2 * kbl);
            vw[j] = (kbl * 8 + row) * 3072 + cc * 64 + ch * 16;
        }
    }
    const unsigned sx0 = (c.bs * 128 + 32 * w) * 1280, sw0 = 16 * w * 3072 + c.bf * 192;
    auto issue = [&](int t) {
        const unsigned base = c.ldsb + (t >= 9 ? t - 9 : (t >= 6 ? t - 6 : (t >= 3 ? t - 3 : t))) * G1_STAGE;
#pragma unroll
        for (int i = 0; i < 4; ++i) dma16(base + (32 * w + 8 * i) * 128, vx[i & 1], rq, sx0 + i * 8 * 1280 + t * 128);
#pragma unroll
        for (int j = 0; j < 3; ++j) dma16(base + 16384 + w * 3072 + j * 1024, vw[j], rg_, sw0 + t * 64 * 3072);
#pragma unroll
        for (int j = 0; j < 3; ++j) dma16(base + 28672 + w * 3072 + j * 1024, vw[j], ru_, sw0 + t * 64 * 3072);
    };
    constexpr int NX = MODE == 2 ? 0 : 4;
    PrepRegs R;
    if (w == 0) load_epoch(c);
    if (MODE == 1) { __builtin_amdgcn_s_sleep(SLEEP1); prep_load<true>(c, R); }
    if (w < 2) {
        rsrc_t rgw = mkrsrc(p.gw, D * 2);
        if (w == 0 || lane < 16) dma16(c.ldsb + P0_GW + w * 1024, (w * 64 + lane) * 16, rgw, 0);
    }
    if (w == 3 && lane < 8) *(LAS unsigned*)(lds + L_CNT + lane * 4) = 0;
    issue(0); issue(1);
    const int wunit = c.cb * 3 + w, wT = wunit / 10, wdl = wunit % 10;
    if (MODE == 0) {
        rsrc_t rwd = mkrsrc(p.wd, F * D * 2);
        const unsigned vo = w < 3 ? (lane >> 3) * (D * 2) + (lane & 7) * 16 : 0xF0000000u;
#pragma unroll
        for (int i = 0; i < 4; ++i) dma16(c.ldsb + P0_LDS + w * 4096 + i * 1024, vo, rwd, ((32 * wT + 8 * i) * D + 64 * wdl) * 2);
    }
    if (MODE == 1) {
        waitv<20>();
        prep_run<true>(c, R);
    }
    waitv<10 + NX>(); WAITL0(); bar();
    c.tag = get_tag(c);
    STAMPT(3);
    issue(2);
    const int grw = 8 * c.bf + (lane >> 3), gch = (lane & 7) ^ ((grw >> 1) & 7);
    float gacc = 0.f;
    auto gate_step = [&](int t) {
        const int buf = t >= 9 ? t - 9 : (t >= 6 ? t - 6 : (t >= 3 ? t - 3 : t));
        const u32x4 xv = *(LAS u32x4*)(lds + buf * G1_STAGE + grw * 128 + (lane & 7) * 16);
        const u32x4 gv = *(LAS u32x4*)(lds + P0_GW + t * 128 + gch * 16);
#pragma unroll
        for (int i = 0; i < 4; ++i) gacc = dot2(xv[i], gv[i], gacc);
    };
    if (w == 3) gate_step(0);
    for (int t = 0; t < 7; ++t) {
        if (t == 0) waitv<NX + 10>();
        else if (MODE == 0 && (t == 2 || t == 3)) waitv<14>();
        else waitv<10>();
        WAITL0();
        bar();
        issue(t + 3);
        if (w == 3) gate_step(t + 1);
        if (MODE == 1 && t == 1 && w == 0) prep_publish(c);
        if (MODE == 0 && t == 1) {
            const int kh = wT / 24, tt = (wT % 24) >> 1, kk = wT & 1;
            const int g4 = lane >> 4, q = (lane & 15) >> 2, pp = lane & 3, row = 4 * g4 + q;
            LAS char* buf = lds + P0_LDS + w * 4096;
            rsrc_t rw = mkrsrc(p.ws + WS_WT, F * D * 2);
#pragma unroll
            for (int i = 0; i < 4; ++i) {
                const int d16 = 4 * wdl + i, dsl = d16 / 5, n = d16 % 5;
                const s16x8 f = cat(trread(buf + row * 128 + 32 * i + 8 * pp), trread(buf + (row + 16) * 128 + 32 * i + 8 * pp));
                __builtin_amdgcn_raw_buffer_store_b128(__builtin_bit_cast(u32x4, f), rw, w < 3 ? ((((dsl * 12 + tt) * 4 + kh * 2 + kk) * 5 + n) * 64 + lane) * 16 : 0xF0000000u, 0, 16);
            }
        }
        if (MODE == 0 && t == 4 && w == 0 && lane < 8) st_flag(p.ws, FL_WT + lane * 256 + c.cb, c.tag);
    }
    constexpr int NT = MODE == 0 ? 1 : 0, NF = 1;
    if (MODE == 0) {
        rsrc_t rf = mkrsrc(p.ssk, D * S * 2);
        prefetch_lines(c.ldsb + L_DUMMY, rf, lane < 32 ? (c.cb * 4 + w) * (S * 2) + lane * 128 : 0xF0000000u);
    }
    waitv<10 + NT>(); WAITL0(); bar();
    if (w == 3) gate_step(8);
    {
        rsrc_t rfl_ = mkrsrc(p.ws + WS_FLAG, 32768);
        const unsigned fo = lane < 4 ? FL_KR + (MODE == 0 ? (c.cb >> 5) : 0) * 64 + lane * 4 : FL_WT + c.xcd * 256 + (lane < 44 ? lane - 4 : 0) * 4;
        asm volatile("buffer_load_dwordx4 %0, %1, %2, 0 offen sc1" : "=v"(X.kf4) : "v"(fo * 4), "s"(rfl_) : "memory");
    }
    waitv<NT + NF>(); WAITL0(); bar();
    if (w == 3) gate_step(9);
    WAITL0(); bar();
    WAITV(0);
    TIE(X.kf4);
    {
        const bool ok = X.kf4[0] == c.tag && X.kf4[1] == c.tag && X.kf4[2] == c.tag && X.kf4[3] == c.tag;
        X.kr_ready = __all(ok || lane >= 4);
        X.wt_ready = __all(ok || lane < 4);
    }
    if (w == 3) {
        gacc = dpp_add<0x111, 0xf>(gacc); gacc = dpp_add<0x112, 0xf>(gacc); gacc = dpp_add<0x114, 0xf>(gacc);
        rsrc_t rG = mkrsrc(p.ws + WS_G, S * 4);
        __builtin_amdgcn_raw_buffer_store_b32(__float_as_uint(sigmoidf_fast(gacc)), rG, (lane & 7) == 7 ? (c.bs * 128 + grw) * 4 : 0xF0000000u, 0, 16);
    }
}

__global__ __launch_bounds__(512, 2) void k_fused(Params p) {
    extern __shared__ __attribute__((aligned(16))) char smem[];
    LAS char* lds = (LAS char*)smem;
    const unsigned ldsb = (unsigned)(size_t)lds;
    const int wave = rfl(threadIdx.x >> 6), lane = threadIdx.x & 63;
    const int b = blockIdx.x, xcd = b & 7, idx = b >> 3;
    const bool has_conv = idx < 20;
    const int cb = xcd * 20 + idx, pb = xcd * 12 + idx - 20;
    const int bs = (xcd & 1) * 8 + (idx & 7), bf = (xcd >> 1) * 4 + (idx >> 3);
    const int st = xcd * 4 + (idx & 3), ds = idx >> 2;
    STAMPT(0);
    Ctx c; c.p = p; c.lds = lds; c.ldsb = ldsb; c.lane = lane; c.wave = wave; c.b = b; c.xcd = xcd; c.idx = idx; c.cb = cb; c.pb = pb; c.bs = bs; c.bf = bf;
    c.tag = 0;

    if (wave >= 4) {
        const int w = wave - 4;
        Tail X;
        X.kr_ready = false; X.wt_ready = false;
        if (has_conv) loader_gemm1<0>(c, X);
        else if (pb < 80) loader_gemm1<1>(c, X);
        else loader_gemm1<2>(c, X);
        const unsigned tag = c.tag;
        STAMPT(4);
        const unsigned vo = lane * 16;
        const int d = cb * 4 + w;
        const unsigned sl = ldsb + w * CV_SLOT;
        if (has_conv) {
            rsrc_t rf = mkrsrc(p.ssk, D * S * 2);
#pragma unroll
            for (int i = 0; i < 4; ++i) dma16(sl + 512 + i * 1024, vo, rf, d * (S * 2) + i * 1024);
            LAS char* slot = lds + w * CV_SLOT;
            if (lane < 32) *(LAS u32x4*)(slot + lane * 16) = (u32x4){0, 0, 0, 0};
            else if (lane < 48) *(LAS u32x4*)(slot + CV_KF + ((lane - 32) >> 2) * CV_COPY + 4096 + (lane & 3) * 16) = (u32x4){0, 0, 0, 0};
        }
        rsrc_t rw = mkrsrc(p.ws + WS_WT, F * D * 2);
        if (has_conv) {
            if (!X.kr_ready) poll4<4>(p.ws, FL_KR + (cb >> 5) * 64, tag, lane);
            STAMPT(5);
            rsrc_t r0 = mkrsrc(p.ws + WS_KR0, D * KRS * 2);
#pragma unroll
            for (int i = 0; i < 4; ++i) dma16_sc1(sl + CV_KF + i * 1024, vo, r0, d * (KRS * 2) + i * 1024);
        }
        if (!X.wt_ready) poll4<40>(p.ws, FL_WT + xcd * 256, tag, lane);
        const unsigned sw2 = ds * (48 * 5 * 1024) + w * 5120;
        const int rot = (3 * ds) >> 1;
        auto g2_src = [&](int t) { const int ts = t + rot; return sw2 + (ts >= 12 ? ts - 12 : ts) * G2_STAGE; };
#pragma unroll
        for (int t = 0; t < 3; ++t)
#pragma unroll
            for (int j = 0; j < 5; ++j) dma16_sc1(ldsb + G2_RING + t * G2_STAGE + w * 5120 + j * 1024, vo, rw, g2_src(t) + j * 1024);
        if (has_conv) {
            waitv<15>(); WAITL0();
            LAS char* c0 = lds + w * CV_SLOT + CV_KF;
#pragma unroll
            for (int j = 0; j < 4; ++j) {
                const u32x4 a = *(LAS u32x4*)(c0 + j * 1024 + lane * 16);
                const u32x2 nx = *(LAS u32x2*)(c0 + j * 1024 + lane * 16 + 16);
                *(LAS u32x4*)(c0 + CV_COPY + j * 1024 + lane * 16) =
                    (u32x4){__builtin_amdgcn_alignbit(a[1], a[0], 16), __builtin_amdgcn_alignbit(a[2], a[1], 16), __builtin_amdgcn_alignbit(a[3], a[2], 16), __builtin_amdgcn_alignbit(nx[0], a[3], 16)};
                *(LAS u32x4*)(c0 + 2 * CV_COPY + j * 1024 + lane * 16) = (u32x4){a[1], a[2], a[3], nx[0]};
                *(LAS u32x4*)(c0 + 3 * CV_COPY + j * 1024 + lane * 16) =
                    (u32x4){__builtin_amdgcn_alignbit(a[2], a[1], 16), __builtin_amdgcn_alignbit(a[3], a[2], 16), __builtin_amdgcn_alignbit(nx[0], a[3], 16), __builtin_amdgcn_alignbit(nx[1], nx[0], 16)};
            }
        } else {
            waitv<15>();
        }
        WAITL0();
        bar();
        STAMPT(6);
        {
#pragma unroll
            for (int i = 0; i < 2; ++i) {
                const int ln = (idx & 3) * 480 + (w * 2 + i) * 64 + lane;
                prefetch_lines(ldsb + L_DUMMY, rw, (w * 2 + i) * 64 + lane < 480 ? ds * (48 * 5 * 1024) + ln * 128 : 0xF0000000u);
            }
        }
        if (w == 0) poll4<4>(p.ws, FL_H + (st >> 1) * 64, tag, lane);
        WAITV(0);
        bar();
        STAMPT(7);
        rsrc_t rh = mkrsrc(p.ws + WS_H, S * F * 2);
        const unsigned sh2 = ((st * 2 + (w & 1)) * 96 + (w >> 1) * 48) * 1024;
        auto wslot = [&](int t) { return (t & 3) < 3 ? G2_RING + (t & 3) * G2_STAGE : G2_W3; };
        auto g2_dma = [&](int t) {
            const int ts = t + rot;
            const unsigned src = sh2 + (ts >= 12 ? ts - 12 : ts) * 4096, dst = ldsb + (t & 3) * G2_HST + w * 4096;
#pragma unroll
            for (int j = 0; j < 4; ++j) dma16_sc1(dst + j * 1024, vo, rh, src + j * 1024);
            if (t >= 3) {
#pragma unroll
                for (int j = 0; j < 5; ++j) dma16_sc1(ldsb + wslot(t) + w * 5120 + j * 1024, vo, rw, g2_src(t) + j * 1024);
            }
        };
        constexpr int ND = 9;
        g2_dma(0); g2_dma(1); g2_dma(2); g2_dma(3);
        waitv<8 + ND>(); WAITL0(); bar();
        rsrc_t ry = mkrsrc(p.ws + WS_YT, D * S * 2);
        auto load_y = [&]() {
            u32x4 yv[3];
#pragma unroll
            for (int i = 0; i < 3; ++i) {
                const int it = w + 4 * i, ch = 8 * it + (lane >> 3);
                yv[i] = __builtin_amdgcn_raw_buffer_load_b128(ry, it < 10 ? ((80 * ds + ch) * S + 64 * st + (lane & 7) * 8) * 2 : 0xF0000000u, 0, 16);
            }
#pragma unroll
            for (int i = 0; i < 3; ++i) {
                const int it = w + 4 * i, ch = 8 * it + (lane >> 3);
                if (it < 10) *(LAS u32x4*)(lds + G2_Y + ch * 128 + (lane & 7) * 16) = yv[i];
            }
        };
        waitv<4 + ND>(); bar();
        waitv<2 * ND - ND>(); bar(); g2_dma(4);
        waitv<ND>(); WAITL0(); bar(); g2_dma(5);
        waitv<ND>(); WAITL0(); bar(); g2_dma(6);
        waitv<ND>(); WAITL0(); bar(); g2_dma(7);
        waitv<ND>(); WAITL0(); bar(); g2_dma(8);
        waitv<ND>(); WAITL0(); bar(); g2_dma(9);
        waitv<ND>(); WAITL0(); bar(); g2_dma(10);
        waitv<ND>(); WAITL0(); bar(); g2_dma(11);
        u32x4 yf = {0, 0, 0, 0};
        if (w == 0) {
            rsrc_t rfl_ = mkrsrc(p.ws + WS_FLAG, 32768);
            asm volatile("buffer_load_dwordx4 %0, %1, %2, 0 offen sc1" : "=v"(yf) : "v"((FL_Y + ds * 64 + (lane < 5 ? lane : 0) * 4) * 4), "s"(rfl_) : "memory");
            waitv<ND + 1>();
        } else {
            waitv<ND>();
        }
        WAITL0(); bar();
        WAITV(0);
        if (w == 0) {
            TIE(yf);
            if (__all(yf[0] == tag && yf[1] == tag && yf[2] == tag && yf[3] == tag) && lane == 0) lds_set(lds, L_CNT + 12, 1u);
        }
        WAITL0(); bar();
        const bool yearly = rfl(*(volatile LAS unsigned*)(lds + L_CNT + 12)) == 1u;
        if (yearly) load_y();
        bar();
        if (!yearly) {
            if (w == 0) { poll4<5>(p.ws, FL_Y + ds * 64, tag, lane); if (lane == 0) lds_set(lds, L_CNT + 12, 2u); }
            else lds_wait(lds, L_CNT + 12, 2u);
            load_y();
        }
        bar();
        WAITL0();
        bar();
        if (b == 0 && w == 0) {
            rsrc_t rfl_ = mkrsrc(p.ws + WS_FLAG, 32768);
            const unsigned fo = (FL_H + (lane >> 2) * 64 + (lane & 3) * 4) * 4;
            for (int spins = 0; spins < (1 << 18); ++spins) {
                u32x4 x;
                asm volatile("buffer_load_dwordx4 %0, %1, %2, 0 offen sc1\n\ts_waitcnt vmcnt(0)" : "=&v"(x) : "v"(fo), "s"(rfl_) : "memory");
                if (__all(x[0] == tag && x[1] == tag && x[2] == tag && x[3] == tag)) break;
                __builtin_amdgcn_s_sleep(4);
            }
            if (lane == 0) st_flag(p.ws, FL_EPOCH, tag);
        }
        STAMPT(8);
    } else {
        unsigned tag = 0;
        float Dd = bflo(p.sd[(has_conv ? cb * 4 : 0) + wave]);
        asm volatile("" : "+v"(Dd));
        {
            const int wsx = wave & 1, wf = wave >> 1;
            const int g = lane >> 4, fr = lane & 15, q = (lane & 15) >> 2, pp = lane & 3;
            int oa[3];
#pragma unroll
            for (int i = 0; i < 3; ++i) {
                const int ft = 3 * wf + i, c = ft >> 1, half = ft & 1;
                oa[i] = 16384 + (g * 3 + c) * 512 + q * 64 + 16 * ((2 * half + (pp >> 1)) ^ (2 * (g & 1))) + 8 * (pp & 1);
            }
            const int ob0 = (64 * wsx + fr) * 128 + ((g ^ (fr >> 1)) * 16);
            const int ob1 = (64 * wsx + fr) * 128 + ((g ^ (fr >> 1) ^ 4) * 16);
            f32x4 accg[3][4], accu[3][4];
#pragma unroll
            for (int i = 0; i < 3; ++i)
#pragma unroll
                for (int m = 0; m < 4; ++m) { accg[i][m] = (f32x4){0, 0, 0, 0}; accu[i][m] = (f32x4){0, 0, 0, 0}; }
            struct Fr { s16x8 a[6]; s16x8 b[4]; } fa, fb;
            auto rd = [&](Fr& f, int ks, int sb) {
#pragma unroll
                for (int m = 0; m < 4; ++m) f.b[m] = *(LAS s16x8*)(lds + sb + (ks ? ob1 : ob0) + m * 2048);
#pragma unroll
                for (int i = 0; i < 3; ++i) {
                    f.a[i] = cat(trread(lds + sb + oa[i] + ks * 6144), trread(lds + sb + oa[i] + ks * 6144 + 256));
                    f.a[3 + i] = cat(trread(lds + sb + oa[i] + ks * 6144 + 12288), trread(lds + sb + oa[i] + ks * 6144 + 256 + 12288));
                }
            };
            auto mm = [&](const Fr& f) {
#pragma unroll
                for (int i = 0; i < 3; ++i)
#pragma unroll
                    for (int m = 0; m < 4; ++m) {
                        accg[i][m] = mfma16(f.a[i], f.b[m], accg[i][m]);
                        accu[i][m] = mfma16(f.a[3 + i], f.b[m], accu[i][m]);
                    }
            };
            auto interleave = [&]() {
#pragma unroll
                for (int i = 0; i < 16; ++i) { SGB(0x008, 1); SGB(0x100, 1); }
                SGB(0x008, 8);
                SB0();
            };
            const bool kvblock = !has_conv && pb < 80;
            const int pi = kvblock ? pb : (has_conv ? cb : 160 + pb - 80), np = kvblock ? 80 : 176, t0 = kvblock ? 6 : 2;
            auto pf = [&]() {
                rsrc_t rpf = mkrsrc(wave == 0 ? p.q : (wave == 1 ? p.wg : p.wu), wave == 0 ? S * D * 2 : D * F * 2);
                const int nl = wave == 0 ? 8192 : 6144;
                for (int l0 = pi * 64; l0 < nl && wave < 3; l0 += np * 64) {
                    const int l = l0 + lane;
                    prefetch_lines(ldsb + L_DUMMY, rpf, wave == 0 ? (l >> 2) * 1280 + (t0 + (l & 3)) * 128 : (t0 * 64 + l / 24) * 3072 + (l % 24) * 128);
                }
            };
            if (kvblock) {
                PrepRegs R;
                __builtin_amdgcn_s_sleep(SLEEP1);
                prep_load<true>(c, R); pf(); WAITV(0); prep_run<true>(c, R);
            } else {
                __builtin_amdgcn_s_sleep(SLEEP0);
                pf();
            }
            __builtin_amdgcn_s_setprio(1);
            bar();
            STAMPT(3);
#define RSTAMP1() do {} while (0)
            rd(fa, 0, 0);
            int sb = 0;
            for (int t = 0; t < 10; ++t) {
                rd(fb, 1, sb); mm(fa); interleave();
                WAITL0();
                if (t == 1 && kvblock) WAITV(0);
                RSTAMP1();
                bar();
                RSTAMP1();
                sb = (sb == 2 * G1_STAGE) ? 0 : sb + G1_STAGE;
                if (t < 9) rd(fa, 0, sb);
                mm(fb); interleave();
            }
            STAMPT(4);
            WAITV(0);
            tag = get_tag(c);
            rsrc_t rh = mkrsrc(p.ws + WS_H, S * F * 2);
#pragma unroll
            for (int mp = 0; mp < 2; ++mp)
#pragma unroll
                for (int i = 0; i < 3; ++i) {
                    float hv[8];
#pragma unroll
                    for (int e = 0; e < 8; ++e) hv[e] = accu[i][2 * mp + (e >> 2)][e & 3] * sigmoidf_fast(accg[i][2 * mp + (e >> 2)][e & 3]);
                    const u32x4 o = {cvtpk(hv[0], hv[1]), cvtpk(hv[2], hv[3]), cvtpk(hv[4], hv[5]), cvtpk(hv[6], hv[7])};
                    const int sp = bs * 4 + wsx * 2 + mp, ft = bf * 6 + wf * 3 + i;
                    __builtin_amdgcn_raw_buffer_store_b128(o, rh, ((sp * 96 + ft) * 64 + lane) * 16, 0, 16);
                }
        }
        STAMPT(5);
        auto publish_h = [&]() {
            WAITV(0);
            unsigned old = 0;
            if (lane == 0) old = __hip_atomic_fetch_add((LAS unsigned*)(lds + L_CNT), 1u, __ATOMIC_RELAXED, __HIP_MEMORY_SCOPE_WORKGROUP);
            if (lane == 0 && old == 3) st_flag(p.ws, FL_H + bs * 64 + bf, tag);
        };
        bar();
        STAMPT(6);
        f32x4 cacc[8];
        u32x2 ckv[8];
        const int ca = lane & 15, cg = lane >> 4;
        if (has_conv) {
            LAS char* slot = lds + wave * CV_SLOT;
#pragma unroll
            for (int n = 0; n < 8; ++n) cacc[n] = (f32x4){0, 0, 0, 0};
            unsigned pg = (unsigned)(size_t)slot + 480 + 32 * ca + 16 * cg;
            unsigned pf = (unsigned)(size_t)slot + CV_KF + (3 - (ca & 3)) * CV_COPY + 472 + 16 * cg - 8 * (ca >> 2);
            struct Res { s16x8 G[8]; s16x4 Fl[8], Fh[8]; } RA, RB;
            auto rd1 = [&](Res& R, int k) {
                const int j = k / 3, m = k % 3;
                if (m == 0) asm volatile("ds_read_b64 %0, %1 offset:%2" : "=v"(R.Fl[j]) : "v"(pf), "n"(512 * (7 - j)));
                else if (m == 1) asm volatile("ds_read_b64 %0, %1 offset:%2" : "=v"(R.Fh[j]) : "v"(pf), "n"(512 * (7 - j) + 8));
                else asm volatile("ds_read_b128 %0, %1 offset:%2" : "=v"(R.G[j]) : "v"(pg), "n"(512 * j));
            };
            auto land = [&](Res& R) {
                asm volatile("s_waitcnt lgkmcnt(0)"
                             : "+v"(R.G[0]), "+v"(R.G[1]), "+v"(R.G[2]), "+v"(R.G[3]), "+v"(R.G[4]), "+v"(R.G[5]), "+v"(R.G[6]), "+v"(R.G[7]),
                               "+v"(R.Fl[0]), "+v"(R.Fl[1]), "+v"(R.Fl[2]), "+v"(R.Fl[3]), "+v"(R.Fl[4]), "+v"(R.Fl[5]), "+v"(R.Fl[6]), "+v"(R.Fl[7]));
                asm volatile(""
                             : "+v"(R.Fh[0]), "+v"(R.Fh[1]), "+v"(R.Fh[2]), "+v"(R.Fh[3]), "+v"(R.Fh[4]), "+v"(R.Fh[5]), "+v"(R.Fh[6]), "+v"(R.Fh[7]));
                SB0();
            };
            auto residue = [&](Res& R, Res& N) {
                pg -= 64; pf -= 64;
                int k = 0;
#pragma unroll
                for (int I = 0; I < 8; ++I) {
                    const s16x8 Fv = cat(R.Fl[I], R.Fh[I]);
#pragma unroll
                    for (int n = I; n < 8; ++n) {
                        cacc[n] = mfma16(Fv, R.G[n - I], cacc[n]);
                        if (k < 24) rd1(N, k);
                        ++k;
                        SB0();
                    }
                }
                land(N);
            };
#pragma unroll
            for (int k = 0; k < 24; ++k) rd1(RA, k);
            land(RA);
            for (int r2 = 0; r2 < 4; ++r2) {
                residue(RA, RB);
                residue(RB, RA);
                if (r2 == 0) publish_h();
            }
#pragma unroll
            for (int n = 0; n < 8; ++n) ckv[n] = *(LAS u32x2*)(slot + CV_KF + 2 * (2044 - 256 * n - 16 * ca - 4 * cg));
        } else {
            publish_h();
        }
        STAMPT(7);
        {
            const int sp = wave & 1, kh = wave >> 1;
            WAITL0();
            bar();
            STAMPT(8);
            SB0();
            if (has_conv) {
                const int d = cb * 4 + wave;
                rsrc_t ry = mkrsrc(p.ws + WS_YT, D * S * 2);
                u32x2 o[8];
#pragma unroll
                for (int n = 0; n < 8; ++n) {
                    o[n][0] = cvtpk(cacc[n][0] + bfhi(ckv[n][1]) * Dd, cacc[n][1] + bflo(ckv[n][1]) * Dd);
                    o[n][1] = cvtpk(cacc[n][2] + bfhi(ckv[n][0]) * Dd, cacc[n][3] + bflo(ckv[n][0]) * Dd);
                }
#pragma unroll
                for (int n = 0; n < 8; n += 2) {
                    auto x = __builtin_amdgcn_permlane16_swap(o[n][0], o[n + 1][0], false, false);
                    auto y = __builtin_amdgcn_permlane16_swap(o[n][1], o[n + 1][1], false, false);
                    const u32x4 val = {x[0], y[0], x[1], y[1]};
                    const int s = 256 * (n + (cg & 1)) + 16 * ca + 4 * (cg & 2);
                    __builtin_amdgcn_raw_buffer_store_b128(val, ry, (d * S + s) * 2, 0, 16);
                }
            }
            SB0();
            STAMPT(9);
            const int s16 = lane & 15, g = lane >> 4, q = s16 >> 2, pp = lane & 3;
            const int srow = 64 * st + 32 * sp + 16 * kh + s16;
            const float gt = __hip_atomic_load((const GAS float*)(p.ws + WS_G) + srow, RLX_AGENT);
            u32x2 qv[5];
#pragma unroll
            for (int n = 0; n < 5; ++n) qv[n] = *(const u32x2*)(p.q + (size_t)srow * D + 80 * ds + 16 * n + 4 * g);
            f32x4 acc[2][5];
#pragma unroll
            for (int n = 0; n < 5; ++n) { acc[0][n] = (f32x4){0, 0, 0, 0}; acc[1][n] = (f32x4){0, 0, 0, 0}; }
            const unsigned pw = ldsb + kh * 10240 + lane * 16, ph = ldsb + wave * 4096 + lane * 16;
            struct Fr2 { s16x8 w[5]; u32x4 h[2]; } fa, fb;
            auto rd2 = [&](Fr2& f, int s4, int kk) {
                LAS char* bh = (LAS char*)(size_t)(ph + s4 * G2_HST + kk * 2048);
                LAS char* bw = (LAS char*)(size_t)(pw + (s4 < 3 ? G2_RING + s4 * G2_STAGE : G2_W3) + kk * 5120);
                f.h[0] = *(LAS u32x4*)bh; f.h[1] = *(LAS u32x4*)(bh + 1024);
#pragma unroll
                for (int n = 0; n < 5; ++n) f.w[n] = *(LAS s16x8*)(bw + n * 1024);
            };
            auto swp = [&](Fr2& f) {
                unsigned a2 = f.h[0][2], a3 = f.h[0][3], b0 = f.h[1][0], b1 = f.h[1][1];
                asm volatile("v_swap_b32 %0, %1\n\tv_swap_b32 %2, %3\n\ts_nop 1" : "+v"(a2), "+v"(b0), "+v"(a3), "+v"(b1));
                f.h[0][2] = a2; f.h[0][3] = a3; f.h[1][0] = b0; f.h[1][1] = b1;
            };
            auto step = [&](const Fr2& f) {
                const s16x8 h0 = __builtin_bit_cast(s16x8, f.h[0]), h1 = __builtin_bit_cast(s16x8, f.h[1]);
#pragma unroll
                for (int n = 0; n < 5; ++n) { acc[0][n] = mfma16(f.w[n], h0, acc[0][n]); acc[1][n] = mfma16(f.w[n], h1, acc[1][n]); }
            };
            auto il2 = [&]() {
#pragma unroll
                for (int i = 0; i < 7; ++i) { SGB(0x008, 1); SGB(0x100, 1); }
                SGB(0x008, 3);
                SB0();
            };
            STAMPT(1);
            bar();
            STAMPT(2);
#define RSTAMP() do {} while (0)
            rd2(fa, 0, 0);
            swp(fa);
            for (int t = 0; t < 12; ++t) {
                rd2(fb, t & 3, 1);
                step(fa);
                il2();
                swp(fb);
                SB0();
                if (t == 4) WAITV(0);
                RSTAMP();
                bar();
                RSTAMP();
                if (t == 4 && has_conv && wave == 0 && lane == 0) st_flag(p.ws, FL_Y + (cb / 20) * 64 + cb % 20, tag);
                rd2(fa, (t + 1) & 3, 0);
                step(fb);
                il2();
                swp(fa);
                SB0();
            }
            WAITL0();
            bar();
            STAMPT(10);
#pragma unroll
            for (int n = 0; n < 5; ++n) *(LAS f32x4*)(lds + G2_RED + (wave * 5 + n) * 1024 + lane * 16) = kh ? acc[0][n] : acc[1][n];
            WAITL0();
            bar();
            STAMPT(11);
#pragma unroll
            for (int n = 0; n < 5; ++n) {
                const f32x4 o4 = (kh ? acc[1][n] : acc[0][n]) + *(LAS f32x4*)(lds + G2_RED + ((wave ^ 2) * 5 + n) * 1024 + lane * 16);
                const u32x2 yv = __builtin_bit_cast(u32x2, trread(lds + G2_Y + (16 * n + 4 * g + q) * 128 + (32 * sp + 16 * kh + 4 * pp) * 2));
                const float o0 = gt * (bflo(qv[n][0]) * bflo(yv[0])) + (1.f - gt) * o4[0];
                const float o1 = gt * (bfhi(qv[n][0]) * bfhi(yv[0])) + (1.f - gt) * o4[1];
                const float o2 = gt * (bflo(qv[n][1]) * bflo(yv[1])) + (1.f - gt) * o4[2];
                const float o3 = gt * (bfhi(qv[n][1]) * bfhi(yv[1])) + (1.f - gt) * o4[3];
                *(u32x2*)(p.out + (size_t)srow * D + 80 * ds + 16 * n + 4 * g) = (u32x2){cvtpk(o0, o1), cvtpk(o2, o3)};
            }
            STAMPT(12);
        }
    }
}

extern "C" void kernel_launch(void* const* d_in, const int* in_sizes, int n_in,
                              void* d_out, int out_size, void* d_ws, size_t ws_size,
                              hipStream_t stream) {
    Params p;
    p.q = (const u16*)d_in[0]; p.k = (const u16*)d_in[1]; p.v = (const u16*)d_in[2];
    p.sk = (const u16*)d_in[3]; p.ssk = (const u16*)d_in[4]; p.sd = (const u16*)d_in[5];
    p.wg = (const u16*)d_in[7]; p.wu = (const u16*)d_in[8]; p.wd = (const u16*)d_in[9]; p.gw = (const u16*)d_in[10];
    p.out = (u16*)d_out; p.ws = (char*)d_ws;
    hipFuncSetAttribute((const void*)k_fused, hipFuncAttributeMaxDynamicSharedMemorySize, LDS_BYTES);
    k_fused<<<256, 512, LDS_BYTES, stream>>>(p);
}
```

```cpp
#include <hip/hip_runtime.h>
#include <hip/hip_bf16.h>
#include <stdint.h>

typedef unsigned short u16;
typedef short s16x4 __attribute__((ext_vector_type(4)));
typedef short s16x8 __attribute__((ext_vector_type(8)));
typedef __bf16 b16x8 __attribute__((ext_vector_type(8)));
typedef __bf16 b16x2 __attribute__((ext_vector_type(2)));
typedef float f32x2 __attribute__((ext_vector_type(2)));
typedef float f32x4 __attribute__((ext_vector_type(4)));
typedef unsigned u32x2 __attribute__((ext_vector_type(2)));
typedef unsigned u32x4 __attribute__((ext_vector_type(4)));
#define LAS __attribute__((address_space(3)))
#define GAS __attribute__((address_space(1)))
#define DI __device__ __forceinline__

static constexpr int S = 2048, D = 640, F = 1536;
static constexpr int KRS = 2112;
static constexpr unsigned WS_H = 0, WS_KR = 8u << 20, WS_YT = 14u << 20, WS_G = 17u << 20, WS_WT = 18u << 20, WS_STAMP = 20u << 20, WS_FLAG = 24u << 20;
static constexpr unsigned FL_EPOCH = 0, FL_KR = 1024, FL_WT = 2048  , FL_H = 4096, FL_Y = 6144;
static constexpr unsigned OOB = 0xF0000000u;

struct Params {
    const u16 *q, *k, *v, *sk, *ssk, *sd, *wg, *wu, *wd, *gw;
    u16* out; char* ws;
};

DI float bflo(unsigned x) { return __uint_as_float(x << 16); }
DI float bfhi(unsigned x) { return __uint_as_float(x & 0xffff0000u); }
DI unsigned cvtpk(float lo, float hi) { f32x2 v = {lo, hi}; b16x2 b = __builtin_convertvector(v, b16x2); return __builtin_bit_cast(unsigned, b); }
DI unsigned rfl(unsigned x) { return __builtin_amdgcn_readfirstlane(x); }
DI float sigmoidf(float x) { return __builtin_amdgcn_rcpf(1.f + __builtin_amdgcn_exp2f(x * -1.44269504089f)); }
DI f32x4 mfma16(s16x8 a, s16x8 b, f32x4 c) {
    return __builtin_amdgcn_mfma_f32_16x16x32_bf16(__builtin_bit_cast(b16x8, a), __builtin_bit_cast(b16x8, b), c, 0, 0, 0);
}
DI s16x4 trread(LAS char* p) { return __builtin_amdgcn_ds_read_tr16_b64_v4i16((LAS s16x4*)p); }
DI s16x8 cat(s16x4 a, s16x4 b) { return __builtin_shufflevector(a, b, 0, 1, 2, 3, 4, 5, 6, 7); }
typedef __amdgpu_buffer_rsrc_t rsrc_t;
DI rsrc_t mkrsrc(const void* p, unsigned bytes) { return __builtin_amdgcn_make_buffer_rsrc((void*)p, 0, bytes, 0x00020000); }
DI void dma16(unsigned m0v, unsigned voff, rsrc_t r, unsigned soff) {
    asm volatile("s_mov_b32 m0, %0\n\ts_nop 0\n\tbuffer_load_dwordx4 %1, %2, %3 offen lds" ::"s"(m0v), "v"(voff), "s"(r), "s"(soff) : "m0", "memory");
}
DI void dma16_sc1(unsigned m0v, unsigned voff, rsrc_t r, unsigned soff) {
    asm volatile("s_mov_b32 m0, %0\n\ts_nop 0\n\tbuffer_load_dwordx4 %1, %2, %3 offen sc1 lds" ::"s"(m0v), "v"(voff), "s"(r), "s"(soff) : "m0", "memory");
}
DI u32x4 aload(rsrc_t r, unsigned voff) { u32x4 x; asm volatile("buffer_load_dwordx4 %0, %1, %2, 0 offen" : "=v"(x) : "v"(voff), "s"(r) : "memory"); return x; }
DI u32x4 aload_sc1(rsrc_t r, unsigned voff) { u32x4 x; asm volatile("buffer_load_dwordx4 %0, %1, %2, 0 offen sc1" : "=v"(x) : "v"(voff), "s"(r) : "memory"); return x; }
DI void prefetch_lines(unsigned scratch, rsrc_t r, unsigned voff) {
    asm volatile("s_mov_b32 m0, %0\n\ts_nop 0\n\tbuffer_load_dword %1, %2, 0 offen lds" ::"s"(scratch), "v"(voff), "s"(r) : "m0", "memory");
}
template <int N> DI void waitv() { asm volatile("s_waitcnt vmcnt(%0)" ::"n"(N) : "memory"); }
DI float dot2(unsigned a, unsigned b, float c) { return __builtin_amdgcn_fdot2_f32_bf16(__builtin_bit_cast(b16x2, a), __builtin_bit_cast(b16x2, b), c, false); }
template <int CTRL> DI float dpp_add(float x) { return x + __int_as_float(__builtin_amdgcn_update_dpp(0, __float_as_int(x), CTRL, 0xf, 0xf, true)); }
#define TIE(x) asm volatile("" : "+v"(x))
#define WAITL0() asm volatile("s_waitcnt lgkmcnt(0)" ::: "memory")
DI void bar() { asm volatile("" ::: "memory"); __builtin_amdgcn_s_barrier(); asm volatile("" ::: "memory"); }
#define SGB(m, n) __builtin_amdgcn_sched_group_barrier(m, n, 0)
#define SB0() __builtin_amdgcn_sched_barrier(0)
#define RLX_AGENT __ATOMIC_RELAXED, __HIP_MEMORY_SCOPE_AGENT

#define SLEEP_L 0
#define SLEEP_LK 12
#define SLEEP_C 12

DI void st_flag(char* ws, unsigned word, unsigned tag) { __hip_atomic_store((GAS unsigned*)(ws + WS_FLAG) + word, tag, RLX_AGENT); }
DI rsrc_t flag_rsrc(char* ws) { return mkrsrc(ws + WS_FLAG, 32768); }
DI bool all_tag(u32x4 x, unsigned tag) { return x[0] == tag && x[1] == tag && x[2] == tag && x[3] == tag; }
template <int NL>
DI void poll4(char* ws, unsigned word, unsigned tag, int lane, unsigned care = 15u) {
    rsrc_t r = flag_rsrc(ws);
    for (int spins = 0; spins < (1 << 18); ++spins) {
        u32x4 x;
        asm volatile("buffer_load_dwordx4 %0, %1, %2, 0 offen sc1\n\ts_waitcnt vmcnt(0)" : "=&v"(x) : "v"((word + (lane < NL ? lane : 0) * 4) * 4), "s"(r) : "memory");
        const bool ok = (x[0] == tag || !(care & 1)) && (x[1] == tag || !(care & 2)) && (x[2] == tag || !(care & 4)) && (x[3] == tag || !(care & 8));
        if (__all(ok)) return;
        __builtin_amdgcn_s_sleep(4);
    }
}
DI void lds_set(LAS char* lds, int off, unsigned v) { __hip_atomic_store((LAS unsigned*)(lds + off), v, __ATOMIC_RELAXED, __HIP_MEMORY_SCOPE_WORKGROUP); }
DI unsigned lds_get(LAS char* lds, int off) { return rfl(__hip_atomic_load((LAS unsigned*)(lds + off), __ATOMIC_RELAXED, __HIP_MEMORY_SCOPE_WORKGROUP)); }
DI unsigned lds_wait(LAS char* lds, int off) {
    unsigned v = 0;
    for (int spins = 0; spins < (1 << 22); ++spins) {
        v = lds_get(lds, off);
        if (v) break;
        __builtin_amdgcn_s_sleep(8);
    }
    asm volatile("" ::: "memory");
    return v;
}
DI bool lds_last(LAS char* lds, int off, unsigned n, int lane) {
    unsigned old = 0;
    if (lane == 0) old = __hip_atomic_fetch_add((LAS unsigned*)(lds + off), 1u, __ATOMIC_RELAXED, __HIP_MEMORY_SCOPE_WORKGROUP);
    return rfl(old) == n - 1;
}

#define STAMPT(i) do {} while (0)

constexpr int G1_STAGE = 40960;
constexpr int P0_LDS = 122880;
constexpr int TP = 272;
constexpr int P0_T = P0_LDS, P0_GW = P0_LDS + 128 * TP;
constexpr int CV_SLOT = 21248, CV_COPY = 4160, CV_KF = 4608;
constexpr int G2_WST = 20480, G2_HST = 16384;
constexpr int G2_WA = 86016, G2_HA = G2_WA + 2 * G2_WST;
constexpr int G2_WB = 0, G2_HB = 2 * G2_WST;
constexpr int G2_Y = G2_HB + 2 * G2_HST;
constexpr int G2_RED = 0;
constexpr int L_DUMMY = 163328;
constexpr int L_CNT = 163776;
constexpr int L_EPOCH = 163824;
constexpr int LDS_BYTES = 163840;

struct Ctx {
    Params p; LAS char* lds; unsigned ldsb; int lane, wave, xcd, cb, pb, bs, bf; unsigned tag;
};
DI void load_epoch(const Ctx& c) {
    rsrc_t r = mkrsrc(c.p.ws + WS_FLAG, 64);
    if (c.lane == 0) asm volatile("s_mov_b32 m0, %0\n\ts_nop 0\n\tbuffer_load_dword %1, %2, 0 offen sc1 lds" ::"s"(c.ldsb + L_EPOCH), "v"(0u), "s"(r) : "m0", "memory");
}
DI unsigned get_tag(const Ctx& c) { return lds_get(c.lds, L_EPOCH) + 1u; }

DI void kv_prepare(const Ctx& c) {
    const Params& p = c.p;
    LAS char* lds = c.lds;
    const int lane = c.lane, wave = c.wave;
    (void)wave;
    const int kdc = c.pb % 5, s0 = (c.pb / 5) * 128, d0 = kdc * 128, tid = c.wave * 64 + lane, c16 = tid & 15, rg = tid >> 4, s = s0 + 8 * rg;
    u32x4 kr[11], vr[8], w4[4];
    {
        rsrc_t rk = mkrsrc(p.k, S * D * 2), rv = mkrsrc(p.v, S * D * 2), rsk = mkrsrc(p.sk, D * 8);
#pragma unroll
        for (int i = 0; i < 11; ++i) kr[i] = aload(rk, (unsigned)(((s - 3 + i) * D + d0 + 8 * c16) * 2));
#pragma unroll
        for (int i = 0; i < 8; ++i) vr[i] = aload(rv, ((s + i) * D + d0 + 8 * c16) * 2);
#pragma unroll
        for (int i = 0; i < 4; ++i) w4[i] = aload(rsk, (d0 + 8 * c16) * 8 + i * 16);
    }
    waitv<0>();
    STAMPT(2);
#pragma unroll
    for (int i = 0; i < 11; ++i) TIE(kr[i]);
#pragma unroll
    for (int i = 0; i < 8; ++i) TIE(vr[i]);
#pragma unroll
    for (int i = 0; i < 4; ++i) TIE(w4[i]);
    LAS char* tw = lds + P0_T + 8 * c16 * TP + (((15 - rg) ^ c16) * 16);
#pragma unroll
    for (int e = 0; e < 8; ++e) {
        const unsigned sel = (e & 1) ? 0x07060302u : 0x05040100u;
        const unsigned wa = w4[e >> 1][(e & 1) * 2], wb = w4[e >> 1][(e & 1) * 2 + 1];
        unsigned pr[10];
#pragma unroll
        for (int i = 0; i < 10; ++i) pr[i] = __builtin_amdgcn_perm(kr[i][e >> 1], kr[i + 1][e >> 1], sel);
        float o[8];
#pragma unroll
        for (int r = 0; r < 8; ++r) o[r] = dot2(pr[r + 2], wa, dot2(pr[r], wb, 0.f)) * ((e & 1) ? bfhi(vr[r][e >> 1]) : bflo(vr[r][e >> 1]));
        *(LAS u32x4*)(tw + e * TP) = (u32x4){cvtpk(o[7], o[6]), cvtpk(o[5], o[4]), cvtpk(o[3], o[2]), cvtpk(o[1], o[0])};
    }
    STAMPT(13);
    WAITL0(); bar();
    rsrc_t r0 = mkrsrc(p.ws + WS_KR, D * KRS * 2);
#pragma unroll
    for (int it = 0; it < 8; ++it) {
        const int item = tid + it * 256, ch = item >> 4, j = item & 15;
        __builtin_amdgcn_raw_buffer_store_b128(*(LAS u32x4*)(lds + P0_T + ch * TP + ((j ^ ((ch >> 3) & 15)) * 16)), r0, ((d0 + ch) * KRS + (1920 - s0) + 8 * j) * 2, 0, 16);
    }
    STAMPT(14);
    waitv<0>();
    if (lds_last(lds, L_CNT + 16, 4, lane) && lane == 0) st_flag(p.ws, FL_KR + kdc * 64 + c.pb / 5, get_tag(c));
    STAMPT(15);
}

struct Tail { u32x4 wd[4]; bool kr_ready; float gate; };
template <int MODE>
DI void loader_gemm1(Ctx& c, Tail& X) {
    const Params& p = c.p;
    const int lane = c.lane, w = c.wave - 4, wave = c.wave;
    (void)wave;
    LAS char* lds = c.lds;
    rsrc_t rq = mkrsrc(p.q, S * D * 2), rg_ = mkrsrc(p.wg, D * F * 2), ru_ = mkrsrc(p.wu, D * F * 2);
    unsigned vx[2], vw[3];
    {
        const int r = lane >> 3, slot = lane & 7;
        vx[0] = r * 1280 + ((slot ^ (r >> 1)) * 16);
        vx[1] = r * 1280 + ((slot ^ (r >> 1) ^ 4) * 16);
#pragma unroll
        for (int j = 0; j < 3; ++j) {
            const int L = 2 * j + (lane >> 5), kbl = L / 3, cc = L % 3, row = (lane & 31) >> 2, ch = (lane & 3) ^ (2 * kbl);
            vw[j] = (kbl * 8 + row) * 3072 + cc * 64 + ch * 16;
        }
    }
    const unsigned sx0 = (c.bs * 128 + 32 * w) * 1280, sw0 = 16 * w * 3072 + c.bf * 192;
    auto issue = [&](int t) {
        const unsigned base = c.ldsb + (t >= 9 ? t - 9 : (t >= 6 ? t - 6 : (t >= 3 ? t - 3 : t))) * G1_STAGE;
#pragma unroll
        for (int i = 0; i < 4; ++i) dma16(base + (32 * w + 8 * i) * 128, vx[i & 1], rq, sx0 + i * 8 * 1280 + t * 128);
#pragma unroll
        for (int j = 0; j < 3; ++j) dma16(base + 16384 + w * 3072 + j * 1024, vw[j], rg_, sw0 + t * 64 * 3072);
#pragma unroll
        for (int j = 0; j < 3; ++j) dma16(base + 28672 + w * 3072 + j * 1024, vw[j], ru_, sw0 + t * 64 * 3072);
    };
    if (w == 0) load_epoch(c);
    if (w < 2) {
        rsrc_t rgw = mkrsrc(p.gw, D * 2);
        if (w == 0 || lane < 16) dma16(c.ldsb + P0_GW + w * 1024, (w * 64 + lane) * 16, rgw, 0);
    }
    if (w == 3 && lane < 8) *(LAS unsigned*)(lds + L_CNT + lane * 4) = 0;
    if (MODE == 1) {
        waitv<0>();
        __builtin_amdgcn_s_sleep(SLEEP_LK);
        issue(0); issue(1);
        WAITL0(); bar();
    } else {
        __builtin_amdgcn_s_sleep(SLEEP_L);
        issue(0); issue(1);
    }
    waitv<10>(); WAITL0(); bar();
    c.tag = get_tag(c);
    STAMPT(3);
    issue(2);
    const int grw = 8 * c.bf + (lane >> 3), gch = (lane & 7) ^ ((grw >> 1) & 7);
    float gacc = 0.f;
    auto gate_step = [&](int t) {
        const int buf = t >= 9 ? t - 9 : (t >= 6 ? t - 6 : (t >= 3 ? t - 3 : t));
        const u32x4 xv = *(LAS u32x4*)(lds + buf * G1_STAGE + grw * 128 + (lane & 7) * 16);
        const u32x4 gv = *(LAS u32x4*)(lds + P0_GW + t * 128 + gch * 16);
#pragma unroll
        for (int i = 0; i < 4; ++i) gacc = dot2(xv[i], gv[i], gacc);
    };
    if (w == 3) gate_step(0);
    for (int t = 0; t < 7; ++t) {
        waitv<10>(); WAITL0(); bar();
        issue(t + 3);
        if (w == 3) gate_step(t + 1);
    }
    constexpr int NT = MODE == 0 ? 5 : 0, NF = MODE == 0 ? 1 : 0;
    u32x4 kf4 = {0, 0, 0, 0};
    if (MODE == 0) {
        rsrc_t rf = mkrsrc(p.ssk, D * S * 2), rwd = mkrsrc(p.wd, F * D * 2);
        prefetch_lines(c.ldsb + L_DUMMY, rf, lane < 32 ? (c.cb * 4 + w) * (S * 2) + lane * 128 : OOB);
        const int unit = c.cb * 3 + w, T = unit / 10, dl = unit % 10;
#pragma unroll
        for (int i = 0; i < 4; ++i) X.wd[i] = aload(rwd, w < 3 ? ((32 * T + 8 * i + (lane >> 3)) * D + 64 * dl + (lane & 7) * 8) * 2 : OOB);
    }
    waitv<10 + NT>(); WAITL0(); bar();
    if (w == 3) gate_step(8);
    if (MODE == 0) kf4 = aload_sc1(flag_rsrc(p.ws), (FL_KR + (c.cb >> 5) * 64 + (lane & 3) * 4) * 4);
    waitv<NT + NF>(); WAITL0(); bar();
    if (w == 3) gate_step(9);
    WAITL0(); bar();
    waitv<0>();
    if (MODE == 0) {
        TIE(kf4);
#pragma unroll
        for (int i = 0; i < 4; ++i) TIE(X.wd[i]);
        X.kr_ready = __all(all_tag(kf4, c.tag));
    }
    gacc = dpp_add<0x111>(gacc); gacc = dpp_add<0x112>(gacc); gacc = dpp_add<0x114>(gacc);
    X.gate = sigmoidf(gacc);
}

__global__ __launch_bounds__(512, 2) void k_fused(Params p) {
    extern __shared__ __attribute__((aligned(16))) char smem[];
    LAS char* lds = (LAS char*)smem;
    const unsigned ldsb = (unsigned)(size_t)lds;
    const int wave = rfl(threadIdx.x >> 6), lane = threadIdx.x & 63;
    const int b = blockIdx.x, xcd = b & 7, idx = b >> 3;
    const bool has_conv = idx < 20;
    const int cb = xcd * 20 + idx, pb = xcd * 12 + idx - 20;
    const bool kvblock = !has_conv && pb < 80;
    const int bs = (xcd & 1) * 8 + (idx & 7), bf = (xcd >> 1) * 4 + (idx >> 3);
    const int st = xcd * 4 + (idx & 3), ds = idx >> 2;
    const bool hi4 = ((st >> 1) & 7) >= 4;
    const int ne = hi4 ? 6 : 8;
    unsigned long long order;
    {
        const unsigned E = hi4 ? 0x876210u : 0x98763210u, L = hi4 ? 0xBA9543u : 0xBA54u;
        const int nl = 12 - ne, re = ds % ne, rl = ds % nl;
        const unsigned long long Er = (((unsigned long long)E << (4 * ne) | E) >> (4 * re)) & ((1ull << (4 * ne)) - 1);
        const unsigned long long Lr = (((unsigned long long)L << (4 * nl) | L) >> (4 * rl)) & ((1ull << (4 * nl)) - 1);
        order = Er | (Lr << (4 * ne));
    }
    STAMPT(0);
    Ctx c; c.p = p; c.lds = lds; c.ldsb = ldsb; c.lane = lane; c.wave = wave; c.xcd = xcd; c.cb = cb; c.pb = pb; c.bs = bs; c.bf = bf; c.tag = 0;

    if (wave >= 4) {
        const int w = wave - 4;
        Tail X;
        X.kr_ready = false; X.gate = 0.f;
#pragma unroll
        for (int i = 0; i < 4; ++i) X.wd[i] = (u32x4){0, 0, 0, 0};
        if (has_conv) loader_gemm1<0>(c, X);
        else if (kvblock) loader_gemm1<1>(c, X);
        else loader_gemm1<2>(c, X);
        const unsigned tag = c.tag;
        STAMPT(4);
        const unsigned vo = lane * 16;
        rsrc_t rw = mkrsrc(p.ws + WS_WT, F * D * 2);
        auto gate_store = [&]() {
            rsrc_t rG = mkrsrc(p.ws + WS_G, S * 4);
            if (w == 3) __builtin_amdgcn_raw_buffer_store_b32(__float_as_uint(X.gate), rG, (lane & 7) == 7 ? (bs * 128 + 8 * bf + (lane >> 3)) * 4 : OOB, 0, 16);
        };
        if (has_conv) {
            const int d = cb * 4 + w;
            const unsigned sl = ldsb + w * CV_SLOT;
            rsrc_t rf = mkrsrc(p.ssk, D * S * 2), r0 = mkrsrc(p.ws + WS_KR, D * KRS * 2);
#pragma unroll
            for (int i = 0; i < 4; ++i) dma16(sl + 512 + i * 1024, vo, rf, d * (S * 2) + i * 1024);
            if (!X.kr_ready) poll4<4>(p.ws, FL_KR + (cb >> 5) * 64, tag, lane);
            STAMPT(5);
#pragma unroll
            for (int i = 0; i < 4; ++i) dma16_sc1(sl + CV_KF + i * 1024, vo, r0, d * (KRS * 2) + i * 1024);
            LAS char* slot = lds + w * CV_SLOT;
            if (lane < 32) *(LAS u32x4*)(slot + lane * 16) = (u32x4){0, 0, 0, 0};
            else if (lane < 48) *(LAS u32x4*)(slot + CV_KF + ((lane - 32) >> 2) * CV_COPY + 4096 + (lane & 3) * 16) = (u32x4){0, 0, 0, 0};
            gate_store();
            {
                const int unit = cb * 3 + w, Tk = unit / 10, dl = unit % 10, kh = Tk / 24, tt = (Tk % 24) >> 1, kk = Tk & 1;
                const int g4 = lane >> 4, q = (lane & 15) >> 2, pp = lane & 3, row = 4 * g4 + q;
                LAS char* buf = lds + P0_LDS + w * 4096;
#pragma unroll
                for (int i = 0; i < 4; ++i) *(LAS u32x4*)(buf + (8 * i + (lane >> 3)) * 128 + (lane & 7) * 16) = X.wd[i];
#pragma unroll
                for (int i = 0; i < 4; ++i) {
                    const int d16 = 4 * dl + i, dsl = d16 / 5, n = d16 % 5;
                    const s16x8 f = cat(trread(buf + row * 128 + 32 * i + 8 * pp), trread(buf + (row + 16) * 128 + 32 * i + 8 * pp));
                    __builtin_amdgcn_raw_buffer_store_b128(__builtin_bit_cast(u32x4, f), rw, w < 3 ? ((((dsl * 12 + tt) * 4 + kh * 2 + kk) * 5 + n) * 64 + lane) * 16 : OOB, 0, 16);
                }
            }
            waitv<4>(); WAITL0();
            LAS char* c0 = slot + CV_KF;
#pragma unroll
            for (int j = 0; j < 4; ++j) {
                const u32x4 a = *(LAS u32x4*)(c0 + j * 1024 + lane * 16);
                const u32x2 nx = *(LAS u32x2*)(c0 + j * 1024 + lane * 16 + 16);
                *(LAS u32x4*)(c0 + CV_COPY + j * 1024 + lane * 16) =
                    (u32x4){__builtin_amdgcn_alignbit(a[1], a[0], 16), __builtin_amdgcn_alignbit(a[2], a[1], 16), __builtin_amdgcn_alignbit(a[3], a[2], 16), __builtin_amdgcn_alignbit(nx[0], a[3], 16)};
                *(LAS u32x4*)(c0 + 2 * CV_COPY + j * 1024 + lane * 16) = (u32x4){a[1], a[2], a[3], nx[0]};
                *(LAS u32x4*)(c0 + 3 * CV_COPY + j * 1024 + lane * 16) =
                    (u32x4){__builtin_amdgcn_alignbit(a[2], a[1], 16), __builtin_amdgcn_alignbit(a[3], a[2], 16), __builtin_amdgcn_alignbit(nx[0], a[3], 16), __builtin_amdgcn_alignbit(nx[1], nx[0], 16)};
            }
        } else {
            gate_store();
            waitv<0>();
        }
        WAITL0();
        bar();
        STAMPT(6);
        if (has_conv) {
            waitv<0>();
            if (lds_last(lds, L_CNT + 4, 4, lane) && lane < 8) st_flag(p.ws, FL_WT + lane * 256 + cb, tag);
        }
        rsrc_t rh = mkrsrc(p.ws + WS_H, S * F * 2);
        const unsigned sw2 = ds * (48 * 5 * 1024) + w * 5120, sh2 = ((st * 2 + (w & 1)) * 96 + (w >> 1) * 48) * 1024;
        auto stage_of = [&](int t) { return (int)((order >> (4 * t)) & 15); };
        auto w_dma = [&](int t) {
            const int s4 = t & 3;
            const unsigned dst = ldsb + (s4 < 2 ? G2_WA + s4 * G2_WST : G2_WB + (s4 - 2) * G2_WST) + w * 5120, src = sw2 + stage_of(t) * G2_WST;
#pragma unroll
            for (int j = 0; j < 5; ++j) dma16_sc1(dst + j * 1024, vo, rw, src + j * 1024);
        };
        auto h_dma = [&](int t) {
            const int s4 = t & 3;
            const unsigned dst = ldsb + (s4 < 2 ? G2_HA + s4 * G2_HST : G2_HB + (s4 - 2) * G2_HST) + w * 4096, src = sh2 + stage_of(t) * 4096;
#pragma unroll
            for (int j = 0; j < 4; ++j) dma16_sc1(dst + j * 1024, vo, rh, src + j * 1024);
        };
        const unsigned late_mask = hi4 ? 12u : 8u, hword = FL_H + (st >> 1) * 64;
        if (w == 0) {
            poll4<4>(p.ws, hword, tag, lane, 15u & ~late_mask);
            if (lane == 0) lds_set(lds, L_CNT + 20, 1u);
        } else {
            lds_wait(lds, L_CNT + 20);
        }
        STAMPT(10);
        h_dma(0); h_dma(1);
        if (w == 0) {
            poll4<40>(p.ws, FL_WT + xcd * 256, tag, lane);
            if (lane == 0) lds_set(lds, L_CNT + 8, 1u);
        } else {
            lds_wait(lds, L_CNT + 8);
        }
        STAMPT(9);
        w_dma(0); w_dma(1);
        const u32x4 hl = aload_sc1(flag_rsrc(p.ws), (hword + (lane & 3) * 4) * 4);
        {
#pragma unroll
            for (int i = 0; i < 2; ++i) {
                const int ln = (idx & 3) * 480 + (w * 2 + i) * 64 + lane;
                prefetch_lines(ldsb + L_DUMMY, rw, (w * 2 + i) * 64 + lane < 480 ? ds * (48 * 5 * 1024) + ln * 128 : OOB);
            }
        }
        waitv<2>();
        u32x4 hl_ = hl; TIE(hl_);
        const bool late_ok = __all(all_tag(hl_, tag));
        bar();
        STAMPT(7);
        h_dma(2); w_dma(2);
        bar();
        h_dma(3); w_dma(3);
        for (int t = 1; t <= 8; ++t) {
            waitv<9>(); bar();
            if (t + 3 == ne && !late_ok) poll4<4>(p.ws, hword, tag, lane);
            h_dma(t + 3); w_dma(t + 3);
        }
        rsrc_t ry = mkrsrc(p.ws + WS_YT, D * S * 2);
        const u32x4 yf = aload_sc1(flag_rsrc(p.ws), (FL_Y + ds * 64 + (lane < 5 ? lane : 0) * 4) * 4);
        waitv<10>(); bar();
        waitv<0>();
        u32x4 yf_ = yf; TIE(yf_);
        if (!__all(all_tag(yf_, tag))) poll4<5>(p.ws, FL_Y + ds * 64, tag, lane);
        u32x4 yv[3];
#pragma unroll
        for (int i = 0; i < 3; ++i) {
            const int it = w + 4 * i, ch = 8 * it + (lane >> 3);
            yv[i] = aload_sc1(ry, it < 10 ? ((80 * ds + ch) * S + 64 * st + (lane & 7) * 8) * 2 : OOB);
        }
        bar();
        bar();
        waitv<0>();
#pragma unroll
        for (int i = 0; i < 3; ++i) {
            const int it = w + 4 * i, ch = 8 * it + (lane >> 3);
            TIE(yv[i]);
            if (it < 10) *(LAS u32x4*)(lds + G2_Y + ch * 128 + (lane & 7) * 16) = yv[i];
        }
        bar();
        WAITL0();
        bar();
        if (b == 0 && w == 0) {
            rsrc_t r = flag_rsrc(p.ws);
            for (int spins = 0; spins < (1 << 18); ++spins) {
                u32x4 x;
                asm volatile("buffer_load_dwordx4 %0, %1, %2, 0 offen sc1\n\ts_waitcnt vmcnt(0)" : "=&v"(x) : "v"((FL_H + (lane >> 2) * 64 + (lane & 3) * 4) * 4), "s"(r) : "memory");
                if (__all(all_tag(x, tag))) break;
                __builtin_amdgcn_s_sleep(4);
            }
            if (lane == 0) st_flag(p.ws, FL_EPOCH, tag);
        }
        STAMPT(8);
    } else {
        unsigned tag = 0;
        float Dd = bflo(p.sd[(has_conv ? cb * 4 : 0) + wave]);
        asm volatile("" : "+v"(Dd));
        {
            const int wsx = wave & 1, wf = wave >> 1;
            const int g = lane >> 4, fr = lane & 15, q = (lane & 15) >> 2, pp = lane & 3;
            int oa[3];
#pragma unroll
            for (int i = 0; i < 3; ++i) {
                const int ft = 3 * wf + i, cc = ft >> 1, half = ft & 1;
                oa[i] = 16384 + (g * 3 + cc) * 512 + q * 64 + 16 * ((2 * half + (pp >> 1)) ^ (2 * (g & 1))) + 8 * (pp & 1);
            }
            const int ob0 = (64 * wsx + fr) * 128 + ((g ^ (fr >> 1)) * 16);
            const int ob1 = (64 * wsx + fr) * 128 + ((g ^ (fr >> 1) ^ 4) * 16);
            f32x4 accg[3][4], accu[3][4];
#pragma unroll
            for (int i = 0; i < 3; ++i)
#pragma unroll
                for (int m = 0; m < 4; ++m) { accg[i][m] = (f32x4){0, 0, 0, 0}; accu[i][m] = (f32x4){0, 0, 0, 0}; }
            struct Fr { s16x8 a[6]; s16x8 b[4]; } fa, fb;
            auto rd = [&](Fr& f, int ks, int sb) {
#pragma unroll
                for (int m = 0; m < 4; ++m) f.b[m] = *(LAS s16x8*)(lds + sb + (ks ? ob1 : ob0) + m * 2048);
#pragma unroll
                for (int i = 0; i < 3; ++i) {
                    f.a[i] = cat(trread(lds + sb + oa[i] + ks * 6144), trread(lds + sb + oa[i] + ks * 6144 + 256));
                    f.a[3 + i] = cat(trread(lds + sb + oa[i] + ks * 6144 + 12288), trread(lds + sb + oa[i] + ks * 6144 + 256 + 12288));
                }
            };
            auto mm = [&](const Fr& f) {
#pragma unroll
                for (int i = 0; i < 3; ++i)
#pragma unroll
                    for (int m = 0; m < 4; ++m) {
                        accg[i][m] = mfma16(f.a[i], f.b[m], accg[i][m]);
                        accu[i][m] = mfma16(f.a[3 + i], f.b[m], accu[i][m]);
                    }
            };
            auto interleave = [&]() {
#pragma unroll
                for (int i = 0; i < 16; ++i) { SGB(0x008, 1); SGB(0x100, 1); }
                SGB(0x008, 8);
                SB0();
            };
            const int pi = kvblock ? pb : (has_conv ? cb : 160 + pb - 80), np = kvblock ? 80 : 176, t0 = kvblock ? 6 : 2;
            if (kvblock) kv_prepare(c); else __builtin_amdgcn_s_sleep(SLEEP_C);
            {
                rsrc_t rpf = mkrsrc(wave == 0 ? p.q : (wave == 1 ? p.wg : p.wu), wave == 0 ? S * D * 2 : D * F * 2);
                const int nl = wave == 0 ? 8192 : 6144;
                for (int l0 = pi * 64; l0 < nl && wave < 3; l0 += np * 64) {
                    const int l = l0 + lane;
                    prefetch_lines(ldsb + L_DUMMY, rpf, wave == 0 ? (l >> 2) * 1280 + (t0 + (l & 3)) * 128 : (t0 * 64 + l / 24) * 3072 + (l % 24) * 128);
                }
            }
            __builtin_amdgcn_s_setprio(1);
            bar();
            STAMPT(3);
            rd(fa, 0, 0);
            int sb = 0;
            for (int t = 0; t < 10; ++t) {
                rd(fb, 1, sb); mm(fa); interleave();
                WAITL0();
                bar();
                sb = (sb == 2 * G1_STAGE) ? 0 : sb + G1_STAGE;
                if (t < 9) rd(fa, 0, sb);
                mm(fb); interleave();
            }
            STAMPT(4);
            tag = get_tag(c);
            rsrc_t rh = mkrsrc(p.ws + WS_H, S * F * 2);
#pragma unroll
            for (int mp = 0; mp < 2; ++mp)
#pragma unroll
                for (int i = 0; i < 3; ++i) {
                    float hv[8];
#pragma unroll
                    for (int e = 0; e < 8; ++e) hv[e] = accu[i][2 * mp + (e >> 2)][e & 3] * sigmoidf(accg[i][2 * mp + (e >> 2)][e & 3]);
                    const u32x4 o = {cvtpk(hv[0], hv[1]), cvtpk(hv[2], hv[3]), cvtpk(hv[4], hv[5]), cvtpk(hv[6], hv[7])};
                    const int sp = bs * 4 + wsx * 2 + mp, ft = bf * 6 + wf * 3 + i;
                    __builtin_amdgcn_raw_buffer_store_b128(o, rh, ((sp * 96 + ft) * 64 + lane) * 16, 0, 16);
                }
        }
        STAMPT(5);
        auto publish_h = [&]() {
            waitv<0>();
            if (lds_last(lds, L_CNT, 4, lane) && lane == 0) st_flag(p.ws, FL_H + bs * 64 + bf, tag);
        };
        bar();
        STAMPT(6);
        publish_h();
        f32x4 cacc[8];
        u32x2 ckv[8];
#pragma unroll
        for (int n = 0; n < 8; ++n) { cacc[n] = (f32x4){0, 0, 0, 0}; ckv[n] = (u32x2){0, 0}; }
        const int ca = lane & 15, cg = lane >> 4;
        if (has_conv) {
            const unsigned slot = ldsb + wave * CV_SLOT;
            unsigned pg = slot + 480 + 32 * ca + 16 * cg;
            unsigned pf = slot + CV_KF + (3 - (ca & 3)) * CV_COPY + 472 + 16 * cg - 8 * (ca >> 2);
            struct Res { s16x8 G[8]; s16x4 Fl[8], Fh[8]; } RA, RB;
            auto rd1 = [&](Res& R, int k) {
                const int j = k / 3, m = k % 3;
                if (m == 0) asm volatile("ds_read_b64 %0, %1 offset:%2" : "=v"(R.Fl[j]) : "v"(pf), "n"(512 * (7 - j)));
                else if (m == 1) asm volatile("ds_read_b64 %0, %1 offset:%2" : "=v"(R.Fh[j]) : "v"(pf), "n"(512 * (7 - j) + 8));
                else asm volatile("ds_read_b128 %0, %1 offset:%2" : "=v"(R.G[j]) : "v"(pg), "n"(512 * j));
            };
            auto land = [&](Res& R) {
                asm volatile("s_waitcnt lgkmcnt(0)"
                             : "+v"(R.G[0]), "+v"(R.G[1]), "+v"(R.G[2]), "+v"(R.G[3]), "+v"(R.G[4]), "+v"(R.G[5]), "+v"(R.G[6]), "+v"(R.G[7]),
                               "+v"(R.Fl[0]), "+v"(R.Fl[1]), "+v"(R.Fl[2]), "+v"(R.Fl[3]), "+v"(R.Fl[4]), "+v"(R.Fl[5]), "+v"(R.Fl[6]), "+v"(R.Fl[7]));
                asm volatile(""
                             : "+v"(R.Fh[0]), "+v"(R.Fh[1]), "+v"(R.Fh[2]), "+v"(R.Fh[3]), "+v"(R.Fh[4]), "+v"(R.Fh[5]), "+v"(R.Fh[6]), "+v"(R.Fh[7]));
                SB0();
            };
            auto residue = [&](Res& R, Res& N) {
                pg -= 64; pf -= 64;
                int k = 0;
#pragma unroll
                for (int I = 0; I < 8; ++I) {
                    const s16x8 Fv = cat(R.Fl[I], R.Fh[I]);
#pragma unroll
                    for (int n = I; n < 8; ++n) {
                        cacc[n] = mfma16(Fv, R.G[n - I], cacc[n]);
                        if (k < 24) rd1(N, k);
                        ++k;
                        SB0();
                    }
                }
                land(N);
            };
#pragma unroll
            for (int k = 0; k < 24; ++k) rd1(RA, k);
            land(RA);
            for (int r2 = 0; r2 < 4; ++r2) {
                residue(RA, RB);
                residue(RB, RA);
            }
#pragma unroll
            for (int n = 0; n < 8; ++n) ckv[n] = *(LAS u32x2*)(lds + wave * CV_SLOT + CV_KF + 2 * (2044 - 256 * n - 16 * ca - 4 * cg));
        }
        STAMPT(7);
        {
            const int sp = wave & 1, kh = wave >> 1;
            const int s16 = lane & 15, g = lane >> 4, q = s16 >> 2, pp = lane & 3;
            const int srow = 64 * st + 32 * sp + 16 * kh + s16;
            f32x4 acc[2][5];
#pragma unroll
            for (int n = 0; n < 5; ++n) { acc[0][n] = (f32x4){0, 0, 0, 0}; acc[1][n] = (f32x4){0, 0, 0, 0}; }
            const unsigned pw = ldsb + kh * 10240 + lane * 16, ph = ldsb + wave * 4096 + lane * 16;
            struct Fr2 { s16x8 w[5]; u32x4 h[2]; } fa, fb;
            auto rd2 = [&](Fr2& f, int s4, int kk) {
                LAS char* bh = (LAS char*)(size_t)(ph + (s4 < 2 ? G2_HA + s4 * G2_HST : G2_HB + (s4 - 2) * G2_HST) + kk * 2048);
                LAS char* bw = (LAS char*)(size_t)(pw + (s4 < 2 ? G2_WA + s4 * G2_WST : G2_WB + (s4 - 2) * G2_WST) + kk * 5120);
                f.h[0] = *(LAS u32x4*)bh; f.h[1] = *(LAS u32x4*)(bh + 1024);
#pragma unroll
                for (int n = 0; n < 5; ++n) f.w[n] = *(LAS s16x8*)(bw + n * 1024);
            };
            auto swp = [&](Fr2& f) {
                unsigned a2 = f.h[0][2], a3 = f.h[0][3], b0 = f.h[1][0], b1 = f.h[1][1];
                asm volatile("v_swap_b32 %0, %1\n\tv_swap_b32 %2, %3\n\ts_nop 1" : "+v"(a2), "+v"(b0), "+v"(a3), "+v"(b1));
                f.h[0][2] = a2; f.h[0][3] = a3; f.h[1][0] = b0; f.h[1][1] = b1;
            };
            auto step = [&](const Fr2& f) {
                const s16x8 h0 = __builtin_bit_cast(s16x8, f.h[0]), h1 = __builtin_bit_cast(s16x8, f.h[1]);
#pragma unroll
                for (int n = 0; n < 5; ++n) { acc[0][n] = mfma16(f.w[n], h0, acc[0][n]); acc[1][n] = mfma16(f.w[n], h1, acc[1][n]); }
            };
            auto il2 = [&]() {
#pragma unroll
                for (int i = 0; i < 7; ++i) { SGB(0x008, 1); SGB(0x100, 1); }
                SGB(0x008, 3);
                SB0();
            };
            WAITL0();
            bar();
            STAMPT(8);
            float gt = 0.f;
            u32x2 qv[5];
#pragma unroll
            for (int n = 0; n < 5; ++n) qv[n] = (u32x2){0, 0};
            rd2(fa, 0, 0);
            swp(fa);
            for (int t = 0; t < 12; ++t) {
                rd2(fb, t & 3, 1);
                step(fa);
                il2();
                swp(fb);
                SB0();
                if (t == 4) waitv<0>();
                bar();
                if (t == 0 && has_conv) {
                    const int d = cb * 4 + wave;
                    rsrc_t ry = mkrsrc(p.ws + WS_YT, D * S * 2);
                    u32x2 o[8];
#pragma unroll
                    for (int n = 0; n < 8; ++n) {
                        o[n][0] = cvtpk(cacc[n][0] + bfhi(ckv[n][1]) * Dd, cacc[n][1] + bflo(ckv[n][1]) * Dd);
                        o[n][1] = cvtpk(cacc[n][2] + bfhi(ckv[n][0]) * Dd, cacc[n][3] + bflo(ckv[n][0]) * Dd);
                    }
#pragma unroll
                    for (int n = 0; n < 8; n += 2) {
                        auto x = __builtin_amdgcn_permlane16_swap(o[n][0], o[n + 1][0], false, false);
                        auto y = __builtin_amdgcn_permlane16_swap(o[n][1], o[n + 1][1], false, false);
                        const u32x4 val = {x[0], y[0], x[1], y[1]};
                        const int s = 256 * (n + (cg & 1)) + 16 * ca + 4 * (cg & 2);
                        __builtin_amdgcn_raw_buffer_store_b128(val, ry, (d * S + s) * 2, 0, 16);
                    }
                    SB0();
                }
                if (t == 4 && has_conv && wave == 0 && lane == 0) st_flag(p.ws, FL_Y + (cb / 20) * 64 + cb % 20, tag);
                if (t == 9) {
                    gt = __hip_atomic_load((const GAS float*)(p.ws + WS_G) + srow, RLX_AGENT);
#pragma unroll
                    for (int n = 0; n < 5; ++n) qv[n] = *(const u32x2*)(p.q + (size_t)srow * D + 80 * ds + 16 * n + 4 * g);
                }
                rd2(fa, (t + 1) & 3, 0);
                step(fb);
                il2();
                swp(fa);
                SB0();
            }
            WAITL0();
            bar();
            STAMPT(10);
#pragma unroll
            for (int n = 0; n < 5; ++n) *(LAS f32x4*)(lds + G2_RED + (wave * 5 + n) * 1024 + lane * 16) = kh ? acc[0][n] : acc[1][n];
            WAITL0();
            bar();
            STAMPT(11);
#pragma unroll
            for (int n = 0; n < 5; ++n) {
                const f32x4 o4 = (kh ? acc[1][n] : acc[0][n]) + *(LAS f32x4*)(lds + G2_RED + ((wave ^ 2) * 5 + n) * 1024 + lane * 16);
                const u32x2 yv = __builtin_bit_cast(u32x2, trread(lds + G2_Y + (16 * n + 4 * g + q) * 128 + (32 * sp + 16 * kh + 4 * pp) * 2));
                const float o0 = gt * (bflo(qv[n][0]) * bflo(yv[0])) + (1.f - gt) * o4[0];
                const float o1 = gt * (bfhi(qv[n][0]) * bfhi(yv[0])) + (1.f - gt) * o4[1];
                const float o2 = gt * (bflo(qv[n][1]) * bflo(yv[1])) + (1.f - gt) * o4[2];
                const float o3 = gt * (bfhi(qv[n][1]) * bfhi(yv[1])) + (1.f - gt) * o4[3];
                *(u32x2*)(p.out + (size_t)srow * D + 80 * ds + 16 * n + 4 * g) = (u32x2){cvtpk(o0, o1), cvtpk(o2, o3)};
            }
            STAMPT(12);
        }
    }
}

extern "C" void kernel_launch(void* const* d_in, const int* in_sizes, int n_in,
                              void* d_out, int out_size, void* d_ws, size_t ws_size,
                              hipStream_t stream) {
    Params p;
    p.q = (const u16*)d_in[0]; p.k = (const u16*)d_in[1]; p.v = (const u16*)d_in[2];
    p.sk = (const u16*)d_in[3]; p.ssk = (const u16*)d_in[4]; p.sd = (const u16*)d_in[5];
    p.wg = (const u16*)d_in[7]; p.wu = (const u16*)d_in[8]; p.wd = (const u16*)d_in[9]; p.gw = (const u16*)d_in[10];
    p.out = (u16*)d_out; p.ws = (char*)d_ws;
    hipFuncSetAttribute((const void*)k_fused, hipFuncAttributeMaxDynamicSharedMemorySize, LDS_BYTES);
    k_fused<<<256, 512, LDS_BYTES, stream>>>(p);
}
```

```cpp
#include <hip/hip_runtime.h>
#include <hip/hip_bf16.h>
#include <stdint.h>

typedef unsigned short u16;
typedef short s16x4 __attribute__((ext_vector_type(4)));
typedef short s16x8 __attribute__((ext_vector_type(8)));
typedef __bf16 b16x8 __attribute__((ext_vector_type(8)));
typedef __bf16 b16x2 __attribute__((ext_vector_type(2)));
typedef float f32x2 __attribute__((ext_vector_type(2)));
typedef float f32x4 __attribute__((ext_vector_type(4)));
typedef unsigned u32x2 __attribute__((ext_vector_type(2)));
typedef unsigned u32x4 __attribute__((ext_vector_type(4)));
#define LAS __attribute__((address_space(3)))
#define GAS __attribute__((address_space(1)))
#define DI __device__ __forceinline__

static constexpr int S = 2048, D = 640, F = 1536;
static constexpr int KRS = 2112;
static constexpr unsigned WS_H = 0, WS_KR = 8u << 20, WS_YT = 14u << 20, WS_G = 17u << 20, WS_WT = 18u << 20, WS_STAMP = 20u << 20, WS_FLAG = 24u << 20;
static constexpr unsigned FL_EPOCH = 0, FL_KR = 1024, FL_WT = 2048  , FL_H = 4096, FL_Y = 6144;
static constexpr unsigned OOB = 0xF0000000u;

struct Params {
    const u16 *q, *k, *v, *sk, *ssk, *sd, *wg, *wu, *wd, *gw;
    u16* out; char* ws;
};

DI float bflo(unsigned x) { return __uint_as_float(x << 16); }
DI float bfhi(unsigned x) { return __uint_as_float(x & 0xffff0000u); }
DI unsigned cvtpk(float lo, float hi) { f32x2 v = {lo, hi}; b16x2 b = __builtin_convertvector(v, b16x2); return __builtin_bit_cast(unsigned, b); }
DI unsigned rfl(unsigned x) { return __builtin_amdgcn_readfirstlane(x); }
DI float sigmoidf(float x) { return __builtin_amdgcn_rcpf(1.f + __builtin_amdgcn_exp2f(x * -1.44269504089f)); }
DI f32x4 mfma16(s16x8 a, s16x8 b, f32x4 c) {
    return __builtin_amdgcn_mfma_f32_16x16x32_bf16(__builtin_bit_cast(b16x8, a), __builtin_bit_cast(b16x8, b), c, 0, 0, 0);
}
DI s16x4 trread(LAS char* p) { return __builtin_amdgcn_ds_read_tr16_b64_v4i16((LAS s16x4*)p); }
DI s16x8 cat(s16x4 a, s16x4 b) { return __builtin_shufflevector(a, b, 0, 1, 2, 3, 4, 5, 6, 7); }
typedef __amdgpu_buffer_rsrc_t rsrc_t;
DI rsrc_t mkrsrc(const void* p, unsigned bytes) { return __builtin_amdgcn_make_buffer_rsrc((void*)p, 0, bytes, 0x00020000); }
DI void dma16(unsigned m0v, unsigned voff, rsrc_t r, unsigned soff) {
    asm volatile("s_mov_b32 m0, %0\n\ts_nop 0\n\tbuffer_load_dwordx4 %1, %2, %3 offen lds" ::"s"(m0v), "v"(voff), "s"(r), "s"(soff) : "m0", "memory");
}
DI void dma16_sc1(unsigned m0v, unsigned voff, rsrc_t r, unsigned soff) {
    asm volatile("s_mov_b32 m0, %0\n\ts_nop 0\n\tbuffer_load_dwordx4 %1, %2, %3 offen sc1 lds" ::"s"(m0v), "v"(voff), "s"(r), "s"(soff) : "m0", "memory");
}
DI u32x4 aload(rsrc_t r, unsigned voff) { u32x4 x; asm volatile("buffer_load_dwordx4 %0, %1, %2, 0 offen" : "=v"(x) : "v"(voff), "s"(r) : "memory"); return x; }
DI u32x4 aload_sc1(rsrc_t r, unsigned voff) { u32x4 x; asm volatile("buffer_load_dwordx4 %0, %1, %2, 0 offen sc1" : "=v"(x) : "v"(voff), "s"(r) : "memory"); return x; }
DI void prefetch_lines(unsigned scratch, rsrc_t r, unsigned voff) {
    asm volatile("s_mov_b32 m0, %0\n\ts_nop 0\n\tbuffer_load_dword %1, %2, 0 offen lds" ::"s"(scratch), "v"(voff), "s"(r) : "m0", "memory");
}
template <int N> DI void waitv() { asm volatile("s_waitcnt vmcnt(%0)" ::"n"(N) : "memory"); }
DI float dot2(unsigned a, unsigned b, float c) { return __builtin_amdgcn_fdot2_f32_bf16(__builtin_bit_cast(b16x2, a), __builtin_bit_cast(b16x2, b), c, false); }
template <int CTRL> DI float dpp_add(float x) { return x + __int_as_float(__builtin_amdgcn_update_dpp(0, __float_as_int(x), CTRL, 0xf, 0xf, true)); }
#define TIE(x) asm volatile("" : "+v"(x))
#define WAITL0() asm volatile("s_waitcnt lgkmcnt(0)" ::: "memory")
DI void bar() { asm volatile("" ::: "memory"); __builtin_amdgcn_s_barrier(); asm volatile("" ::: "memory"); }
#define SGB(m, n) __builtin_amdgcn_sched_group_barrier(m, n, 0)
#define SB0() __builtin_amdgcn_sched_barrier(0)
#define RLX_AGENT __ATOMIC_RELAXED, __HIP_MEMORY_SCOPE_AGENT

#define SLEEP_L 0
#define SLEEP_LK 12
#define SLEEP_K 0
#define SLEEP_C 12

DI void st_flag(char* ws, unsigned word, unsigned tag) { __hip_atomic_store((GAS unsigned*)(ws + WS_FLAG) + word, tag, RLX_AGENT); }
DI rsrc_t flag_rsrc(char* ws) { return mkrsrc(ws + WS_FLAG, 32768); }
DI bool all_tag(u32x4 x, unsigned tag) { return x[0] == tag && x[1] == tag && x[2] == tag && x[3] == tag; }
template <int NL>
DI void poll4(char* ws, unsigned word, unsigned tag, int lane, unsigned care = 15u) {
    rsrc_t r = flag_rsrc(ws);
    for (int spins = 0; spins < (1 << 18); ++spins) {
        u32x4 x;
        asm volatile("buffer_load_dwordx4 %0, %1, %2, 0 offen sc1\n\ts_waitcnt vmcnt(0)" : "=&v"(x) : "v"((word + (lane < NL ? lane : 0) * 4) * 4), "s"(r) : "memory");
        const bool ok = (x[0] == tag || !(care & 1)) && (x[1] == tag || !(care & 2)) && (x[2] == tag || !(care & 4)) && (x[3] == tag || !(care & 8));
        if (__all(ok)) return;
        __builtin_amdgcn_s_sleep(4);
    }
}
DI void lds_set(LAS char* lds, int off, unsigned v) { __hip_atomic_store((LAS unsigned*)(lds + off), v, __ATOMIC_RELAXED, __HIP_MEMORY_SCOPE_WORKGROUP); }
DI unsigned lds_get(LAS char* lds, int off) { return rfl(__hip_atomic_load((LAS unsigned*)(lds + off), __ATOMIC_RELAXED, __HIP_MEMORY_SCOPE_WORKGROUP)); }
DI unsigned lds_wait(LAS char* lds, int off) {
    unsigned v = 0;
    for (int spins = 0; spins < (1 << 22); ++spins) {
        v = lds_get(lds, off);
        if (v) break;
        __builtin_amdgcn_s_sleep(8);
    }
    asm volatile("" ::: "memory");
    return v;
}
DI bool lds_last(LAS char* lds, int off, unsigned n, int lane) {
    unsigned old = 0;
    if (lane == 0) old = __hip_atomic_fetch_add((LAS unsigned*)(lds + off), 1u, __ATOMIC_RELAXED, __HIP_MEMORY_SCOPE_WORKGROUP);
    return rfl(old) == n - 1;
}

#define STAMPT(i) do {} while (0)

constexpr int G1_STAGE = 40960;
constexpr int P0_LDS = 122880;
constexpr int TP = 272;
constexpr int P0_T = P0_LDS, P0_GW = P0_LDS + 128 * TP;
constexpr int CV_BASE = G1_STAGE;
constexpr int CV_SLOT = 21248, CV_COPY = 4160, CV_KF = 4608;
constexpr int WT_SCR = 126976;
constexpr int G2_WST = 20480, G2_HST = 16384;
constexpr int G2_WA = 0, G2_HA = 126976;
constexpr int G2_WB = 2 * G2_WST, G2_HB = G2_WB + 2 * G2_WST;
constexpr int G2_Y = G2_HB + 2 * G2_HST;
constexpr int G2_RED = 0;
constexpr int L_DUMMY = 163328;
constexpr int L_CNT = 163776;
constexpr int L_EPOCH = 163824;
constexpr int LDS_BYTES = 163840;

struct Ctx {
    Params p; LAS char* lds; unsigned ldsb; int lane, wave, xcd, cb, pb, bs, bf; unsigned tag;
};
DI void load_epoch(const Ctx& c) {
    rsrc_t r = mkrsrc(c.p.ws + WS_FLAG, 64);
    if (c.lane == 0) asm volatile("s_mov_b32 m0, %0\n\ts_nop 0\n\tbuffer_load_dword %1, %2, 0 offen sc1 lds" ::"s"(c.ldsb + L_EPOCH), "v"(0u), "s"(r) : "m0", "memory");
}
DI unsigned get_tag(const Ctx& c) { return lds_get(c.lds, L_EPOCH) + 1u; }

DI void kv_prepare(const Ctx& c) {
    const Params& p = c.p;
    LAS char* lds = c.lds;
    const int lane = c.lane, wave = c.wave;
    (void)wave;
    const int kdc = c.pb % 5, s0 = (c.pb / 5) * 128, d0 = kdc * 128, tid = c.wave * 64 + lane, c16 = tid & 15, rg = tid >> 4, s = s0 + 8 * rg;
    u32x4 kr[11], vr[8], w4[4];
    {
        rsrc_t rk = mkrsrc(p.k, S * D * 2), rv = mkrsrc(p.v, S * D * 2), rsk = mkrsrc(p.sk, D * 8);
#pragma unroll
        for (int i = 0; i < 11; ++i) kr[i] = aload(rk, (unsigned)(((s - 3 + i) * D + d0 + 8 * c16) * 2));
#pragma unroll
        for (int i = 0; i < 8; ++i) vr[i] = aload(rv, ((s + i) * D + d0 + 8 * c16) * 2);
#pragma unroll
        for (int i = 0; i < 4; ++i) w4[i] = aload(rsk, (d0 + 8 * c16) * 8 + i * 16);
    }
    waitv<0>();
    STAMPT(2);
#pragma unroll
    for (int i = 0; i < 11; ++i) TIE(kr[i]);
#pragma unroll
    for (int i = 0; i < 8; ++i) TIE(vr[i]);
#pragma unroll
    for (int i = 0; i < 4; ++i) TIE(w4[i]);
    LAS char* tw = lds + P0_T + 8 * c16 * TP + (((15 - rg) ^ c16) * 16);
#pragma unroll
    for (int e = 0; e < 8; ++e) {
        const unsigned sel = (e & 1) ? 0x07060302u : 0x05040100u;
        const unsigned wa = w4[e >> 1][(e & 1) * 2], wb = w4[e >> 1][(e & 1) * 2 + 1];
        unsigned pr[10];
#pragma unroll
        for (int i = 0; i < 10; ++i) pr[i] = __builtin_amdgcn_perm(kr[i][e >> 1], kr[i + 1][e >> 1], sel);
        float o[8];
#pragma unroll
        for (int r = 0; r < 8; ++r) o[r] = dot2(pr[r + 2], wa, dot2(pr[r], wb, 0.f)) * ((e & 1) ? bfhi(vr[r][e >> 1]) : bflo(vr[r][e >> 1]));
        *(LAS u32x4*)(tw + e * TP) = (u32x4){cvtpk(o[7], o[6]), cvtpk(o[5], o[4]), cvtpk(o[3], o[2]), cvtpk(o[1], o[0])};
    }
    STAMPT(13);
    WAITL0(); bar();
    rsrc_t r0 = mkrsrc(p.ws + WS_KR, D * KRS * 2);
#pragma unroll
    for (int it = 0; it < 8; ++it) {
        const int item = tid + it * 256, ch = item >> 4, j = item & 15;
        __builtin_amdgcn_raw_buffer_store_b128(*(LAS u32x4*)(lds + P0_T + ch * TP + ((j ^ ((ch >> 3) & 15)) * 16)), r0, ((d0 + ch) * KRS + (1920 - s0) + 8 * j) * 2, 0, 16);
    }
    STAMPT(14);
    waitv<0>();
    if (lds_last(lds, L_CNT + 16, 4, lane) && lane == 0) st_flag(p.ws, FL_KR + kdc * 64 + c.pb / 5, get_tag(c));
    STAMPT(15);
}

struct Tail { u32x4 wd[4]; bool kr_ready; };
template <int MODE>
DI void loader_gemm1(Ctx& c, Tail& X) {
    const Params& p = c.p;
    const int lane = c.lane, w = c.wave - 4, wave = c.wave;
    (void)wave;
    LAS char* lds = c.lds;
    rsrc_t rq = mkrsrc(p.q, S * D * 2), rg_ = mkrsrc(p.wg, D * F * 2), ru_ = mkrsrc(p.wu, D * F * 2);
    unsigned vx[2], vw[3];
    {
        const int r = lane >> 3, slot = lane & 7;
        vx[0] = r * 1280 + ((slot ^ (r >> 1)) * 16);
        vx[1] = r * 1280 + ((slot ^ (r >> 1) ^ 4) * 16);
#pragma unroll
        for (int j = 0; j < 3; ++j) {
            const int L = 2 * j + (lane >> 5), kbl = L / 3, cc = L % 3, row = (lane & 31) >> 2, ch = (lane & 3) ^ (2 * kbl);
            vw[j] = (kbl * 8 + row) * 3072 + cc * 64 + ch * 16;
        }
    }
    const unsigned sx0 = (c.bs * 128 + 32 * w) * 1280, sw0 = 16 * w * 3072 + c.bf * 192;
    auto issue = [&](int t) {
        const unsigned base = c.ldsb + (t >= 9 ? t - 9 : (t >= 6 ? t - 6 : (t >= 3 ? t - 3 : t))) * G1_STAGE;
#pragma unroll
        for (int i = 0; i < 4; ++i) dma16(base + (32 * w + 8 * i) * 128, vx[i & 1], rq, sx0 + i * 8 * 1280 + t * 128);
#pragma unroll
        for (int j = 0; j < 3; ++j) dma16(base + 16384 + w * 3072 + j * 1024, vw[j], rg_, sw0 + t * 64 * 3072);
#pragma unroll
        for (int j = 0; j < 3; ++j) dma16(base + 28672 + w * 3072 + j * 1024, vw[j], ru_, sw0 + t * 64 * 3072);
    };
    if (w == 0) load_epoch(c);
    if (w < 2) {
        rsrc_t rgw = mkrsrc(p.gw, D * 2);
        if (w == 0 || lane < 16) dma16(c.ldsb + P0_GW + w * 1024, (w * 64 + lane) * 16, rgw, 0);
    }
    if (w == 3 && lane < 8) *(LAS unsigned*)(lds + L_CNT + lane * 4) = 0;
    if (MODE == 1) {
        waitv<0>();
        __builtin_amdgcn_s_sleep(SLEEP_K + SLEEP_LK);
        issue(0); issue(1);
        WAITL0(); bar();
    } else {
        __builtin_amdgcn_s_sleep(SLEEP_L);
        STAMPT(1);
        issue(0);
        STAMPT(2);
        STAMPT(11);
    }
    if (MODE == 1) waitv<10>(); else waitv<0>();
    WAITL0(); bar();
    c.tag = get_tag(c);
    STAMPT(3);
    if (MODE != 1) issue(1);
    issue(2);
    const int grw = 8 * c.bf + (lane >> 3), gch = (lane & 7) ^ ((grw >> 1) & 7);
    float gacc = 0.f;
    auto gate_step = [&](int t) {
        const int buf = t >= 9 ? t - 9 : (t >= 6 ? t - 6 : (t >= 3 ? t - 3 : t));
        const u32x4 xv = *(LAS u32x4*)(lds + buf * G1_STAGE + grw * 128 + (lane & 7) * 16);
        const u32x4 gv = *(LAS u32x4*)(lds + P0_GW + t * 128 + gch * 16);
#pragma unroll
        for (int i = 0; i < 4; ++i) gacc = dot2(xv[i], gv[i], gacc);
    };
    if (w == 3) gate_step(0);
    for (int t = 0; t < 6; ++t) {
        waitv<10>(); WAITL0(); bar();
        issue(t + 3);
        if (w == 3) gate_step(t + 1);
    }
    u32x4 kf4 = {0, 0, 0, 0};
    waitv<10>(); WAITL0(); bar();
    if (MODE == 0) kf4 = aload_sc1(flag_rsrc(p.ws), (FL_KR + (c.cb >> 5) * 64 + (lane & 3) * 4) * 4);
    issue(9);
    if (w == 3) gate_step(7);
    waitv<10>(); WAITL0(); bar();
    if (MODE == 0) {
        TIE(kf4);
        X.kr_ready = __all(all_tag(kf4, c.tag));
        rsrc_t rwd = mkrsrc(p.wd, F * D * 2);
        const int unit = c.cb * 3 + w, T = unit / 10, dl = unit % 10;
#pragma unroll
        for (int i = 0; i < 4; ++i) X.wd[i] = aload(rwd, w < 3 ? ((32 * T + 8 * i + (lane >> 3)) * D + 64 * dl + (lane & 7) * 8) * 2 : OOB);
    }
    if (w == 3) gate_step(8);
    if (MODE == 0) waitv<4>(); else waitv<0>();
    WAITL0(); bar();
    if (MODE == 0) {
        const int d = c.cb * 4 + w;
        const unsigned sl = c.ldsb + CV_BASE + w * CV_SLOT, vo = lane * 16;
        rsrc_t rf = mkrsrc(p.ssk, D * S * 2), r0 = mkrsrc(p.ws + WS_KR, D * KRS * 2);
#pragma unroll
        for (int i = 0; i < 4; ++i) dma16(sl + 512 + i * 1024, vo, rf, d * (S * 2) + i * 1024);
        if (X.kr_ready) {
#pragma unroll
            for (int i = 0; i < 4; ++i) dma16_sc1(sl + CV_KF + i * 1024, vo, r0, d * (KRS * 2) + i * 1024);
        }
        LAS char* slot = lds + CV_BASE + w * CV_SLOT;
        if (lane < 32) *(LAS u32x4*)(slot + lane * 16) = (u32x4){0, 0, 0, 0};
        else if (lane < 48) *(LAS u32x4*)(slot + CV_KF + ((lane - 32) >> 2) * CV_COPY + 4096 + (lane & 3) * 16) = (u32x4){0, 0, 0, 0};
    }
    if (w == 3) gate_step(9);
    WAITL0(); bar();
    waitv<0>();
    gacc = dpp_add<0x111>(gacc); gacc = dpp_add<0x112>(gacc); gacc = dpp_add<0x114>(gacc);
    if (w == 3) {
        rsrc_t rG = mkrsrc(p.ws + WS_G, S * 4);
        __builtin_amdgcn_raw_buffer_store_b32(__float_as_uint(sigmoidf(gacc)), rG, (lane & 7) == 7 ? (c.bs * 128 + grw) * 4 : OOB, 0, 16);
    }
    if (MODE == 0) {
#pragma unroll
        for (int i = 0; i < 4; ++i) TIE(X.wd[i]);
    }
}

__global__ __launch_bounds__(512, 2) void k_fused(Params p) {
    extern __shared__ __attribute__((aligned(16))) char smem[];
    LAS char* lds = (LAS char*)smem;
    const unsigned ldsb = (unsigned)(size_t)lds;
    const int wave = rfl(threadIdx.x >> 6), lane = threadIdx.x & 63;
    const int b = blockIdx.x, xcd = b & 7, idx = b >> 3;
    const bool has_conv = idx < 20;
    const int cb = xcd * 20 + idx, pb = xcd * 12 + idx - 20;
    const bool kvblock = !has_conv && pb < 80;
    const int bs = (xcd & 1) * 8 + (idx & 7), bf = (xcd >> 1) * 4 + (idx >> 3);
    const int st = xcd * 4 + (idx & 3), ds = idx >> 2;
    const bool hi4 = ((st >> 1) & 7) >= 4;
    const int ne = hi4 ? 6 : 8;
    unsigned long long order;
    {
        const unsigned E = hi4 ? 0x876210u : 0x98763210u, L = hi4 ? 0xBA9543u : 0xBA54u;
        const int nl = 12 - ne, re = ds % ne, rl = ds % nl;
        const unsigned long long Er = (((unsigned long long)E << (4 * ne) | E) >> (4 * re)) & ((1ull << (4 * ne)) - 1);
        const unsigned long long Lr = (((unsigned long long)L << (4 * nl) | L) >> (4 * rl)) & ((1ull << (4 * nl)) - 1);
        order = Er | (Lr << (4 * ne));
    }
    STAMPT(0);
    Ctx c; c.p = p; c.lds = lds; c.ldsb = ldsb; c.lane = lane; c.wave = wave; c.xcd = xcd; c.cb = cb; c.pb = pb; c.bs = bs; c.bf = bf; c.tag = 0;

    if (wave >= 4) {
        const int w = wave - 4;
        Tail X;
        X.kr_ready = false;
#pragma unroll
        for (int i = 0; i < 4; ++i) X.wd[i] = (u32x4){0, 0, 0, 0};
        if (has_conv) loader_gemm1<0>(c, X);
        else if (kvblock) loader_gemm1<1>(c, X);
        else loader_gemm1<2>(c, X);
        const unsigned tag = c.tag;
        STAMPT(4);
        const unsigned vo = lane * 16;
        rsrc_t rw = mkrsrc(p.ws + WS_WT, F * D * 2);
        if (has_conv) {
            LAS char* c0 = lds + CV_BASE + w * CV_SLOT + CV_KF;
            if (!X.kr_ready) {
                poll4<4>(p.ws, FL_KR + (cb >> 5) * 64, tag, lane);
                rsrc_t r0 = mkrsrc(p.ws + WS_KR, D * KRS * 2);
#pragma unroll
                for (int i = 0; i < 4; ++i) dma16_sc1(ldsb + CV_BASE + w * CV_SLOT + CV_KF + i * 1024, vo, r0, (cb * 4 + w) * (KRS * 2) + i * 1024);
                waitv<0>();
            }
            STAMPT(5);
#pragma unroll
            for (int j = 0; j < 4; ++j) {
                const u32x4 a = *(LAS u32x4*)(c0 + j * 1024 + lane * 16);
                const u32x2 nx = *(LAS u32x2*)(c0 + j * 1024 + lane * 16 + 16);
                *(LAS u32x4*)(c0 + CV_COPY + j * 1024 + lane * 16) =
                    (u32x4){__builtin_amdgcn_alignbit(a[1], a[0], 16), __builtin_amdgcn_alignbit(a[2], a[1], 16), __builtin_amdgcn_alignbit(a[3], a[2], 16), __builtin_amdgcn_alignbit(nx[0], a[3], 16)};
                *(LAS u32x4*)(c0 + 2 * CV_COPY + j * 1024 + lane * 16) = (u32x4){a[1], a[2], a[3], nx[0]};
                *(LAS u32x4*)(c0 + 3 * CV_COPY + j * 1024 + lane * 16) =
                    (u32x4){__builtin_amdgcn_alignbit(a[2], a[1], 16), __builtin_amdgcn_alignbit(a[3], a[2], 16), __builtin_amdgcn_alignbit(nx[0], a[3], 16), __builtin_amdgcn_alignbit(nx[1], nx[0], 16)};
            }
            const int unit = cb * 3 + w, Tk = unit / 10, dl = unit % 10, kh = Tk / 24, tt = (Tk % 24) >> 1, kk = Tk & 1;
            const int g4 = lane >> 4, q = (lane & 15) >> 2, pp = lane & 3, row = 4 * g4 + q;
            LAS char* buf = lds + WT_SCR + w * 4096;
#pragma unroll
            for (int i = 0; i < 4; ++i) *(LAS u32x4*)(buf + (8 * i + (lane >> 3)) * 128 + (lane & 7) * 16) = X.wd[i];
#pragma unroll
            for (int i = 0; i < 4; ++i) {
                const int d16 = 4 * dl + i, dsl = d16 / 5, n = d16 % 5;
                const s16x8 f = cat(trread(buf + row * 128 + 32 * i + 8 * pp), trread(buf + (row + 16) * 128 + 32 * i + 8 * pp));
                __builtin_amdgcn_raw_buffer_store_b128(__builtin_bit_cast(u32x4, f), rw, w < 3 ? ((((dsl * 12 + tt) * 4 + kh * 2 + kk) * 5 + n) * 64 + lane) * 16 : OOB, 0, 16);
            }
            waitv<4>();
        } else {
            waitv<0>();
        }
        WAITL0();
        bar();
        STAMPT(6);
        if (has_conv) {
            waitv<0>();
            if (lds_last(lds, L_CNT + 4, 4, lane) && lane < 8) st_flag(p.ws, FL_WT + lane * 256 + cb, tag);
        }
        rsrc_t rh = mkrsrc(p.ws + WS_H, S * F * 2);
        const unsigned sw2 = ds * (48 * 5 * 1024) + w * 5120, sh2 = ((st * 2 + (w & 1)) * 96 + (w >> 1) * 48) * 1024;
        auto stage_of = [&](int t) { return (int)((order >> (4 * t)) & 15); };
        auto w_dma = [&](int t) {
            const int s4 = t & 3;
            const unsigned dst = ldsb + (s4 < 2 ? G2_WA + s4 * G2_WST : G2_WB + (s4 - 2) * G2_WST) + w * 5120, src = sw2 + stage_of(t) * G2_WST;
#pragma unroll
            for (int j = 0; j < 5; ++j) dma16_sc1(dst + j * 1024, vo, rw, src + j * 1024);
        };
        auto h_dma = [&](int t) {
            const int s4 = t & 3;
            const unsigned dst = ldsb + (s4 < 2 ? G2_HA + s4 * G2_HST : G2_HB + (s4 - 2) * G2_HST) + w * 4096, src = sh2 + stage_of(t) * 4096;
#pragma unroll
            for (int j = 0; j < 4; ++j) dma16_sc1(dst + j * 1024, vo, rh, src + j * 1024);
        };
        const unsigned late_mask = hi4 ? 12u : 8u, hword = FL_H + (st >> 1) * 64;
        if (w == 0) {
            poll4<4>(p.ws, hword, tag, lane, 15u & ~late_mask);
            if (lane == 0) lds_set(lds, L_CNT + 20, 1u);
        } else {
            lds_wait(lds, L_CNT + 20);
        }
        STAMPT(10);
        h_dma(0); h_dma(1);
        if (w == 0) {
            poll4<40>(p.ws, FL_WT + xcd * 256, tag, lane);
            if (lane == 0) lds_set(lds, L_CNT + 8, 1u);
        } else {
            lds_wait(lds, L_CNT + 8);
        }
        STAMPT(9);
        w_dma(0); w_dma(1);
        const u32x4 hl = aload_sc1(flag_rsrc(p.ws), (hword + (lane & 3) * 4) * 4);
        {
#pragma unroll
            for (int i = 0; i < 2; ++i) {
                const int ln = (idx & 3) * 480 + (w * 2 + i) * 64 + lane;
                prefetch_lines(ldsb + L_DUMMY, rw, (w * 2 + i) * 64 + lane < 480 ? ds * (48 * 5 * 1024) + ln * 128 : OOB);
            }
        }
        waitv<2>();
        u32x4 hl_ = hl; TIE(hl_);
        const bool late_ok = __all(all_tag(hl_, tag));
        bar();
        STAMPT(7);
        h_dma(2); w_dma(2);
        bar();
        h_dma(3); w_dma(3);
        for (int t = 1; t <= 8; ++t) {
            waitv<9>(); bar();
            if (t + 3 == ne && !late_ok) poll4<4>(p.ws, hword, tag, lane);
            h_dma(t + 3); w_dma(t + 3);
        }
        rsrc_t ry = mkrsrc(p.ws + WS_YT, D * S * 2);
        const u32x4 yf = aload_sc1(flag_rsrc(p.ws), (FL_Y + ds * 64 + (lane < 5 ? lane : 0) * 4) * 4);
        waitv<10>(); bar();
        waitv<0>();
        u32x4 yf_ = yf; TIE(yf_);
        if (!__all(all_tag(yf_, tag))) poll4<5>(p.ws, FL_Y + ds * 64, tag, lane);
        u32x4 yv[3];
#pragma unroll
        for (int i = 0; i < 3; ++i) {
            const int it = w + 4 * i, ch = 8 * it + (lane >> 3);
            yv[i] = aload_sc1(ry, it < 10 ? ((80 * ds + ch) * S + 64 * st + (lane & 7) * 8) * 2 : OOB);
        }
        bar();
        bar();
        waitv<0>();
#pragma unroll
        for (int i = 0; i < 3; ++i) {
            const int it = w + 4 * i, ch = 8 * it + (lane >> 3);
            TIE(yv[i]);
            if (it < 10) *(LAS u32x4*)(lds + G2_Y + ch * 128 + (lane & 7) * 16) = yv[i];
        }
        bar();
        WAITL0();
        bar();
        if (b == 0 && w == 0) {
            rsrc_t r = flag_rsrc(p.ws);
            for (int spins = 0; spins < (1 << 18); ++spins) {
                u32x4 x;
                asm volatile("buffer_load_dwordx4 %0, %1, %2, 0 offen sc1\n\ts_waitcnt vmcnt(0)" : "=&v"(x) : "v"((FL_H + (lane >> 2) * 64 + (lane & 3) * 4) * 4), "s"(r) : "memory");
                if (__all(all_tag(x, tag))) break;
                __builtin_amdgcn_s_sleep(4);
            }
            if (lane == 0) st_flag(p.ws, FL_EPOCH, tag);
        }
        STAMPT(8);
    } else {
        unsigned tag = 0;
        float Dd = bflo(p.sd[(has_conv ? cb * 4 : 0) + wave]);
        asm volatile("" : "+v"(Dd));
        {
            const int wsx = wave & 1, wf = wave >> 1;
            const int g = lane >> 4, fr = lane & 15, q = (lane & 15) >> 2, pp = lane & 3;
            int oa[3];
#pragma unroll
            for (int i = 0; i < 3; ++i) {
                const int ft = 3 * wf + i, cc = ft >> 1, half = ft & 1;
                oa[i] = 16384 + (g * 3 + cc) * 512 + q * 64 + 16 * ((2 * half + (pp >> 1)) ^ (2 * (g & 1))) + 8 * (pp & 1);
            }
            const int ob0 = (64 * wsx + fr) * 128 + ((g ^ (fr >> 1)) * 16);
            const int ob1 = (64 * wsx + fr) * 128 + ((g ^ (fr >> 1) ^ 4) * 16);
            f32x4 accg[3][4], accu[3][4];
#pragma unroll
            for (int i = 0; i < 3; ++i)
#pragma unroll
                for (int m = 0; m < 4; ++m) { accg[i][m] = (f32x4){0, 0, 0, 0}; accu[i][m] = (f32x4){0, 0, 0, 0}; }
            struct Fr { s16x8 a[6]; s16x8 b[4]; } fa, fb;
            auto rd = [&](Fr& f, int ks, int sb) {
#pragma unroll
                for (int m = 0; m < 4; ++m) f.b[m] = *(LAS s16x8*)(lds + sb + (ks ? ob1 : ob0) + m * 2048);
#pragma unroll
                for (int i = 0; i < 3; ++i) {
                    f.a[i] = cat(trread(lds + sb + oa[i] + ks * 6144), trread(lds + sb + oa[i] + ks * 6144 + 256));
                    f.a[3 + i] = cat(trread(lds + sb + oa[i] + ks * 6144 + 12288), trread(lds + sb + oa[i] + ks * 6144 + 256 + 12288));
                }
            };
            auto mm = [&](const Fr& f) {
#pragma unroll
                for (int i = 0; i < 3; ++i)
#pragma unroll
                    for (int m = 0; m < 4; ++m) {
                        accg[i][m] = mfma16(f.a[i], f.b[m], accg[i][m]);
                        accu[i][m] = mfma16(f.a[3 + i], f.b[m], accu[i][m]);
                    }
            };
            auto interleave = [&]() {
#pragma unroll
                for (int i = 0; i < 16; ++i) { SGB(0x008, 1); SGB(0x100, 1); }
                SGB(0x008, 8);
                SB0();
            };
            const int pi = kvblock ? pb : (has_conv ? cb : 160 + pb - 80), np = kvblock ? 80 : 176, t0 = kvblock ? 6 : 2;
            if (kvblock) { __builtin_amdgcn_s_sleep(SLEEP_K); kv_prepare(c); } else __builtin_amdgcn_s_sleep(SLEEP_C);
            {
                rsrc_t rpf = mkrsrc(wave == 0 ? p.q : (wave == 1 ? p.wg : p.wu), wave == 0 ? S * D * 2 : D * F * 2);
                const int nl = wave == 0 ? 8192 : 6144;
                for (int l0 = pi * 64; l0 < nl && wave < 3; l0 += np * 64) {
                    const int l = l0 + lane;
                    prefetch_lines(ldsb + L_DUMMY, rpf, wave == 0 ? (l >> 2) * 1280 + (t0 + (l & 3)) * 128 : (t0 * 64 + l / 24) * 3072 + (l % 24) * 128);
                }
                if (kvblock && wave == 3) {
                    rsrc_t rwd = mkrsrc(p.wd, F * D * 2), rf = mkrsrc(p.ssk, D * S * 2);
                    for (int l0 = pb * 64; l0 < 15360; l0 += 80 * 64) prefetch_lines(ldsb + L_DUMMY, rwd, (l0 + lane) * 128);
                    for (int l0 = pb * 64; l0 < 20480; l0 += 80 * 64) prefetch_lines(ldsb + L_DUMMY, rf, (l0 + lane) * 128);
                }
            }
            __builtin_amdgcn_s_setprio(1);
            bar();
            STAMPT(3);
            rd(fa, 0, 0);
            int sb = 0;
            for (int t = 0; t < 10; ++t) {
                rd(fb, 1, sb); mm(fa); interleave();
                WAITL0();
                bar();
                sb = (sb == 2 * G1_STAGE) ? 0 : sb + G1_STAGE;
                if (t < 9) rd(fa, 0, sb);
                mm(fb); interleave();
            }
            STAMPT(4);
            tag = get_tag(c);
            rsrc_t rh = mkrsrc(p.ws + WS_H, S * F * 2);
#pragma unroll
            for (int mp = 0; mp < 2; ++mp)
#pragma unroll
                for (int i = 0; i < 3; ++i) {
                    float hv[8];
#pragma unroll
                    for (int e = 0; e < 8; ++e) hv[e] = accu[i][2 * mp + (e >> 2)][e & 3] * sigmoidf(accg[i][2 * mp + (e >> 2)][e & 3]);
                    const u32x4 o = {cvtpk(hv[0], hv[1]), cvtpk(hv[2], hv[3]), cvtpk(hv[4], hv[5]), cvtpk(hv[6], hv[7])};
                    const int sp = bs * 4 + wsx * 2 + mp, ft = bf * 6 + wf * 3 + i;
                    __builtin_amdgcn_raw_buffer_store_b128(o, rh, ((sp * 96 + ft) * 64 + lane) * 16, 0, 16);
                }
        }
        STAMPT(5);
        auto publish_h = [&]() {
            waitv<0>();
            if (lds_last(lds, L_CNT, 4, lane) && lane == 0) st_flag(p.ws, FL_H + bs * 64 + bf, tag);
        };
        bar();
        STAMPT(6);
        publish_h();
        f32x4 cacc[8];
        u32x2 ckv[8];
#pragma unroll
        for (int n = 0; n < 8; ++n) { cacc[n] = (f32x4){0, 0, 0, 0}; ckv[n] = (u32x2){0, 0}; }
        const int ca = lane & 15, cg = lane >> 4;
        if (has_conv) {
            const unsigned slot = ldsb + CV_BASE + wave * CV_SLOT;
            unsigned pg = slot + 480 + 32 * ca + 16 * cg;
            unsigned pf = slot + CV_KF + (3 - (ca & 3)) * CV_COPY + 472 + 16 * cg - 8 * (ca >> 2);
            struct Res { s16x8 G[8]; s16x4 Fl[8], Fh[8]; } RA, RB;
            auto rd1 = [&](Res& R, int k) {
                const int j = k / 3, m = k % 3;
                if (m == 0) asm volatile("ds_read_b64 %0, %1 offset:%2" : "=v"(R.Fl[j]) : "v"(pf), "n"(512 * (7 - j)));
                else if (m == 1) asm volatile("ds_read_b64 %0, %1 offset:%2" : "=v"(R.Fh[j]) : "v"(pf), "n"(512 * (7 - j) + 8));
                else asm volatile("ds_read_b128 %0, %1 offset:%2" : "=v"(R.G[j]) : "v"(pg), "n"(512 * j));
            };
            auto land = [&](Res& R) {
                asm volatile("s_waitcnt lgkmcnt(0)"
                             : "+v"(R.G[0]), "+v"(R.G[1]), "+v"(R.G[2]), "+v"(R.G[3]), "+v"(R.G[4]), "+v"(R.G[5]), "+v"(R.G[6]), "+v"(R.G[7]),
                               "+v"(R.Fl[0]), "+v"(R.Fl[1]), "+v"(R.Fl[2]), "+v"(R.Fl[3]), "+v"(R.Fl[4]), "+v"(R.Fl[5]), "+v"(R.Fl[6]), "+v"(R.Fl[7]));
                asm volatile(""
                             : "+v"(R.Fh[0]), "+v"(R.Fh[1]), "+v"(R.Fh[2]), "+v"(R.Fh[3]), "+v"(R.Fh[4]), "+v"(R.Fh[5]), "+v"(R.Fh[6]), "+v"(R.Fh[7]));
                SB0();
            };
            auto residue = [&](Res& R, Res& N) {
                pg -= 64; pf -= 64;
                int k = 0;
#pragma unroll
                for (int I = 0; I < 8; ++I) {
                    const s16x8 Fv = cat(R.Fl[I], R.Fh[I]);
#pragma unroll
                    for (int n = I; n < 8; ++n) {
                        cacc[n] = mfma16(Fv, R.G[n - I], cacc[n]);
                        if (k < 24) rd1(N, k);
                        ++k;
                        SB0();
                    }
                }
                land(N);
            };
#pragma unroll
            for (int k = 0; k < 24; ++k) rd1(RA, k);
            land(RA);
            for (int r2 = 0; r2 < 4; ++r2) {
                residue(RA, RB);
                residue(RB, RA);
            }
#pragma unroll
            for (int n = 0; n < 8; ++n) ckv[n] = *(LAS u32x2*)(lds + CV_BASE + wave * CV_SLOT + CV_KF + 2 * (2044 - 256 * n - 16 * ca - 4 * cg));
        }
        STAMPT(7);
        {
            const int sp = wave & 1, kh = wave >> 1;
            const int s16 = lane & 15, g = lane >> 4, q = s16 >> 2, pp = lane & 3;
            const int srow = 64 * st + 32 * sp + 16 * kh + s16;
            f32x4 acc[2][5];
#pragma unroll
            for (int n = 0; n < 5; ++n) { acc[0][n] = (f32x4){0, 0, 0, 0}; acc[1][n] = (f32x4){0, 0, 0, 0}; }
            const unsigned pw = ldsb + kh * 10240 + lane * 16, ph = ldsb + wave * 4096 + lane * 16;
            struct Fr2 { s16x8 w[5]; u32x4 h[2]; } fa, fb;
            auto rd2 = [&](Fr2& f, int s4, int kk) {
                LAS char* bh = (LAS char*)(size_t)(ph + (s4 < 2 ? G2_HA + s4 * G2_HST : G2_HB + (s4 - 2) * G2_HST) + kk * 2048);
                LAS char* bw = (LAS char*)(size_t)(pw + (s4 < 2 ? G2_WA + s4 * G2_WST : G2_WB + (s4 - 2) * G2_WST) + kk * 5120);
                f.h[0] = *(LAS u32x4*)bh; f.h[1] = *(LAS u32x4*)(bh + 1024);
#pragma unroll
                for (int n = 0; n < 5; ++n) f.w[n] = *(LAS s16x8*)(bw + n * 1024);
            };
            auto swp = [&](Fr2& f) {
                unsigned a2 = f.h[0][2], a3 = f.h[0][3], b0 = f.h[1][0], b1 = f.h[1][1];
                asm volatile("v_swap_b32 %0, %1\n\tv_swap_b32 %2, %3\n\ts_nop 1" : "+v"(a2), "+v"(b0), "+v"(a3), "+v"(b1));
                f.h[0][2] = a2; f.h[0][3] = a3; f.h[1][0] = b0; f.h[1][1] = b1;
            };
            auto step = [&](const Fr2& f) {
                const s16x8 h0 = __builtin_bit_cast(s16x8, f.h[0]), h1 = __builtin_bit_cast(s16x8, f.h[1]);
#pragma unroll
                for (int n = 0; n < 5; ++n) { acc[0][n] = mfma16(f.w[n], h0, acc[0][n]); acc[1][n] = mfma16(f.w[n], h1, acc[1][n]); }
            };
            auto il2 = [&]() {
#pragma unroll
                for (int i = 0; i < 7; ++i) { SGB(0x008, 1); SGB(0x100, 1); }
                SGB(0x008, 3);
                SB0();
            };
            WAITL0();
            bar();
            STAMPT(8);
            float gt = 0.f;
            u32x2 qv[5];
#pragma unroll
            for (int n = 0; n < 5; ++n) qv[n] = (u32x2){0, 0};
            rd2(fa, 0, 0);
            swp(fa);
            for (int t = 0; t < 12; ++t) {
                rd2(fb, t & 3, 1);
                step(fa);
                il2();
                swp(fb);
                SB0();
                if (t == 4) waitv<0>();
                bar();
                if (t == 0 && has_conv) {
                    const int d = cb * 4 + wave;
                    rsrc_t ry = mkrsrc(p.ws + WS_YT, D * S * 2);
                    u32x2 o[8];
#pragma unroll
                    for (int n = 0; n < 8; ++n) {
                        o[n][0] = cvtpk(cacc[n][0] + bfhi(ckv[n][1]) * Dd, cacc[n][1] + bflo(ckv[n][1]) * Dd);
                        o[n][1] = cvtpk(cacc[n][2] + bfhi(ckv[n][0]) * Dd, cacc[n][3] + bflo(ckv[n][0]) * Dd);
                    }
#pragma unroll
                    for (int n = 0; n < 8; n += 2) {
                        auto x = __builtin_amdgcn_permlane16_swap(o[n][0], o[n + 1][0], false, false);
                        auto y = __builtin_amdgcn_permlane16_swap(o[n][1], o[n + 1][1], false, false);
                        const u32x4 val = {x[0], y[0], x[1], y[1]};
                        const int s = 256 * (n + (cg & 1)) + 16 * ca + 4 * (cg & 2);
                        __builtin_amdgcn_raw_buffer_store_b128(val, ry, (d * S + s) * 2, 0, 16);
                    }
                    SB0();
                }
                if (t == 4 && has_conv && wave == 0 && lane == 0) st_flag(p.ws, FL_Y + (cb / 20) * 64 + cb % 20, tag);
                if (t == 9) {
                    gt = __hip_atomic_load((const GAS float*)(p.ws + WS_G) + srow, RLX_AGENT);
#pragma unroll
                    for (int n = 0; n < 5; ++n) qv[n] = *(const u32x2*)(p.q + (size_t)srow * D + 80 * ds + 16 * n + 4 * g);
                }
                rd2(fa, (t + 1) & 3, 0);
                step(fb);
                il2();
                swp(fa);
                SB0();
            }
            WAITL0();
            bar();
            STAMPT(10);
#pragma unroll
            for (int n = 0; n < 5; ++n) *(LAS f32x4*)(lds + G2_RED + (wave * 5 + n) * 1024 + lane * 16) = kh ? acc[0][n] : acc[1][n];
            WAITL0();
            bar();
            STAMPT(11);
#pragma unroll
            for (int n = 0; n < 5; ++n) {
                const f32x4 o4 = (kh ? acc[1][n] : acc[0][n]) + *(LAS f32x4*)(lds + G2_RED + ((wave ^ 2) * 5 + n) * 1024 + lane * 16);
                const u32x2 yv = __builtin_bit_cast(u32x2, trread(lds + G2_Y + (16 * n + 4 * g + q) * 128 + (32 * sp + 16 * kh + 4 * pp) * 2));
                const float o0 = gt * (bflo(qv[n][0]) * bflo(yv[0])) + (1.f - gt) * o4[0];
                const float o1 = gt * (bfhi(qv[n][0]) * bfhi(yv[0])) + (1.f - gt) * o4[1];
                const float o2 = gt * (bflo(qv[n][1]) * bflo(yv[1])) + (1.f - gt) * o4[2];
                const float o3 = gt * (bfhi(qv[n][1]) * bfhi(yv[1])) + (1.f - gt) * o4[3];
                *(u32x2*)(p.out + (size_t)srow * D + 80 * ds + 16 * n + 4 * g) = (u32x2){cvtpk(o0, o1), cvtpk(o2, o3)};
            }
            STAMPT(12);
        }
    }
}

extern "C" void kernel_launch(void* const* d_in, const int* in_sizes, int n_in,
                              void* d_out, int out_size, void* d_ws, size_t ws_size,
                              hipStream_t stream) {
    Params p;
    p.q = (const u16*)d_in[0]; p.k = (const u16*)d_in[1]; p.v = (const u16*)d_in[2];
    p.sk = (const u16*)d_in[3]; p.ssk = (const u16*)d_in[4]; p.sd = (const u16*)d_in[5];
    p.wg = (const u16*)d_in[7]; p.wu = (const u16*)d_in[8]; p.wd = (const u16*)d_in[9]; p.gw = (const u16*)d_in[10];
    p.out = (u16*)d_out; p.ws = (char*)d_ws;
    hipFuncSetAttribute((const void*)k_fused, hipFuncAttributeMaxDynamicSharedMemorySize, LDS_BYTES);
    k_fused<<<256, 512, LDS_BYTES, stream>>>(p);
}
```

```cpp
#include <hip/hip_runtime.h>
#include <hip/hip_bf16.h>
#include <stdint.h>

typedef unsigned short u16;
typedef short s16x4 __attribute__((ext_vector_type(4)));
typedef short s16x8 __attribute__((ext_vector_type(8)));
typedef __bf16 b16x8 __attribute__((ext_vector_type(8)));
typedef __bf16 b16x2 __attribute__((ext_vector_type(2)));
typedef float f32x2 __attribute__((ext_vector_type(2)));
typedef float f32x4 __attribute__((ext_vector_type(4)));
typedef unsigned u32x2 __attribute__((ext_vector_type(2)));
typedef unsigned u32x4 __attribute__((ext_vector_type(4)));
#define LAS __attribute__((address_space(3)))
#define GAS __attribute__((address_space(1)))
#define DI __device__ __forceinline__

static constexpr int S = 2048, D = 640, F = 1536;
static constexpr int KRS = 2112;
static constexpr unsigned WS_H = 0, WS_KR = 8u << 20, WS_YT = 14u << 20, WS_G = 17u << 20, WS_WT = 18u << 20, WS_STAMP = 20u << 20, WS_FLAG = 24u << 20;
static constexpr unsigned FL_EPOCH = 0, FL_KR = 1024, FL_WT = 2048  , FL_H = 4096, FL_Y = 6144;
static constexpr unsigned OOB = 0xF0000000u;

struct Params {
    const u16 *q, *k, *v, *sk, *ssk, *sd, *wg, *wu, *wd, *gw;
    u16* out; char* ws;
};

DI float bflo(unsigned x) { return __uint_as_float(x << 16); }
DI float bfhi(unsigned x) { return __uint_as_float(x & 0xffff0000u); }
DI unsigned cvtpk(float lo, float hi) { f32x2 v = {lo, hi}; b16x2 b = __builtin_convertvector(v, b16x2); return __builtin_bit_cast(unsigned, b); }
DI unsigned rfl(unsigned x) { return __builtin_amdgcn_readfirstlane(x); }
DI float sigmoidf(float x) { return __builtin_amdgcn_rcpf(1.f + __builtin_amdgcn_exp2f(x * -1.44269504089f)); }
DI f32x4 mfma16(s16x8 a, s16x8 b, f32x4 c) {
    return __builtin_amdgcn_mfma_f32_16x16x32_bf16(__builtin_bit_cast(b16x8, a), __builtin_bit_cast(b16x8, b), c, 0, 0, 0);
}
DI s16x4 trread(LAS char* p) { return __builtin_amdgcn_ds_read_tr16_b64_v4i16((LAS s16x4*)p); }
DI s16x8 cat(s16x4 a, s16x4 b) { return __builtin_shufflevector(a, b, 0, 1, 2, 3, 4, 5, 6, 7); }
typedef __amdgpu_buffer_rsrc_t rsrc_t;
DI rsrc_t mkrsrc(const void* p, unsigned bytes) { return __builtin_amdgcn_make_buffer_rsrc((void*)p, 0, bytes, 0x00020000); }
DI void dma16(unsigned m0v, unsigned voff, rsrc_t r, unsigned soff) {
    asm volatile("s_mov_b32 m0, %0\n\ts_nop 0\n\tbuffer_load_dwordx4 %1, %2, %3 offen lds" ::"s"(m0v), "v"(voff), "s"(r), "s"(soff) : "m0", "memory");
}
DI void dma16_sc1(unsigned m0v, unsigned voff, rsrc_t r, unsigned soff) {
    asm volatile("s_mov_b32 m0, %0\n\ts_nop 0\n\tbuffer_load_dwordx4 %1, %2, %3 offen sc1 lds" ::"s"(m0v), "v"(voff), "s"(r), "s"(soff) : "m0", "memory");
}
DI u32x4 aload(rsrc_t r, unsigned voff) { u32x4 x; asm volatile("buffer_load_dwordx4 %0, %1, %2, 0 offen" : "=v"(x) : "v"(voff), "s"(r) : "memory"); return x; }
DI u32x4 aload_sc1(rsrc_t r, unsigned voff) { u32x4 x; asm volatile("buffer_load_dwordx4 %0, %1, %2, 0 offen sc1" : "=v"(x) : "v"(voff), "s"(r) : "memory"); return x; }
DI void prefetch_lines(unsigned scratch, rsrc_t r, unsigned voff) {
    asm volatile("s_mov_b32 m0, %0\n\ts_nop 0\n\tbuffer_load_dword %1, %2, 0 offen lds" ::"s"(scratch), "v"(voff), "s"(r) : "m0", "memory");
}
template <int N> DI void waitv() { asm volatile("s_waitcnt vmcnt(%0)" ::"n"(N) : "memory"); }
DI float dot2(unsigned a, unsigned b, float c) { return __builtin_amdgcn_fdot2_f32_bf16(__builtin_bit_cast(b16x2, a), __builtin_bit_cast(b16x2, b), c, false); }
template <int CTRL> DI float dpp_add(float x) { return x + __int_as_float(__builtin_amdgcn_update_dpp(0, __float_as_int(x), CTRL, 0xf, 0xf, true)); }
#define TIE(x) asm volatile("" : "+v"(x))
#define WAITL0() asm volatile("s_waitcnt lgkmcnt(0)" ::: "memory")
DI void bar() { asm volatile("" ::: "memory"); __builtin_amdgcn_s_barrier(); asm volatile("" ::: "memory"); }
#define SGB(m, n) __builtin_amdgcn_sched_group_barrier(m, n, 0)
#define SB0() __builtin_amdgcn_sched_barrier(0)
#define RLX_AGENT __ATOMIC_RELAXED, __HIP_MEMORY_SCOPE_AGENT

#define SLEEP_L 0
#define SLEEP_LK 12
#define SLEEP_K 0
#define SLEEP_C 12

DI void st_flag(char* ws, unsigned word, unsigned tag) { __hip_atomic_store((GAS unsigned*)(ws + WS_FLAG) + word, tag, RLX_AGENT); }
DI rsrc_t flag_rsrc(char* ws) { return mkrsrc(ws + WS_FLAG, 32768); }
DI bool all_tag(u32x4 x, unsigned tag) { return x[0] == tag && x[1] == tag && x[2] == tag && x[3] == tag; }
template <int NL>
DI void poll4(char* ws, unsigned word, unsigned tag, int lane, unsigned care = 15u) {
    rsrc_t r = flag_rsrc(ws);
    for (int spins = 0; spins < (1 << 18); ++spins) {
        u32x4 x;
        asm volatile("buffer_load_dwordx4 %0, %1, %2, 0 offen sc1\n\ts_waitcnt vmcnt(0)" : "=&v"(x) : "v"((word + (lane < NL ? lane : 0) * 4) * 4), "s"(r) : "memory");
        const bool ok = (x[0] == tag || !(care & 1)) && (x[1] == tag || !(care & 2)) && (x[2] == tag || !(care & 4)) && (x[3] == tag || !(care & 8));
        if (__all(ok)) return;
        __builtin_amdgcn_s_sleep(4);
    }
}
DI void lds_set(LAS char* lds, int off, unsigned v) { __hip_atomic_store((LAS unsigned*)(lds + off), v, __ATOMIC_RELAXED, __HIP_MEMORY_SCOPE_WORKGROUP); }
DI unsigned lds_get(LAS char* lds, int off) { return rfl(__hip_atomic_load((LAS unsigned*)(lds + off), __ATOMIC_RELAXED, __HIP_MEMORY_SCOPE_WORKGROUP)); }
DI unsigned lds_wait(LAS char* lds, int off) {
    unsigned v = 0;
    for (int spins = 0; spins < (1 << 22); ++spins) {
        v = lds_get(lds, off);
        if (v) break;
        __builtin_amdgcn_s_sleep(8);
    }
    asm volatile("" ::: "memory");
    return v;
}
DI bool lds_last(LAS char* lds, int off, unsigned n, int lane) {
    unsigned old = 0;
    if (lane == 0) old = __hip_atomic_fetch_add((LAS unsigned*)(lds + off), 1u, __ATOMIC_RELAXED, __HIP_MEMORY_SCOPE_WORKGROUP);
    return rfl(old) == n - 1;
}

#define STAMPT(i) do {} while (0)

constexpr int G1_STAGE = 40960;
constexpr int P0_LDS = 122880;
constexpr int TP = 272;
constexpr int P0_T = P0_LDS, P0_GW = P0_LDS + 128 * TP;
constexpr int CV_BASE = G1_STAGE;
constexpr int CV_COPY = 4160, CV_KF = 4608, CV_SLOT = CV_KF + 2 * CV_COPY;
constexpr int G2_WST = 20480, G2_HST = 16384;
constexpr int G2_W0 = 0, G2_W2 = CV_BASE + 4 * CV_SLOT, G2_W3 = CV_BASE;
constexpr int G2_H0 = G2_W2 + G2_WST, G2_H3 = G2_W3 + G2_WST;
constexpr int G2_Y = G2_H3 + G2_HST;
constexpr int G2_Q = G2_H0, G2_G = G2_H0 + 12288;
constexpr int WT_SCR = G2_H0;
constexpr int G2_RED = 0;
constexpr unsigned long long G2_WTAB = (unsigned long long)(G2_W0 >> 8) | (unsigned long long)((G2_W0 + G2_WST) >> 8) << 16 | (unsigned long long)(G2_W2 >> 8) << 32 | (unsigned long long)(G2_W3 >> 8) << 48;
constexpr unsigned long long G2_HTAB = (unsigned long long)(G2_H0 >> 8) | (unsigned long long)((G2_H0 + G2_HST) >> 8) << 16 | (unsigned long long)((G2_H0 + 2 * G2_HST) >> 8) << 32 | (unsigned long long)(G2_H3 >> 8) << 48;
DI int wslot(int s4) { return (int)((G2_WTAB >> (16 * s4)) & 0xFFFF) << 8; }
DI int hslot(int s4) { return (int)((G2_HTAB >> (16 * s4)) & 0xFFFF) << 8; }
constexpr int L_DUMMY = 163328;
constexpr int L_CNT = 163776;
constexpr int L_EPOCH = 163824;
constexpr int LDS_BYTES = 163840;

struct Ctx {
    Params p; LAS char* lds; unsigned ldsb; int lane, wave, xcd, cb, pb, bs, bf; unsigned tag;
};
DI void load_epoch(const Ctx& c) {
    rsrc_t r = mkrsrc(c.p.ws + WS_FLAG, 64);
    if (c.lane == 0) asm volatile("s_mov_b32 m0, %0\n\ts_nop 0\n\tbuffer_load_dword %1, %2, 0 offen sc1 lds" ::"s"(c.ldsb + L_EPOCH), "v"(0u), "s"(r) : "m0", "memory");
}
DI unsigned get_tag(const Ctx& c) { return lds_get(c.lds, L_EPOCH) + 1u; }

DI void kv_prepare(const Ctx& c) {
    const Params& p = c.p;
    LAS char* lds = c.lds;
    const int lane = c.lane, wave = c.wave;
    (void)wave;
    const int kdc = c.pb % 5, s0 = (c.pb / 5) * 128, d0 = kdc * 128, tid = c.wave * 64 + lane, c16 = tid & 15, rg = tid >> 4, s = s0 + 8 * rg;
    u32x4 kr[11], vr[8], w4[4];
    {
        rsrc_t rk = mkrsrc(p.k, S * D * 2), rv = mkrsrc(p.v, S * D * 2), rsk = mkrsrc(p.sk, D * 8);
#pragma unroll
        for (int i = 0; i < 11; ++i) kr[i] = aload(rk, (unsigned)(((s - 3 + i) * D + d0 + 8 * c16) * 2));
#pragma unroll
        for (int i = 0; i < 8; ++i) vr[i] = aload(rv, ((s + i) * D + d0 + 8 * c16) * 2);
#pragma unroll
        for (int i = 0; i < 4; ++i) w4[i] = aload(rsk, (d0 + 8 * c16) * 8 + i * 16);
    }
    waitv<0>();
    STAMPT(2);
#pragma unroll
    for (int i = 0; i < 11; ++i) TIE(kr[i]);
#pragma unroll
    for (int i = 0; i < 8; ++i) TIE(vr[i]);
#pragma unroll
    for (int i = 0; i < 4; ++i) TIE(w4[i]);
    LAS char* tw = lds + P0_T + 8 * c16 * TP + (((15 - rg) ^ c16) * 16);
#pragma unroll
    for (int e = 0; e < 8; ++e) {
        const unsigned sel = (e & 1) ? 0x07060302u : 0x05040100u;
        const unsigned wa = w4[e >> 1][(e & 1) * 2], wb = w4[e >> 1][(e & 1) * 2 + 1];
        unsigned pr[10];
#pragma unroll
        for (int i = 0; i < 10; ++i) pr[i] = __builtin_amdgcn_perm(kr[i][e >> 1], kr[i + 1][e >> 1], sel);
        float o[8];
#pragma unroll
        for (int r = 0; r < 8; ++r) o[r] = dot2(pr[r + 2], wa, dot2(pr[r], wb, 0.f)) * ((e & 1) ? bfhi(vr[r][e >> 1]) : bflo(vr[r][e >> 1]));
        *(LAS u32x4*)(tw + e * TP) = (u32x4){cvtpk(o[7], o[6]), cvtpk(o[5], o[4]), cvtpk(o[3], o[2]), cvtpk(o[1], o[0])};
    }
    STAMPT(13);
    WAITL0(); bar();
    rsrc_t r0 = mkrsrc(p.ws + WS_KR, D * KRS * 2);
#pragma unroll
    for (int it = 0; it < 8; ++it) {
        const int item = tid + it * 256, ch = item >> 4, j = item & 15;
        __builtin_amdgcn_raw_buffer_store_b128(*(LAS u32x4*)(lds + P0_T + ch * TP + ((j ^ ((ch >> 3) & 15)) * 16)), r0, ((d0 + ch) * KRS + (1920 - s0) + 8 * j) * 2, 0, 16);
    }
    STAMPT(14);
    waitv<0>();
    if (lds_last(lds, L_CNT + 16, 4, lane) && lane == 0) st_flag(p.ws, FL_KR + kdc * 64 + c.pb / 5, get_tag(c));
    STAMPT(15);
}

struct Tail { u32x4 wd[4]; bool kr_ready; };
template <int MODE>
DI void loader_gemm1(Ctx& c, Tail& X) {
    const Params& p = c.p;
    const int lane = c.lane, w = c.wave - 4, wave = c.wave;
    (void)wave;
    LAS char* lds = c.lds;
    rsrc_t rq = mkrsrc(p.q, S * D * 2), rg_ = mkrsrc(p.wg, D * F * 2), ru_ = mkrsrc(p.wu, D * F * 2);
    unsigned vx[2], vw[3];
    {
        const int r = lane >> 3, slot = lane & 7;
        vx[0] = r * 1280 + ((slot ^ (r >> 1)) * 16);
        vx[1] = r * 1280 + ((slot ^ (r >> 1) ^ 4) * 16);
#pragma unroll
        for (int j = 0; j < 3; ++j) {
            const int L = 2 * j + (lane >> 5), kbl = L / 3, cc = L % 3, row = (lane & 31) >> 2, ch = (lane & 3) ^ (2 * kbl);
            vw[j] = (kbl * 8 + row) * 3072 + cc * 64 + ch * 16;
        }
    }
    const unsigned sx0 = (c.bs * 128 + 32 * w) * 1280, sw0 = 16 * w * 3072 + c.bf * 192;
    auto issue = [&](int t) {
        const unsigned base = c.ldsb + (int)((0x24924924u >> (2 * t)) & 3) * G1_STAGE;
#pragma unroll
        for (int i = 0; i < 4; ++i) dma16(base + (32 * w + 8 * i) * 128, vx[i & 1], rq, sx0 + i * 8 * 1280 + t * 128);
#pragma unroll
        for (int j = 0; j < 3; ++j) dma16(base + 16384 + w * 3072 + j * 1024, vw[j], rg_, sw0 + t * 64 * 3072);
#pragma unroll
        for (int j = 0; j < 3; ++j) dma16(base + 28672 + w * 3072 + j * 1024, vw[j], ru_, sw0 + t * 64 * 3072);
    };
    if (w == 0) load_epoch(c);
    if (w < 2) {
        rsrc_t rgw = mkrsrc(p.gw, D * 2);
        if (w == 0 || lane < 16) dma16(c.ldsb + P0_GW + w * 1024, (w * 64 + lane) * 16, rgw, 0);
    }
    if (w == 3 && lane < 8) *(LAS unsigned*)(lds + L_CNT + lane * 4) = 0;
    if (MODE == 1) {
        waitv<0>();
        __builtin_amdgcn_s_sleep(SLEEP_K + SLEEP_LK);
        issue(0); issue(1);
        WAITL0(); bar();
    } else {
        __builtin_amdgcn_s_sleep(SLEEP_L);
        issue(0);
    }
    if (MODE == 1) waitv<10>(); else waitv<0>();
    WAITL0(); bar();
    c.tag = get_tag(c);
    STAMPT(3);
    if (MODE != 1) issue(1);
    issue(2);
    const int grw = 8 * c.bf + (lane >> 3), gch = (lane & 7) ^ ((grw >> 1) & 7);
    float gacc = 0.f;
    auto gate_step = [&](int t) {
        const int buf = (int)((0x24924924u >> (2 * t)) & 3);
        const u32x4 xv = *(LAS u32x4*)(lds + buf * G1_STAGE + grw * 128 + (lane & 7) * 16);
        const u32x4 gv = *(LAS u32x4*)(lds + P0_GW + t * 128 + gch * 16);
#pragma unroll
        for (int i = 0; i < 4; ++i) gacc = dot2(xv[i], gv[i], gacc);
    };
    if (w == 3) gate_step(0);
#pragma unroll 1
    for (int t = 0; t < 6; ++t) {
        waitv<10>(); WAITL0(); bar();
        issue(t + 3);
        if (w == 3) gate_step(t + 1);
    }
    u32x4 kf4 = {0, 0, 0, 0};
    waitv<10>(); WAITL0(); bar();
    if (MODE == 0) kf4 = aload_sc1(flag_rsrc(p.ws), (FL_KR + (c.cb >> 5) * 64 + (lane & 3) * 4) * 4);
    issue(9);
    if (w == 3) gate_step(7);
    waitv<10>(); WAITL0(); bar();
    if (MODE == 0) {
        TIE(kf4);
        X.kr_ready = __all(all_tag(kf4, c.tag));
        rsrc_t rwd = mkrsrc(p.wd, F * D * 2);
        const int unit = c.cb * 3 + w, T = unit / 10, dl = unit % 10;
#pragma unroll
        for (int i = 0; i < 4; ++i) X.wd[i] = aload(rwd, w < 3 ? ((32 * T + 8 * i + (lane >> 3)) * D + 64 * dl + (lane & 7) * 8) * 2 : OOB);
    }
    if (w == 3) gate_step(8);
    if (MODE == 0) waitv<4>(); else waitv<0>();
    WAITL0(); bar();
    if (MODE == 0) {
        const int d = c.cb * 4 + w;
        const unsigned sl = c.ldsb + CV_BASE + w * CV_SLOT, vo = lane * 16;
        rsrc_t rf = mkrsrc(p.ssk, D * S * 2), r0 = mkrsrc(p.ws + WS_KR, D * KRS * 2);
#pragma unroll
        for (int i = 0; i < 4; ++i) dma16(sl + 512 + i * 1024, vo, rf, d * (S * 2) + i * 1024);
        if (X.kr_ready) {
#pragma unroll
            for (int i = 0; i < 4; ++i) dma16_sc1(sl + CV_KF + i * 1024, vo, r0, d * (KRS * 2) + i * 1024);
        }
        LAS char* slot = lds + CV_BASE + w * CV_SLOT;
        if (lane < 32) *(LAS u32x4*)(slot + lane * 16) = (u32x4){0, 0, 0, 0};
        else if (lane < 40) *(LAS u32x4*)(slot + CV_KF + ((lane - 32) >> 2) * CV_COPY + 4096 + (lane & 3) * 16) = (u32x4){0, 0, 0, 0};
    }
    if (w == 3) gate_step(9);
    WAITL0(); bar();
    waitv<0>();
    gacc = dpp_add<0x111>(gacc); gacc = dpp_add<0x112>(gacc); gacc = dpp_add<0x114>(gacc);
    if (w == 3) {
        rsrc_t rG = mkrsrc(p.ws + WS_G, S * 4);
        __builtin_amdgcn_raw_buffer_store_b32(__float_as_uint(sigmoidf(gacc)), rG, (lane & 7) == 7 ? (c.bs * 128 + grw) * 4 : OOB, 0, 16);
    }
    if (MODE == 0) {
#pragma unroll
        for (int i = 0; i < 4; ++i) TIE(X.wd[i]);
    }
}

__global__ __launch_bounds__(512, 2) void k_fused(Params p) {
    extern __shared__ __attribute__((aligned(16))) char smem[];
    LAS char* lds = (LAS char*)smem;
    const unsigned ldsb = (unsigned)(size_t)lds;
    const int wave = rfl(threadIdx.x >> 6), lane = threadIdx.x & 63;
    const int b = blockIdx.x, xcd = b & 7, idx = b >> 3;
    const bool has_conv = idx < 20;
    const int cb = xcd * 20 + idx, pb = xcd * 12 + idx - 20;
    const bool kvblock = !has_conv && pb < 80;
    const int bs = (xcd & 1) * 8 + (idx & 7), bf = (xcd >> 1) * 4 + (idx >> 3);
    const int st = xcd * 4 + (idx & 3), ds = idx >> 2;
    const bool hi4 = ((st >> 1) & 7) >= 4;
    const int ne = hi4 ? 6 : 8;
    unsigned long long order;
    {
        const unsigned E = hi4 ? 0x876210u : 0x98763210u, L = hi4 ? 0xBA9543u : 0xBA54u;
        const int nl = 12 - ne, re = ds % ne, rl = ds % nl;
        const unsigned long long Er = (((unsigned long long)E << (4 * ne) | E) >> (4 * re)) & ((1ull << (4 * ne)) - 1);
        const unsigned long long Lr = (((unsigned long long)L << (4 * nl) | L) >> (4 * rl)) & ((1ull << (4 * nl)) - 1);
        order = Er | (Lr << (4 * ne));
    }
    STAMPT(0);
    Ctx c; c.p = p; c.lds = lds; c.ldsb = ldsb; c.lane = lane; c.wave = wave; c.xcd = xcd; c.cb = cb; c.pb = pb; c.bs = bs; c.bf = bf; c.tag = 0;

    if (wave >= 4) {
        const int w = wave - 4;
        Tail X;
        X.kr_ready = false;
#pragma unroll
        for (int i = 0; i < 4; ++i) X.wd[i] = (u32x4){0, 0, 0, 0};
        if (has_conv) loader_gemm1<0>(c, X);
        else if (kvblock) loader_gemm1<1>(c, X);
        else loader_gemm1<2>(c, X);
        const unsigned tag = c.tag;
        STAMPT(4);
        const unsigned vo = lane * 16;
        rsrc_t rw = mkrsrc(p.ws + WS_WT, F * D * 2);
        if (has_conv) {
            LAS char* c0 = lds + CV_BASE + w * CV_SLOT + CV_KF;
            if (!X.kr_ready) {
                poll4<4>(p.ws, FL_KR + (cb >> 5) * 64, tag, lane);
                rsrc_t r0 = mkrsrc(p.ws + WS_KR, D * KRS * 2);
#pragma unroll
                for (int i = 0; i < 4; ++i) dma16_sc1(ldsb + CV_BASE + w * CV_SLOT + CV_KF + i * 1024, vo, r0, (cb * 4 + w) * (KRS * 2) + i * 1024);
                waitv<0>();
            }
            STAMPT(5);
            {
                const int unit = cb * 3 + w, Tk = unit / 10, dl = unit % 10, kh = Tk / 24, tt = (Tk % 24) >> 1, kk = Tk & 1;
                const int g4 = lane >> 4, q = (lane & 15) >> 2, pp = lane & 3, row = 4 * g4 + q;
                LAS char* buf = lds + WT_SCR + w * 4096;
#pragma unroll
                for (int i = 0; i < 4; ++i) *(LAS u32x4*)(buf + (8 * i + (lane >> 3)) * 128 + (lane & 7) * 16) = X.wd[i];
#pragma unroll
                for (int i = 0; i < 4; ++i) {
                    const int d16 = 4 * dl + i, dsl = d16 / 5, n = d16 % 5;
                    const s16x8 f = cat(trread(buf + row * 128 + 32 * i + 8 * pp), trread(buf + (row + 16) * 128 + 32 * i + 8 * pp));
                    __builtin_amdgcn_raw_buffer_store_b128(__builtin_bit_cast(u32x4, f), rw, w < 3 ? ((((dsl * 12 + tt) * 4 + kh * 2 + kk) * 5 + n) * 64 + lane) * 16 : OOB, 0, 16);
                }
            }
#pragma unroll
            for (int j = 0; j < 4; ++j) {
                const u32x4 a = *(LAS u32x4*)(c0 + j * 1024 + lane * 16);
                const unsigned nx = *(LAS unsigned*)(c0 + j * 1024 + lane * 16 + 16);
                *(LAS u32x4*)(c0 + CV_COPY + j * 1024 + lane * 16) =
                    (u32x4){__builtin_amdgcn_alignbit(a[1], a[0], 16), __builtin_amdgcn_alignbit(a[2], a[1], 16), __builtin_amdgcn_alignbit(a[3], a[2], 16), __builtin_amdgcn_alignbit(nx, a[3], 16)};
            }
        }
        waitv<0>();
        if (has_conv && lds_last(lds, L_CNT + 4, 4, lane) && lane < 8) st_flag(p.ws, FL_WT + lane * 256 + cb, tag);
        WAITL0();
        bar();
        STAMPT(6);
        rsrc_t rh = mkrsrc(p.ws + WS_H, S * F * 2);
        const unsigned sw2 = ds * (48 * 5 * 1024) + w * 5120, sh2 = ((st * 2 + (w & 1)) * 96 + (w >> 1) * 48) * 1024;
        auto stage_of = [&](int t) { return (int)((order >> (4 * t)) & 15); };
        auto w_dma = [&](int t) {
            const unsigned dst = ldsb + wslot(t & 3) + w * 5120, src = sw2 + stage_of(t) * G2_WST;
#pragma unroll
            for (int j = 0; j < 5; ++j) dma16_sc1(dst + j * 1024, vo, rw, src + j * 1024);
        };
        auto h_dma = [&](int t) {
            const unsigned dst = ldsb + hslot(t & 3) + w * 4096, src = sh2 + stage_of(t) * 4096;
#pragma unroll
            for (int j = 0; j < 4; ++j) dma16_sc1(dst + j * 1024, vo, rh, src + j * 1024);
        };
        const unsigned late_mask = hi4 ? 12u : 8u, hword = FL_H + (st >> 1) * 64;
        if (w == 0) {
            poll4<4>(p.ws, hword, tag, lane, 15u & ~late_mask);
            if (lane == 0) lds_set(lds, L_CNT + 20, 1u);
        } else {
            lds_wait(lds, L_CNT + 20);
        }
        STAMPT(10);
        h_dma(0); h_dma(1); h_dma(2);
        {
            const int ts = stage_of(lane >> 3 < 3 ? lane >> 3 : 0), T = 24 * ((lane >> 2) & 1) + 2 * ts + ((lane >> 1) & 1), dl = (5 * ds + (lane & 1) * 4) >> 2;
            const unsigned fo = (FL_WT + xcd * 256 + (10 * T + dl) / 3) * 4;
            if (w == 0) {
                rsrc_t r = flag_rsrc(p.ws);
                for (int spins = 0; spins < (1 << 18); ++spins) {
                    unsigned x;
                    asm volatile("buffer_load_dword %0, %1, %2, 0 offen sc1\n\ts_waitcnt vmcnt(0)" : "=&v"(x) : "v"(fo), "s"(r) : "memory");
                    if (__all(x == tag)) break;
                    __builtin_amdgcn_s_sleep(4);
                }
                if (lane == 0) lds_set(lds, L_CNT + 8, 1u);
            } else {
                lds_wait(lds, L_CNT + 8);
            }
        }
        STAMPT(9);
        w_dma(0); w_dma(1); w_dma(2);
        const u32x4 hl = aload_sc1(flag_rsrc(p.ws), (hword + (lane & 3) * 4) * 4);
        const u32x4 wl = aload_sc1(flag_rsrc(p.ws), (FL_WT + xcd * 256 + (lane < 40 ? lane : 0) * 4) * 4);
        waitv<0>();
        u32x4 hl_ = hl, wl_ = wl; TIE(hl_); TIE(wl_);
        const bool late_ok = __all(all_tag(hl_, tag));
        const bool wt_ok = __all(all_tag(wl_, tag));
        bar();
        STAMPT(7);
        if (!wt_ok) poll4<40>(p.ws, FL_WT + xcd * 256, tag, lane);
        h_dma(3); w_dma(3);
        bar();
#pragma unroll 1
        for (int t = 1; t <= 8; ++t) {
            waitv<9>(); bar();
            if (t + 3 == ne && !late_ok) poll4<4>(p.ws, hword, tag, lane);
            h_dma(t + 3); w_dma(t + 3);
        }
        rsrc_t ry = mkrsrc(p.ws + WS_YT, D * S * 2);
        const u32x4 yf = aload_sc1(flag_rsrc(p.ws), (FL_Y + ds * 64 + (lane < 5 ? lane : 0) * 4) * 4);
        waitv<10>(); bar();
        waitv<0>();
        u32x4 yf_ = yf; TIE(yf_);
        if (!__all(all_tag(yf_, tag))) poll4<5>(p.ws, FL_Y + ds * 64, tag, lane);
        u32x4 yv[3];
#pragma unroll
        for (int i = 0; i < 3; ++i) {
            const int it = w + 4 * i, ch = 8 * it + (lane >> 3);
            yv[i] = aload_sc1(ry, it < 10 ? ((80 * ds + ch) * S + 64 * st + (lane & 7) * 8) * 2 : OOB);
        }
        {
            rsrc_t rq = mkrsrc(p.q, S * D * 2), rG = mkrsrc(p.ws + WS_G, S * 4);
#pragma unroll
            for (int i = 0; i < 3; ++i) {
                const int ch = (w + 4 * i) * 64 + lane, row = ch / 10;
                dma16(ldsb + G2_Q + (w + 4 * i) * 1024, ch < 640 ? (64 * st + row) * (D * 2) + 160 * ds + (ch - row * 10) * 16 : OOB, rq, 0);
            }
            if (w == 0) asm volatile("s_mov_b32 m0, %0\n\ts_nop 0\n\tbuffer_load_dword %1, %2, 0 offen sc1 lds" ::"s"(ldsb + G2_G), "v"((64 * st + lane) * 4), "s"(rG) : "m0", "memory");
        }
        bar();
        bar();
        waitv<0>();
#pragma unroll
        for (int i = 0; i < 3; ++i) {
            const int it = w + 4 * i, ch = 8 * it + (lane >> 3);
            TIE(yv[i]);
            if (it < 10) *(LAS u32x4*)(lds + G2_Y + ch * 128 + (lane & 7) * 16) = yv[i];
        }
        WAITL0();
        bar();
        if (b == 0 && w == 0) {
            rsrc_t r = flag_rsrc(p.ws);
            for (int spins = 0; spins < (1 << 18); ++spins) {
                u32x4 x;
                asm volatile("buffer_load_dwordx4 %0, %1, %2, 0 offen sc1\n\ts_waitcnt vmcnt(0)" : "=&v"(x) : "v"((FL_H + (lane >> 2) * 64 + (lane & 3) * 4) * 4), "s"(r) : "memory");
                if (__all(all_tag(x, tag))) break;
                __builtin_amdgcn_s_sleep(4);
            }
            if (lane == 0) st_flag(p.ws, FL_EPOCH, tag);
        }
        STAMPT(8);
    } else {
        unsigned tag = 0;
        float Dd = bflo(p.sd[(has_conv ? cb * 4 : 0) + wave]);
        asm volatile("" : "+v"(Dd));
        {
            const int wsx = wave & 1, wf = wave >> 1;
            const int g = lane >> 4, fr = lane & 15, q = (lane & 15) >> 2, pp = lane & 3;
            int oa[3];
#pragma unroll
            for (int i = 0; i < 3; ++i) {
                const int ft = 3 * wf + i, cc = ft >> 1, half = ft & 1;
                oa[i] = 16384 + (g * 3 + cc) * 512 + q * 64 + 16 * ((2 * half + (pp >> 1)) ^ (2 * (g & 1))) + 8 * (pp & 1);
            }
            const int ob0 = (64 * wsx + fr) * 128 + ((g ^ (fr >> 1)) * 16);
            const int ob1 = (64 * wsx + fr) * 128 + ((g ^ (fr >> 1) ^ 4) * 16);
            f32x4 accg[3][4], accu[3][4];
#pragma unroll
            for (int i = 0; i < 3; ++i)
#pragma unroll
                for (int m = 0; m < 4; ++m) { accg[i][m] = (f32x4){0, 0, 0, 0}; accu[i][m] = (f32x4){0, 0, 0, 0}; }
            struct Fr { s16x8 a[6]; s16x8 b[4]; } fa, fb;
            auto rd = [&](Fr& f, int ks, int sb) {
#pragma unroll
                for (int m = 0; m < 4; ++m) f.b[m] = *(LAS s16x8*)(lds + sb + (ks ? ob1 : ob0) + m * 2048);
#pragma unroll
                for (int i = 0; i < 3; ++i) {
                    f.a[i] = cat(trread(lds + sb + oa[i] + ks * 6144), trread(lds + sb + oa[i] + ks * 6144 + 256));
                    f.a[3 + i] = cat(trread(lds + sb + oa[i] + ks * 6144 + 12288), trread(lds + sb + oa[i] + ks * 6144 + 256 + 12288));
                }
            };
            auto mm = [&](const Fr& f) {
#pragma unroll
                for (int i = 0; i < 3; ++i)
#pragma unroll
                    for (int m = 0; m < 4; ++m) {
                        accg[i][m] = mfma16(f.a[i], f.b[m], accg[i][m]);
                        accu[i][m] = mfma16(f.a[3 + i], f.b[m], accu[i][m]);
                    }
            };
            auto interleave = [&]() {
#pragma unroll
                for (int i = 0; i < 16; ++i) { SGB(0x008, 1); SGB(0x100, 1); }
                SGB(0x008, 8);
                SB0();
            };
            const int pi = kvblock ? pb : (has_conv ? cb : 160 + pb - 80), np = kvblock ? 80 : 176, t0 = kvblock ? 6 : 2;
            if (kvblock) { __builtin_amdgcn_s_sleep(SLEEP_K); kv_prepare(c); } else __builtin_amdgcn_s_sleep(SLEEP_C);
            {
                rsrc_t rpf = mkrsrc(wave == 0 ? p.q : (wave == 1 ? p.wg : p.wu), wave == 0 ? S * D * 2 : D * F * 2);
                const int nl = wave == 0 ? 8192 : 6144;
                for (int l0 = pi * 64; l0 < nl && wave < 3; l0 += np * 64) {
                    const int l = l0 + lane;
                    prefetch_lines(ldsb + L_DUMMY, rpf, wave == 0 ? (l >> 2) * 1280 + (t0 + (l & 3)) * 128 : (t0 * 64 + l / 24) * 3072 + (l % 24) * 128);
                }
                if (kvblock && wave == 3) {
                    rsrc_t rwd = mkrsrc(p.wd, F * D * 2), rf = mkrsrc(p.ssk, D * S * 2);
                    for (int l0 = pb * 64; l0 < 15360; l0 += 80 * 64) prefetch_lines(ldsb + L_DUMMY, rwd, (l0 + lane) * 128);
                    for (int l0 = pb * 64; l0 < 20480; l0 += 80 * 64) prefetch_lines(ldsb + L_DUMMY, rf, (l0 + lane) * 128);
                }
            }
            __builtin_amdgcn_s_setprio(1);
            bar();
            STAMPT(3);
            rd(fa, 0, 0);
            int sb = 0;
#pragma unroll 1
            for (int t = 0; t < 9; ++t) {
                rd(fb, 1, sb); mm(fa); interleave();
                WAITL0();
                bar();
                sb = (sb == 2 * G1_STAGE) ? 0 : sb + G1_STAGE;
                rd(fa, 0, sb); mm(fb); interleave();
            }
            rd(fb, 1, sb); mm(fa); interleave();
            WAITL0();
            bar();
            mm(fb);
            STAMPT(4);
            tag = get_tag(c);
            rsrc_t rh = mkrsrc(p.ws + WS_H, S * F * 2);
#pragma unroll
            for (int mp = 0; mp < 2; ++mp)
#pragma unroll
                for (int i = 0; i < 3; ++i) {
                    float hv[8];
#pragma unroll
                    for (int e = 0; e < 8; ++e) hv[e] = accu[i][2 * mp + (e >> 2)][e & 3] * sigmoidf(accg[i][2 * mp + (e >> 2)][e & 3]);
                    const u32x4 o = {cvtpk(hv[0], hv[1]), cvtpk(hv[2], hv[3]), cvtpk(hv[4], hv[5]), cvtpk(hv[6], hv[7])};
                    const int sp = bs * 4 + wsx * 2 + mp, ft = bf * 6 + wf * 3 + i;
                    __builtin_amdgcn_raw_buffer_store_b128(o, rh, ((sp * 96 + ft) * 64 + lane) * 16, 0, 16);
                }
        }
        STAMPT(5);
        auto publish_h = [&]() {
            waitv<0>();
            if (lds_last(lds, L_CNT, 4, lane) && lane == 0) st_flag(p.ws, FL_H + bs * 64 + bf, tag);
        };
        bar();
        STAMPT(6);
        publish_h();
        if (has_conv) {
            const int ca = lane & 15, cg = lane >> 4;
            f32x4 cacc[8];
            u32x2 ckv[8];
#pragma unroll
            for (int n = 0; n < 8; ++n) cacc[n] = (f32x4){0, 0, 0, 0};
            const unsigned slot = ldsb + CV_BASE + wave * CV_SLOT;
            unsigned pg = slot + 480 + 32 * ca + 16 * cg;
            unsigned pf[8];
#pragma unroll
            for (int j = 0; j < 8; ++j) pf[j] = slot + CV_KF + (1 - (ca & 1)) * CV_COPY + 476 + 16 * cg - 4 * (ca >> 1) + 512 * (7 - j);
            struct Res { s16x8 G[8]; s16x4 Fl[8], Fh[8]; } RA, RB;
            auto rd1 = [&](Res& R, int k) {
                const int j = k / 3, m = k % 3;
                if (m == 0) asm volatile("ds_read2_b32 %0, %1 offset0:0 offset1:1" : "=v"(R.Fl[j]) : "v"(pf[j]));
                else if (m == 1) asm volatile("ds_read2_b32 %0, %1 offset0:2 offset1:3" : "=v"(R.Fh[j]) : "v"(pf[j]));
                else asm volatile("ds_read_b128 %0, %1 offset:%2" : "=v"(R.G[j]) : "v"(pg), "n"(512 * j));
            };
            auto land = [&](Res& R) {
                asm volatile("s_waitcnt lgkmcnt(0)"
                             : "+v"(R.G[0]), "+v"(R.G[1]), "+v"(R.G[2]), "+v"(R.G[3]), "+v"(R.G[4]), "+v"(R.G[5]), "+v"(R.G[6]), "+v"(R.G[7]),
                               "+v"(R.Fl[0]), "+v"(R.Fl[1]), "+v"(R.Fl[2]), "+v"(R.Fl[3]), "+v"(R.Fl[4]), "+v"(R.Fl[5]), "+v"(R.Fl[6]), "+v"(R.Fl[7]));
                asm volatile(""
                             : "+v"(R.Fh[0]), "+v"(R.Fh[1]), "+v"(R.Fh[2]), "+v"(R.Fh[3]), "+v"(R.Fh[4]), "+v"(R.Fh[5]), "+v"(R.Fh[6]), "+v"(R.Fh[7]));
                SB0();
            };
            auto next_addr = [&](int j) {
                if (j < 8) asm volatile("v_subrev_u32 %0, 64, %0" : "+v"(pf[j]));
                else asm volatile("v_subrev_u32 %0, 64, %0" : "+v"(pg));
            };
            auto residue = [&](Res& R, Res& N) {
                int k = 0;
#pragma unroll
                for (int I = 0; I < 8; ++I) {
                    const s16x8 Fv = cat(R.Fl[I], R.Fh[I]);
#pragma unroll
                    for (int n = I; n < 8; ++n) {
                        cacc[n] = mfma16(Fv, R.G[n - I], cacc[n]);
                        if (k < 24) rd1(N, k);
                        else if (k < 33) next_addr(k - 24);
                        ++k;
                        SB0();
                    }
                }
                land(N);
            };
#pragma unroll
            for (int k = 0; k < 24; ++k) rd1(RA, k);
#pragma unroll
            for (int j = 0; j < 9; ++j) next_addr(j);
            land(RA);
#pragma unroll 1
            for (int r2 = 0; r2 < 4; ++r2) {
                residue(RA, RB);
                residue(RB, RA);
            }
#pragma unroll
            for (int n = 0; n < 8; ++n) ckv[n] = *(LAS u32x2*)(lds + CV_BASE + wave * CV_SLOT + CV_KF + 2 * (2044 - 256 * n - 16 * ca - 4 * cg));
            const int d = cb * 4 + wave;
            rsrc_t ry = mkrsrc(p.ws + WS_YT, D * S * 2);
            u32x2 o[8];
#pragma unroll
            for (int n = 0; n < 8; ++n) {
                o[n][0] = cvtpk(cacc[n][0] + bfhi(ckv[n][1]) * Dd, cacc[n][1] + bflo(ckv[n][1]) * Dd);
                o[n][1] = cvtpk(cacc[n][2] + bfhi(ckv[n][0]) * Dd, cacc[n][3] + bflo(ckv[n][0]) * Dd);
            }
#pragma unroll
            for (int n = 0; n < 8; n += 2) {
                auto x = __builtin_amdgcn_permlane16_swap(o[n][0], o[n + 1][0], false, false);
                auto y = __builtin_amdgcn_permlane16_swap(o[n][1], o[n + 1][1], false, false);
                const u32x4 val = {x[0], y[0], x[1], y[1]};
                const int s = 256 * (n + (cg & 1)) + 16 * ca + 4 * (cg & 2);
                __builtin_amdgcn_raw_buffer_store_b128(val, ry, (d * S + s) * 2, 0, 16);
            }
        }
        STAMPT(7);
        {
            const int sp = wave & 1, kh = wave >> 1;
            const int s16 = lane & 15, g = lane >> 4, q = s16 >> 2, pp = lane & 3;
            const int srow = 64 * st + 32 * sp + 16 * kh + s16;
            f32x4 acc[2][5];
#pragma unroll
            for (int n = 0; n < 5; ++n) { acc[0][n] = (f32x4){0, 0, 0, 0}; acc[1][n] = (f32x4){0, 0, 0, 0}; }
            const unsigned pw = ldsb + kh * 10240 + lane * 16, ph = ldsb + wave * 4096 + lane * 16;
            struct Fr2 { s16x8 w[5]; u32x4 h[2]; } fa, fb;
            auto rd2 = [&](Fr2& f, int s4, int kk) {
                LAS char* bh = (LAS char*)(size_t)(ph + hslot(s4) + kk * 2048);
                LAS char* bw = (LAS char*)(size_t)(pw + wslot(s4) + kk * 5120);
                f.h[0] = *(LAS u32x4*)bh; f.h[1] = *(LAS u32x4*)(bh + 1024);
#pragma unroll
                for (int n = 0; n < 5; ++n) f.w[n] = *(LAS s16x8*)(bw + n * 1024);
            };
            auto swp = [&](Fr2& f) {
                unsigned a2 = f.h[0][2], a3 = f.h[0][3], b0 = f.h[1][0], b1 = f.h[1][1];
                asm volatile("v_swap_b32 %0, %1\n\tv_swap_b32 %2, %3\n\ts_nop 1" : "+v"(a2), "+v"(b0), "+v"(a3), "+v"(b1));
                f.h[0][2] = a2; f.h[0][3] = a3; f.h[1][0] = b0; f.h[1][1] = b1;
            };
            auto step = [&](const Fr2& f) {
                const s16x8 h0 = __builtin_bit_cast(s16x8, f.h[0]), h1 = __builtin_bit_cast(s16x8, f.h[1]);
#pragma unroll
                for (int n = 0; n < 5; ++n) { acc[0][n] = mfma16(f.w[n], h0, acc[0][n]); acc[1][n] = mfma16(f.w[n], h1, acc[1][n]); }
            };
            auto il2 = [&]() {
#pragma unroll
                for (int i = 0; i < 7; ++i) { SGB(0x008, 1); SGB(0x100, 1); }
                SGB(0x008, 3);
                SB0();
            };
            WAITL0();
            bar();
            STAMPT(8);
            rd2(fa, 0, 0);
            swp(fa);
#pragma unroll 1
            for (int t = 0; t < 12; ++t) {
                rd2(fb, t & 3, 1);
                step(fa);
                il2();
                swp(fb);
                SB0();
                if (t == 3) waitv<0>();
                bar();
                if (t == 3 && has_conv && wave == 0 && lane == 0) st_flag(p.ws, FL_Y + (cb / 20) * 64 + cb % 20, tag);
                rd2(fa, (t + 1) & 3, 0);
                step(fb);
                il2();
                swp(fa);
                SB0();
            }
            STAMPT(10);
#pragma unroll
            for (int n = 0; n < 5; ++n) *(LAS f32x4*)(lds + G2_RED + (wave * 5 + n) * 1024 + lane * 16) = kh ? acc[0][n] : acc[1][n];
            WAITL0();
            bar();
            STAMPT(11);
            const int lrow = 32 * sp + 16 * kh + s16;
            const float gt = *(LAS float*)(lds + G2_G + lrow * 4);
#pragma unroll
            for (int n = 0; n < 5; ++n) {
                const u32x2 qn = *(LAS u32x2*)(lds + G2_Q + lrow * 160 + (16 * n + 4 * g) * 2);
                const f32x4 o4 = (kh ? acc[1][n] : acc[0][n]) + *(LAS f32x4*)(lds + G2_RED + ((wave ^ 2) * 5 + n) * 1024 + lane * 16);
                const u32x2 yv = __builtin_bit_cast(u32x2, trread(lds + G2_Y + (16 * n + 4 * g + q) * 128 + (32 * sp + 16 * kh + 4 * pp) * 2));
                const float o0 = gt * (bflo(qn[0]) * bflo(yv[0])) + (1.f - gt) * o4[0];
                const float o1 = gt * (bfhi(qn[0]) * bfhi(yv[0])) + (1.f - gt) * o4[1];
                const float o2 = gt * (bflo(qn[1]) * bflo(yv[1])) + (1.f - gt) * o4[2];
                const float o3 = gt * (bfhi(qn[1]) * bfhi(yv[1])) + (1.f - gt) * o4[3];
                *(u32x2*)(p.out + (size_t)srow * D + 80 * ds + 16 * n + 4 * g) = (u32x2){cvtpk(o0, o1), cvtpk(o2, o3)};
            }
            STAMPT(12);
        }
    }
}

extern "C" void kernel_launch(void* const* d_in, const int* in_sizes, int n_in,
                              void* d_out, int out_size, void* d_ws, size_t ws_size,
                              hipStream_t stream) {
    Params p;
    p.q = (const u16*)d_in[0]; p.k = (const u16*)d_in[1]; p.v = (const u16*)d_in[2];
    p.sk = (const u16*)d_in[3]; p.ssk = (const u16*)d_in[4]; p.sd = (const u16*)d_in[5];
    p.wg = (const u16*)d_in[7]; p.wu = (const u16*)d_in[8]; p.wd = (const u16*)d_in[9]; p.gw = (const u16*)d_in[10];
    p.out = (u16*)d_out; p.ws = (char*)d_ws;
    hipFuncSetAttribute((const void*)k_fused, hipFuncAttributeMaxDynamicSharedMemorySize, LDS_BYTES);
    k_fused<<<256, 512, LDS_BYTES, stream>>>(p);
}
```

```cpp
#include <hip/hip_runtime.h>
#include <hip/hip_bf16.h>
#include <stdint.h>

typedef unsigned short u16;
typedef short s16x4 __attribute__((ext_vector_type(4)));
typedef short s16x8 __attribute__((ext_vector_type(8)));
typedef __bf16 b16x8 __attribute__((ext_vector_type(8)));
typedef __bf16 b16x2 __attribute__((ext_vector_type(2)));
typedef float f32x2 __attribute__((ext_vector_type(2)));
typedef float f32x4 __attribute__((ext_vector_type(4)));
typedef unsigned u32x2 __attribute__((ext_vector_type(2)));
typedef unsigned u32x4 __attribute__((ext_vector_type(4)));
#define LAS __attribute__((address_space(3)))
#define GAS __attribute__((address_space(1)))
#define DI __device__ __forceinline__

static constexpr int S = 2048, D = 640, F = 1536;
static constexpr int KRS = 2112;
static constexpr unsigned WS_H = 0, WS_KR = 8u << 20, WS_YT = 14u << 20, WS_G = 17u << 20, WS_WT = 18u << 20, WS_STAMP = 20u << 20, WS_FLAG = 24u << 20;
static constexpr unsigned FL_EPOCH = 0, FL_KR = 1024, FL_WT = 2048  , FL_H = 4096, FL_Y = 6144;
static constexpr unsigned OOB = 0xF0000000u;

struct Params {
    const u16 *q, *k, *v, *sk, *ssk, *sd, *wg, *wu, *wd, *gw;
    u16* out; char* ws;
};

DI float bflo(unsigned x) { return __uint_as_float(x << 16); }
DI float bfhi(unsigned x) { return __uint_as_float(x & 0xffff0000u); }
DI unsigned cvtpk(float lo, float hi) { f32x2 v = {lo, hi}; b16x2 b = __builtin_convertvector(v, b16x2); return __builtin_bit_cast(unsigned, b); }
DI unsigned rfl(unsigned x) { return __builtin_amdgcn_readfirstlane(x); }
DI float sigmoidf(float x) { return __builtin_amdgcn_rcpf(1.f + __builtin_amdgcn_exp2f(x * -1.44269504089f)); }
DI f32x4 mfma16(s16x8 a, s16x8 b, f32x4 c) {
    return __builtin_amdgcn_mfma_f32_16x16x32_bf16(__builtin_bit_cast(b16x8, a), __builtin_bit_cast(b16x8, b), c, 0, 0, 0);
}
DI s16x4 trread(LAS char* p) { return __builtin_amdgcn_ds_read_tr16_b64_v4i16((LAS s16x4*)p); }
DI s16x8 cat(s16x4 a, s16x4 b) { return __builtin_shufflevector(a, b, 0, 1, 2, 3, 4, 5, 6, 7); }
typedef __amdgpu_buffer_rsrc_t rsrc_t;
DI rsrc_t mkrsrc(const void* p, unsigned bytes) { return __builtin_amdgcn_make_buffer_rsrc((void*)p, 0, bytes, 0x00020000); }
DI void dma16(unsigned m0v, unsigned voff, rsrc_t r, unsigned soff) {
    asm volatile("s_mov_b32 m0, %0\n\ts_nop 0\n\tbuffer_load_dwordx4 %1, %2, %3 offen lds" ::"s"(m0v), "v"(voff), "s"(r), "s"(soff) : "m0", "memory");
}
DI void dma16_sc1(unsigned m0v, unsigned voff, rsrc_t r, unsigned soff) {
    asm volatile("s_mov_b32 m0, %0\n\ts_nop 0\n\tbuffer_load_dwordx4 %1, %2, %3 offen sc1 lds" ::"s"(m0v), "v"(voff), "s"(r), "s"(soff) : "m0", "memory");
}
DI u32x4 aload(rsrc_t r, unsigned voff) { u32x4 x; asm volatile("buffer_load_dwordx4 %0, %1, %2, 0 offen" : "=v"(x) : "v"(voff), "s"(r) : "memory"); return x; }
DI u32x4 aload_sc1(rsrc_t r, unsigned voff) { u32x4 x; asm volatile("buffer_load_dwordx4 %0, %1, %2, 0 offen sc1" : "=v"(x) : "v"(voff), "s"(r) : "memory"); return x; }
DI void prefetch_lines(unsigned scratch, rsrc_t r, unsigned voff) {
    asm volatile("s_mov_b32 m0, %0\n\ts_nop 0\n\tbuffer_load_dword %1, %2, 0 offen lds" ::"s"(scratch), "v"(voff), "s"(r) : "m0", "memory");
}
template <int N> DI void waitv() { asm volatile("s_waitcnt vmcnt(%0)" ::"n"(N) : "memory"); }
DI float dot2(unsigned a, unsigned b, float c) { return __builtin_amdgcn_fdot2_f32_bf16(__builtin_bit_cast(b16x2, a), __builtin_bit_cast(b16x2, b), c, false); }
template <int CTRL> DI float dpp_add(float x) { return x + __int_as_float(__builtin_amdgcn_update_dpp(0, __float_as_int(x), CTRL, 0xf, 0xf, true)); }
#define TIE(x) asm volatile("" : "+v"(x))
#define WAITL0() asm volatile("s_waitcnt lgkmcnt(0)" ::: "memory")
DI void bar() { asm volatile("" ::: "memory"); __builtin_amdgcn_s_barrier(); asm volatile("" ::: "memory"); }
#define SGB(m, n) __builtin_amdgcn_sched_group_barrier(m, n, 0)
#define SB0() __builtin_amdgcn_sched_barrier(0)
#define RLX_AGENT __ATOMIC_RELAXED, __HIP_MEMORY_SCOPE_AGENT

#define SLEEP_L 0
#define SLEEP_LK 12
#define SLEEP_K 0
#define SLEEP_C 12

DI void st_flag(char* ws, unsigned word, unsigned tag) { __hip_atomic_store((GAS unsigned*)(ws + WS_FLAG) + word, tag, RLX_AGENT); }
DI rsrc_t flag_rsrc(char* ws) { return mkrsrc(ws + WS_FLAG, 32768); }
DI bool all_tag(u32x4 x, unsigned tag) { return x[0] == tag && x[1] == tag && x[2] == tag && x[3] == tag; }
template <int NL>
DI void poll4(char* ws, unsigned word, unsigned tag, int lane, unsigned care = 15u) {
    rsrc_t r = flag_rsrc(ws);
    for (int spins = 0; spins < (1 << 18); ++spins) {
        u32x4 x;
        asm volatile("buffer_load_dwordx4 %0, %1, %2, 0 offen sc1\n\ts_waitcnt vmcnt(0)" : "=&v"(x) : "v"((word + (lane < NL ? lane : 0) * 4) * 4), "s"(r) : "memory");
        const bool ok = (x[0] == tag || !(care & 1)) && (x[1] == tag || !(care & 2)) && (x[2] == tag || !(care & 4)) && (x[3] == tag || !(care & 8));
        if (__all(ok)) return;
        __builtin_amdgcn_s_sleep(4);
    }
}
DI void lds_set(LAS char* lds, int off, unsigned v) { __hip_atomic_store((LAS unsigned*)(lds + off), v, __ATOMIC_RELAXED, __HIP_MEMORY_SCOPE_WORKGROUP); }
DI unsigned lds_get(LAS char* lds, int off) { return rfl(__hip_atomic_load((LAS unsigned*)(lds + off), __ATOMIC_RELAXED, __HIP_MEMORY_SCOPE_WORKGROUP)); }
DI unsigned lds_wait(LAS char* lds, int off) {
    unsigned v = 0;
    for (int spins = 0; spins < (1 << 22); ++spins) {
        v = lds_get(lds, off);
        if (v) break;
        __builtin_amdgcn_s_sleep(8);
    }
    asm volatile("" ::: "memory");
    return v;
}
DI bool lds_last(LAS char* lds, int off, unsigned n, int lane) {
    unsigned old = 0;
    if (lane == 0) old = __hip_atomic_fetch_add((LAS unsigned*)(lds + off), 1u, __ATOMIC_RELAXED, __HIP_MEMORY_SCOPE_WORKGROUP);
    return rfl(old) == n - 1;
}

#define STAMPT(i) do {} while (0)

constexpr int G1_STAGE = 40960;
constexpr int P0_LDS = 122880;
constexpr int TP = 272;
constexpr int P0_T = P0_LDS, P0_GW = P0_LDS + 128 * TP;
constexpr int CV_BASE = G1_STAGE;
constexpr int CV_COPY = 4160, CV_KF = 4608, CV_SLOT = CV_KF + 2 * CV_COPY;
constexpr int G2_WST = 20480, G2_HST = 16384;
constexpr int G2_W0 = 0, G2_W2 = CV_BASE + 4 * CV_SLOT, G2_W3 = CV_BASE;
constexpr int G2_H0 = G2_W2 + G2_WST, G2_H3 = G2_W3 + G2_WST;
constexpr int G2_Y = G2_H3 + G2_HST;
constexpr int G2_Q = G2_H0, G2_G = G2_H0 + 12288;
constexpr int WT_SCR = G2_H0;
constexpr int G2_RED = 0;
constexpr unsigned long long G2_WTAB = (unsigned long long)(G2_W0 >> 8) | (unsigned long long)((G2_W0 + G2_WST) >> 8) << 16 | (unsigned long long)(G2_W2 >> 8) << 32 | (unsigned long long)(G2_W3 >> 8) << 48;
constexpr unsigned long long G2_HTAB = (unsigned long long)(G2_H0 >> 8) | (unsigned long long)((G2_H0 + G2_HST) >> 8) << 16 | (unsigned long long)((G2_H0 + 2 * G2_HST) >> 8) << 32 | (unsigned long long)(G2_H3 >> 8) << 48;
DI int wslot(int s4) { return (int)((G2_WTAB >> (16 * s4)) & 0xFFFF) << 8; }
DI int hslot(int s4) { return (int)((G2_HTAB >> (16 * s4)) & 0xFFFF) << 8; }
constexpr int L_DUMMY = 163328;
constexpr int L_CNT = 163776;
constexpr int L_EPOCH = 163824;
constexpr int LDS_BYTES = 163840;

struct Ctx {
    Params p; LAS char* lds; unsigned ldsb; int lane, wave, xcd, cb, pb, bs, bf; unsigned tag;
};
DI void load_epoch(const Ctx& c) {
    rsrc_t r = mkrsrc(c.p.ws + WS_FLAG, 64);
    if (c.lane == 0) asm volatile("s_mov_b32 m0, %0\n\ts_nop 0\n\tbuffer_load_dword %1, %2, 0 offen sc1 lds" ::"s"(c.ldsb + L_EPOCH), "v"(0u), "s"(r) : "m0", "memory");
}
DI unsigned get_tag(const Ctx& c) { return lds_get(c.lds, L_EPOCH) + 1u; }

DI void kv_prepare(const Ctx& c) {
    const Params& p = c.p;
    LAS char* lds = c.lds;
    const int lane = c.lane, wave = c.wave;
    (void)wave;
    const int kdc = c.pb % 5, s0 = (c.pb / 5) * 128, d0 = kdc * 128, tid = c.wave * 64 + lane, c16 = tid & 15, rg = tid >> 4, s = s0 + 8 * rg;
    u32x4 kr[11], vr[8], w4[4];
    {
        rsrc_t rk = mkrsrc(p.k, S * D * 2), rv = mkrsrc(p.v, S * D * 2), rsk = mkrsrc(p.sk, D * 8);
#pragma unroll
        for (int i = 0; i < 11; ++i) kr[i] = aload(rk, (unsigned)(((s - 3 + i) * D + d0 + 8 * c16) * 2));
#pragma unroll
        for (int i = 0; i < 8; ++i) vr[i] = aload(rv, ((s + i) * D + d0 + 8 * c16) * 2);
#pragma unroll
        for (int i = 0; i < 4; ++i) w4[i] = aload(rsk, (d0 + 8 * c16) * 8 + i * 16);
    }
    waitv<0>();
    STAMPT(2);
#pragma unroll
    for (int i = 0; i < 11; ++i) TIE(kr[i]);
#pragma unroll
    for (int i = 0; i < 8; ++i) TIE(vr[i]);
#pragma unroll
    for (int i = 0; i < 4; ++i) TIE(w4[i]);
    LAS char* tw = lds + P0_T + 8 * c16 * TP + (((15 - rg) ^ c16) * 16);
#pragma unroll
    for (int e = 0; e < 8; ++e) {
        const unsigned sel = (e & 1) ? 0x07060302u : 0x05040100u;
        const unsigned wa = w4[e >> 1][(e & 1) * 2], wb = w4[e >> 1][(e & 1) * 2 + 1];
        unsigned pr[10];
#pragma unroll
        for (int i = 0; i < 10; ++i) pr[i] = __builtin_amdgcn_perm(kr[i][e >> 1], kr[i + 1][e >> 1], sel);
        float o[8];
#pragma unroll
        for (int r = 0; r < 8; ++r) o[r] = dot2(pr[r + 2], wa, dot2(pr[r], wb, 0.f)) * ((e & 1) ? bfhi(vr[r][e >> 1]) : bflo(vr[r][e >> 1]));
        *(LAS u32x4*)(tw + e * TP) = (u32x4){cvtpk(o[7], o[6]), cvtpk(o[5], o[4]), cvtpk(o[3], o[2]), cvtpk(o[1], o[0])};
    }
    STAMPT(13);
    WAITL0(); bar();
    rsrc_t r0 = mkrsrc(p.ws + WS_KR, D * KRS * 2);
#pragma unroll
    for (int it = 0; it < 8; ++it) {
        const int item = tid + it * 256, ch = item >> 4, j = item & 15;
        __builtin_amdgcn_raw_buffer_store_b128(*(LAS u32x4*)(lds + P0_T + ch * TP + ((j ^ ((ch >> 3) & 15)) * 16)), r0, ((d0 + ch) * KRS + (1920 - s0) + 8 * j) * 2, 0, 16);
    }
    STAMPT(14);
    waitv<0>();
    if (lds_last(lds, L_CNT + 16, 4, lane) && lane == 0) st_flag(p.ws, FL_KR + kdc * 64 + c.pb / 5, get_tag(c));
    STAMPT(15);
}

struct Tail { u32x4 wd[4]; bool kr_ready; };
template <int MODE>
DI void loader_gemm1(Ctx& c, Tail& X) {
    const Params& p = c.p;
    const int lane = c.lane, w = c.wave - 4, wave = c.wave;
    (void)wave;
    LAS char* lds = c.lds;
    rsrc_t rq = mkrsrc(p.q, S * D * 2), rg_ = mkrsrc(p.wg, D * F * 2), ru_ = mkrsrc(p.wu, D * F * 2);
    unsigned vx[2], vw[3];
    {
        const int r = lane >> 3, slot = lane & 7;
        vx[0] = r * 1280 + ((slot ^ (r >> 1)) * 16);
        vx[1] = r * 1280 + ((slot ^ (r >> 1) ^ 4) * 16);
#pragma unroll
        for (int j = 0; j < 3; ++j) {
            const int L = 2 * j + (lane >> 5), kbl = L / 3, cc = L % 3, row = (lane & 31) >> 2, ch = (lane & 3) ^ (2 * kbl);
            vw[j] = (kbl * 8 + row) * 3072 + cc * 64 + ch * 16;
        }
    }
    const unsigned sx0 = (c.bs * 128 + 32 * w) * 1280, sw0 = 16 * w * 3072 + c.bf * 192;
    auto issue = [&](int t) {
        const unsigned base = c.ldsb + (int)((0x24924924u >> (2 * t)) & 3) * G1_STAGE;
#pragma unroll
        for (int i = 0; i < 4; ++i) dma16(base + (32 * w + 8 * i) * 128, vx[i & 1], rq, sx0 + i * 8 * 1280 + t * 128);
#pragma unroll
        for (int j = 0; j < 3; ++j) dma16(base + 16384 + w * 3072 + j * 1024, vw[j], rg_, sw0 + t * 64 * 3072);
#pragma unroll
        for (int j = 0; j < 3; ++j) dma16(base + 28672 + w * 3072 + j * 1024, vw[j], ru_, sw0 + t * 64 * 3072);
    };
    if (MODE != 1) issue(0);
    if (w == 0) load_epoch(c);
    if (w < 2) {
        rsrc_t rgw = mkrsrc(p.gw, D * 2);
        if (w == 0 || lane < 16) dma16(c.ldsb + P0_GW + w * 1024, (w * 64 + lane) * 16, rgw, 0);
    }
    if (w == 3 && lane < 8) *(LAS unsigned*)(lds + L_CNT + lane * 4) = 0;
    if (MODE == 1) {
        waitv<0>();
        __builtin_amdgcn_s_sleep(SLEEP_K + SLEEP_LK);
        issue(0); issue(1);
        WAITL0(); bar();
        waitv<10>();
    } else {
        waitv<0>();
    }
    WAITL0(); bar();
    c.tag = get_tag(c);
    STAMPT(3);
    if (MODE != 1) issue(1);
    issue(2);
    const int grw = 8 * c.bf + (lane >> 3), gch = (lane & 7) ^ ((grw >> 1) & 7);
    float gacc = 0.f;
    auto gate_step = [&](int t) {
        const int buf = (int)((0x24924924u >> (2 * t)) & 3);
        const u32x4 xv = *(LAS u32x4*)(lds + buf * G1_STAGE + grw * 128 + (lane & 7) * 16);
        const u32x4 gv = *(LAS u32x4*)(lds + P0_GW + t * 128 + gch * 16);
#pragma unroll
        for (int i = 0; i < 4; ++i) gacc = dot2(xv[i], gv[i], gacc);
    };
    if (w == 3) gate_step(0);
#pragma unroll 1
    for (int t = 0; t < 6; ++t) {
        waitv<10>(); WAITL0(); bar();
        issue(t + 3);
        if (w == 3) gate_step(t + 1);
    }
    u32x4 kf4 = {0, 0, 0, 0};
    waitv<10>(); WAITL0(); bar();
    if (MODE == 0) kf4 = aload_sc1(flag_rsrc(p.ws), (FL_KR + (c.cb >> 5) * 64 + (lane & 3) * 4) * 4);
    issue(9);
    if (w == 3) gate_step(7);
    waitv<10>(); WAITL0(); bar();
    if (MODE == 0) {
        TIE(kf4);
        X.kr_ready = __all(all_tag(kf4, c.tag));
        rsrc_t rwd = mkrsrc(p.wd, F * D * 2);
        const int unit = c.cb * 3 + w, T = unit / 10, dl = unit % 10;
#pragma unroll
        for (int i = 0; i < 4; ++i) X.wd[i] = aload(rwd, w < 3 ? ((32 * T + 8 * i + (lane >> 3)) * D + 64 * dl + (lane & 7) * 8) * 2 : OOB);
    }
    if (w == 3) gate_step(8);
    if (MODE == 0) waitv<4>(); else waitv<0>();
    WAITL0(); bar();
    if (MODE == 0) {
        const int d = c.cb * 4 + w;
        const unsigned sl = c.ldsb + CV_BASE + w * CV_SLOT, vo = lane * 16;
        rsrc_t rf = mkrsrc(p.ssk, D * S * 2), r0 = mkrsrc(p.ws + WS_KR, D * KRS * 2);
#pragma unroll
        for (int i = 0; i < 4; ++i) dma16(sl + 512 + i * 1024, vo, rf, d * (S * 2) + i * 1024);
        if (X.kr_ready) {
#pragma unroll
            for (int i = 0; i < 4; ++i) dma16_sc1(sl + CV_KF + i * 1024, vo, r0, d * (KRS * 2) + i * 1024);
        }
        LAS char* slot = lds + CV_BASE + w * CV_SLOT;
        if (lane < 32) *(LAS u32x4*)(slot + lane * 16) = (u32x4){0, 0, 0, 0};
        else if (lane < 40) *(LAS u32x4*)(slot + CV_KF + ((lane - 32) >> 2) * CV_COPY + 4096 + (lane & 3) * 16) = (u32x4){0, 0, 0, 0};
    }
    if (w == 3) gate_step(9);
    WAITL0(); bar();
    waitv<0>();
    gacc = dpp_add<0x111>(gacc); gacc = dpp_add<0x112>(gacc); gacc = dpp_add<0x114>(gacc);
    if (w == 3) {
        rsrc_t rG = mkrsrc(p.ws + WS_G, S * 4);
        __builtin_amdgcn_raw_buffer_store_b32(__float_as_uint(sigmoidf(gacc)), rG, (lane & 7) == 7 ? (c.bs * 128 + grw) * 4 : OOB, 0, 16);
    }
    if (MODE == 0) {
#pragma unroll
        for (int i = 0; i < 4; ++i) TIE(X.wd[i]);
    }
}

__global__ __launch_bounds__(512, 2) void k_fused(Params p) {
    extern __shared__ __attribute__((aligned(16))) char smem[];
    LAS char* lds = (LAS char*)smem;
    const unsigned ldsb = (unsigned)(size_t)lds;
    const int wave = rfl(threadIdx.x >> 6), lane = threadIdx.x & 63;
    const int b = blockIdx.x, xcd = b & 7, idx = b >> 3;
    const bool has_conv = idx < 20;
    const int cb = xcd * 20 + idx, pb = xcd * 12 + idx - 20;
    const bool kvblock = !has_conv && pb < 80;
    const int bs = (xcd & 1) * 8 + (idx & 7), bf = (xcd >> 1) * 4 + (idx >> 3);
    const int st = xcd * 4 + (idx & 3), ds = idx >> 2;
    STAMPT(0);
    Ctx c; c.p = p; c.lds = lds; c.ldsb = ldsb; c.lane = lane; c.wave = wave; c.xcd = xcd; c.cb = cb; c.pb = pb; c.bs = bs; c.bf = bf; c.tag = 0;

    if (__builtin_expect(wave >= 4, 1)) {
        const int w = wave - 4;
        Tail X;
        X.kr_ready = false;
#pragma unroll
        for (int i = 0; i < 4; ++i) X.wd[i] = (u32x4){0, 0, 0, 0};
        if (has_conv) loader_gemm1<0>(c, X);
        else if (kvblock) loader_gemm1<1>(c, X);
        else loader_gemm1<2>(c, X);
        const unsigned tag = c.tag;
        STAMPT(4);
        const unsigned vo = lane * 16;
        rsrc_t rw = mkrsrc(p.ws + WS_WT, F * D * 2);
        if (has_conv) {
            LAS char* c0 = lds + CV_BASE + w * CV_SLOT + CV_KF;
            if (!X.kr_ready) {
                poll4<4>(p.ws, FL_KR + (cb >> 5) * 64, tag, lane);
                rsrc_t r0 = mkrsrc(p.ws + WS_KR, D * KRS * 2);
#pragma unroll
                for (int i = 0; i < 4; ++i) dma16_sc1(ldsb + CV_BASE + w * CV_SLOT + CV_KF + i * 1024, vo, r0, (cb * 4 + w) * (KRS * 2) + i * 1024);
                waitv<0>();
            }
            STAMPT(5);
            {
                const int unit = cb * 3 + w, Tk = unit / 10, dl = unit % 10, kh = Tk / 24, tt = (Tk % 24) >> 1, kk = Tk & 1;
                const int g4 = lane >> 4, q = (lane & 15) >> 2, pp = lane & 3, row = 4 * g4 + q;
                LAS char* buf = lds + WT_SCR + w * 4096;
#pragma unroll
                for (int i = 0; i < 4; ++i) *(LAS u32x4*)(buf + (8 * i + (lane >> 3)) * 128 + (lane & 7) * 16) = X.wd[i];
#pragma unroll
                for (int i = 0; i < 4; ++i) {
                    const int d16 = 4 * dl + i, dsl = d16 / 5, n = d16 % 5;
                    const s16x8 f = cat(trread(buf + row * 128 + 32 * i + 8 * pp), trread(buf + (row + 16) * 128 + 32 * i + 8 * pp));
                    __builtin_amdgcn_raw_buffer_store_b128(__builtin_bit_cast(u32x4, f), rw, w < 3 ? ((((dsl * 12 + tt) * 4 + kh * 2 + kk) * 5 + n) * 64 + lane) * 16 : OOB, 0, 16);
                }
            }
#pragma unroll
            for (int j = 0; j < 4; ++j) {
                const u32x4 a = *(LAS u32x4*)(c0 + j * 1024 + lane * 16);
                const unsigned nx = *(LAS unsigned*)(c0 + j * 1024 + lane * 16 + 16);
                *(LAS u32x4*)(c0 + CV_COPY + j * 1024 + lane * 16) =
                    (u32x4){__builtin_amdgcn_alignbit(a[1], a[0], 16), __builtin_amdgcn_alignbit(a[2], a[1], 16), __builtin_amdgcn_alignbit(a[3], a[2], 16), __builtin_amdgcn_alignbit(nx, a[3], 16)};
            }
        }
        waitv<0>();
        if (has_conv && lds_last(lds, L_CNT + 4, 4, lane) && lane < 8) st_flag(p.ws, FL_WT + lane * 256 + cb, tag);
        WAITL0();
        bar();
        STAMPT(6);
        const bool hi4 = ((st >> 1) & 7) >= 4;
        const int ne = hi4 ? 6 : 8;
        unsigned long long order;
        {
            const unsigned E = hi4 ? 0x876210u : 0x98763210u, L = hi4 ? 0xBA9543u : 0xBA54u;
            const int nl = 12 - ne, re = ds % ne, rl = ds % nl;
            const unsigned long long Er = (((unsigned long long)E << (4 * ne) | E) >> (4 * re)) & ((1ull << (4 * ne)) - 1);
            const unsigned long long Lr = (((unsigned long long)L << (4 * nl) | L) >> (4 * rl)) & ((1ull << (4 * nl)) - 1);
            order = Er | (Lr << (4 * ne));
        }
        rsrc_t rh = mkrsrc(p.ws + WS_H, S * F * 2);
        const unsigned sw2 = ds * (48 * 5 * 1024) + w * 5120, sh2 = ((st * 2 + (w & 1)) * 96 + (w >> 1) * 48) * 1024;
        auto stage_of = [&](int t) { return (int)((order >> (4 * t)) & 15); };
        auto w_dma = [&](int t) {
            const unsigned dst = ldsb + wslot(t & 3) + w * 5120, src = sw2 + stage_of(t) * G2_WST;
#pragma unroll
            for (int j = 0; j < 5; ++j) dma16_sc1(dst + j * 1024, vo, rw, src + j * 1024);
        };
        auto h_dma = [&](int t) {
            const unsigned dst = ldsb + hslot(t & 3) + w * 4096, src = sh2 + stage_of(t) * 4096;
#pragma unroll
            for (int j = 0; j < 4; ++j) dma16_sc1(dst + j * 1024, vo, rh, src + j * 1024);
        };
        const unsigned late_mask = hi4 ? 12u : 8u, hword = FL_H + (st >> 1) * 64;
        if (w == 0) {
            poll4<4>(p.ws, hword, tag, lane, 15u & ~late_mask);
            if (lane == 0) lds_set(lds, L_CNT + 20, 1u);
        } else {
            lds_wait(lds, L_CNT + 20);
        }
        STAMPT(10);
        h_dma(0); h_dma(1); h_dma(2);
        {
            const int ts = stage_of(lane >> 3 < 3 ? lane >> 3 : 0), T = 24 * ((lane >> 2) & 1) + 2 * ts + ((lane >> 1) & 1), dl = (5 * ds + (lane & 1) * 4) >> 2;
            const unsigned fo = (FL_WT + xcd * 256 + (10 * T + dl) / 3) * 4;
            if (w == 0) {
                rsrc_t r = flag_rsrc(p.ws);
                for (int spins = 0; spins < (1 << 18); ++spins) {
                    unsigned x;
                    asm volatile("buffer_load_dword %0, %1, %2, 0 offen sc1\n\ts_waitcnt vmcnt(0)" : "=&v"(x) : "v"(fo), "s"(r) : "memory");
                    if (__all(x == tag)) break;
                    __builtin_amdgcn_s_sleep(4);
                }
                if (lane == 0) lds_set(lds, L_CNT + 8, 1u);
            } else {
                lds_wait(lds, L_CNT + 8);
            }
        }
        STAMPT(9);
        w_dma(0); w_dma(1); w_dma(2);
        const u32x4 hl = aload_sc1(flag_rsrc(p.ws), (hword + (lane & 3) * 4) * 4);
        const u32x4 wl = aload_sc1(flag_rsrc(p.ws), (FL_WT + xcd * 256 + (lane < 40 ? lane : 0) * 4) * 4);
        waitv<0>();
        u32x4 hl_ = hl, wl_ = wl; TIE(hl_); TIE(wl_);
        const bool late_ok = __all(all_tag(hl_, tag));
        const bool wt_ok = __all(all_tag(wl_, tag));
        bar();
        STAMPT(7);
        if (!wt_ok) poll4<40>(p.ws, FL_WT + xcd * 256, tag, lane);
        h_dma(3); w_dma(3);
        bar();
#pragma unroll 1
        for (int t = 1; t <= 8; ++t) {
            waitv<9>(); bar();
            if (t + 3 == ne && !late_ok) poll4<4>(p.ws, hword, tag, lane);
            h_dma(t + 3); w_dma(t + 3);
        }
        rsrc_t ry = mkrsrc(p.ws + WS_YT, D * S * 2);
        const u32x4 yf = aload_sc1(flag_rsrc(p.ws), (FL_Y + ds * 64 + (lane < 5 ? lane : 0) * 4) * 4);
        waitv<10>(); bar();
        waitv<0>();
        u32x4 yf_ = yf; TIE(yf_);
        if (!__all(all_tag(yf_, tag))) poll4<5>(p.ws, FL_Y + ds * 64, tag, lane);
        u32x4 yv[3];
#pragma unroll
        for (int i = 0; i < 3; ++i) {
            const int it = w + 4 * i, ch = 8 * it + (lane >> 3);
            yv[i] = aload_sc1(ry, it < 10 ? ((80 * ds + ch) * S + 64 * st + (lane & 7) * 8) * 2 : OOB);
        }
        {
            rsrc_t rq = mkrsrc(p.q, S * D * 2), rG = mkrsrc(p.ws + WS_G, S * 4);
#pragma unroll
            for (int i = 0; i < 3; ++i) {
                const int ch = (w + 4 * i) * 64 + lane, row = ch / 10;
                dma16(ldsb + G2_Q + (w + 4 * i) * 1024, ch < 640 ? (64 * st + row) * (D * 2) + 160 * ds + (ch - row * 10) * 16 : OOB, rq, 0);
            }
            if (w == 0) asm volatile("s_mov_b32 m0, %0\n\ts_nop 0\n\tbuffer_load_dword %1, %2, 0 offen sc1 lds" ::"s"(ldsb + G2_G), "v"((64 * st + lane) * 4), "s"(rG) : "m0", "memory");
        }
        bar();
        bar();
        waitv<0>();
#pragma unroll
        for (int i = 0; i < 3; ++i) {
            const int it = w + 4 * i, ch = 8 * it + (lane >> 3);
            TIE(yv[i]);
            if (it < 10) *(LAS u32x4*)(lds + G2_Y + ch * 128 + (lane & 7) * 16) = yv[i];
        }
        WAITL0();
        bar();
        if (b == 0 && w == 0) {
            rsrc_t r = flag_rsrc(p.ws);
            for (int spins = 0; spins < (1 << 18); ++spins) {
                u32x4 x;
                asm volatile("buffer_load_dwordx4 %0, %1, %2, 0 offen sc1\n\ts_waitcnt vmcnt(0)" : "=&v"(x) : "v"((FL_H + (lane >> 2) * 64 + (lane & 3) * 4) * 4), "s"(r) : "memory");
                if (__all(all_tag(x, tag))) break;
                __builtin_amdgcn_s_sleep(4);
            }
            if (lane == 0) st_flag(p.ws, FL_EPOCH, tag);
        }
        STAMPT(8);
    } else {
        unsigned tag = 0;
        {
            const int wsx = wave & 1, wf = wave >> 1;
            const int g = lane >> 4, fr = lane & 15, q = (lane & 15) >> 2, pp = lane & 3;
            int oa[3];
#pragma unroll
            for (int i = 0; i < 3; ++i) {
                const int ft = 3 * wf + i, cc = ft >> 1, half = ft & 1;
                oa[i] = 16384 + (g * 3 + cc) * 512 + q * 64 + 16 * ((2 * half + (pp >> 1)) ^ (2 * (g & 1))) + 8 * (pp & 1);
            }
            const int ob0 = (64 * wsx + fr) * 128 + ((g ^ (fr >> 1)) * 16);
            const int ob1 = (64 * wsx + fr) * 128 + ((g ^ (fr >> 1) ^ 4) * 16);
            f32x4 accg[3][4], accu[3][4];
#pragma unroll
            for (int i = 0; i < 3; ++i)
#pragma unroll
                for (int m = 0; m < 4; ++m) { accg[i][m] = (f32x4){0, 0, 0, 0}; accu[i][m] = (f32x4){0, 0, 0, 0}; }
            struct Fr { s16x8 a[6]; s16x8 b[4]; } fa, fb;
            auto rd = [&](Fr& f, int ks, int sb) {
#pragma unroll
                for (int m = 0; m < 4; ++m) f.b[m] = *(LAS s16x8*)(lds + sb + (ks ? ob1 : ob0) + m * 2048);
#pragma unroll
                for (int i = 0; i < 3; ++i) {
                    f.a[i] = cat(trread(lds + sb + oa[i] + ks * 6144), trread(lds + sb + oa[i] + ks * 6144 + 256));
                    f.a[3 + i] = cat(trread(lds + sb + oa[i] + ks * 6144 + 12288), trread(lds + sb + oa[i] + ks * 6144 + 256 + 12288));
                }
            };
            auto mm = [&](const Fr& f) {
#pragma unroll
                for (int i = 0; i < 3; ++i)
#pragma unroll
                    for (int m = 0; m < 4; ++m) {
                        accg[i][m] = mfma16(f.a[i], f.b[m], accg[i][m]);
                        accu[i][m] = mfma16(f.a[3 + i], f.b[m], accu[i][m]);
                    }
            };
            auto interleave = [&]() {
#pragma unroll
                for (int i = 0; i < 16; ++i) { SGB(0x008, 1); SGB(0x100, 1); }
                SGB(0x008, 8);
                SB0();
            };
            const int pi = kvblock ? pb : (has_conv ? cb : 160 + pb - 80), np = kvblock ? 80 : 176, t0 = kvblock ? 6 : 2;
            if (kvblock) { __builtin_amdgcn_s_sleep(SLEEP_K); kv_prepare(c); } else __builtin_amdgcn_s_sleep(SLEEP_C);
            {
                rsrc_t rpf = mkrsrc(wave == 0 ? p.q : (wave == 1 ? p.wg : p.wu), wave == 0 ? S * D * 2 : D * F * 2);
                const int nl = wave == 0 ? 8192 : 6144;
                for (int l0 = pi * 64; l0 < nl && wave < 3; l0 += np * 64) {
                    const int l = l0 + lane;
                    prefetch_lines(ldsb + L_DUMMY, rpf, wave == 0 ? (l >> 2) * 1280 + (t0 + (l & 3)) * 128 : (t0 * 64 + l / 24) * 3072 + (l % 24) * 128);
                }
                if (kvblock && wave == 3) {
                    rsrc_t rwd = mkrsrc(p.wd, F * D * 2), rf = mkrsrc(p.ssk, D * S * 2);
                    for (int l0 = pb * 64; l0 < 15360; l0 += 80 * 64) prefetch_lines(ldsb + L_DUMMY, rwd, (l0 + lane) * 128);
                    for (int l0 = pb * 64; l0 < 20480; l0 += 80 * 64) prefetch_lines(ldsb + L_DUMMY, rf, (l0 + lane) * 128);
                }
            }
            __builtin_amdgcn_s_setprio(1);
            bar();
            STAMPT(3);
            rd(fa, 0, 0);
            int sb = 0;
#pragma unroll 1
            for (int t = 0; t < 9; ++t) {
                rd(fb, 1, sb); mm(fa); interleave();
                WAITL0();
                bar();
                sb = (sb == 2 * G1_STAGE) ? 0 : sb + G1_STAGE;
                rd(fa, 0, sb); mm(fb); interleave();
            }
            rd(fb, 1, sb); mm(fa); interleave();
            WAITL0();
            bar();
            mm(fb);
            STAMPT(4);
            tag = get_tag(c);
            rsrc_t rh = mkrsrc(p.ws + WS_H, S * F * 2);
#pragma unroll
            for (int mp = 0; mp < 2; ++mp)
#pragma unroll
                for (int i = 0; i < 3; ++i) {
                    float hv[8];
#pragma unroll
                    for (int e = 0; e < 8; ++e) hv[e] = accu[i][2 * mp + (e >> 2)][e & 3] * sigmoidf(accg[i][2 * mp + (e >> 2)][e & 3]);
                    const u32x4 o = {cvtpk(hv[0], hv[1]), cvtpk(hv[2], hv[3]), cvtpk(hv[4], hv[5]), cvtpk(hv[6], hv[7])};
                    const int sp = bs * 4 + wsx * 2 + mp, ft = bf * 6 + wf * 3 + i;
                    __builtin_amdgcn_raw_buffer_store_b128(o, rh, ((sp * 96 + ft) * 64 + lane) * 16, 0, 16);
                }
        }
        STAMPT(5);
        auto publish_h = [&]() {
            waitv<0>();
            if (lds_last(lds, L_CNT, 4, lane) && lane == 0) st_flag(p.ws, FL_H + bs * 64 + bf, tag);
        };
        bar();
        STAMPT(6);
        publish_h();
        if (has_conv) {
            const int ca = lane & 15, cg = lane >> 4;
            f32x4 cacc[8];
            u32x2 ckv[8];
#pragma unroll
            for (int n = 0; n < 8; ++n) cacc[n] = (f32x4){0, 0, 0, 0};
            const unsigned slot = ldsb + CV_BASE + wave * CV_SLOT;
            const unsigned sdw = *(const unsigned*)(p.sd + ((cb * 4 + wave) & ~1));
            unsigned pg = slot + 480 + 32 * ca + 16 * cg;
            unsigned pf[8];
#pragma unroll
            for (int j = 0; j < 8; ++j) pf[j] = slot + CV_KF + (1 - (ca & 1)) * CV_COPY + 476 + 16 * cg - 4 * (ca >> 1) + 512 * (7 - j);
            struct Res { s16x8 G[8]; s16x4 Fl[8], Fh[8]; } RA, RB;
            auto rd1 = [&](Res& R, int k) {
                const int j = k / 3, m = k % 3;
                if (m == 0) asm volatile("ds_read2_b32 %0, %1 offset0:0 offset1:1" : "=v"(R.Fl[j]) : "v"(pf[j]));
                else if (m == 1) asm volatile("ds_read2_b32 %0, %1 offset0:2 offset1:3" : "=v"(R.Fh[j]) : "v"(pf[j]));
                else asm volatile("ds_read_b128 %0, %1 offset:%2" : "=v"(R.G[j]) : "v"(pg), "n"(512 * j));
            };
            auto land = [&](Res& R) {
                asm volatile("s_waitcnt lgkmcnt(0)"
                             : "+v"(R.G[0]), "+v"(R.G[1]), "+v"(R.G[2]), "+v"(R.G[3]), "+v"(R.G[4]), "+v"(R.G[5]), "+v"(R.G[6]), "+v"(R.G[7]),
                               "+v"(R.Fl[0]), "+v"(R.Fl[1]), "+v"(R.Fl[2]), "+v"(R.Fl[3]), "+v"(R.Fl[4]), "+v"(R.Fl[5]), "+v"(R.Fl[6]), "+v"(R.Fl[7]));
                asm volatile(""
                             : "+v"(R.Fh[0]), "+v"(R.Fh[1]), "+v"(R.Fh[2]), "+v"(R.Fh[3]), "+v"(R.Fh[4]), "+v"(R.Fh[5]), "+v"(R.Fh[6]), "+v"(R.Fh[7]));
                SB0();
            };
            auto next_addr = [&](int j) {
                if (j < 8) asm volatile("v_subrev_u32 %0, 64, %0" : "+v"(pf[j]));
                else asm volatile("v_subrev_u32 %0, 64, %0" : "+v"(pg));
            };
            auto residue = [&](Res& R, Res& N) {
                int k = 0;
#pragma unroll
                for (int I = 0; I < 8; ++I) {
                    const s16x8 Fv = cat(R.Fl[I], R.Fh[I]);
#pragma unroll
                    for (int n = I; n < 8; ++n) {
                        cacc[n] = mfma16(Fv, R.G[n - I], cacc[n]);
                        if (k < 24) rd1(N, k);
                        else if (k < 33) next_addr(k - 24);
                        ++k;
                        SB0();
                    }
                }
                land(N);
            };
#pragma unroll
            for (int k = 0; k < 24; ++k) rd1(RA, k);
#pragma unroll
            for (int j = 0; j < 9; ++j) next_addr(j);
            land(RA);
#pragma unroll 1
            for (int r2 = 0; r2 < 4; ++r2) {
                residue(RA, RB);
                residue(RB, RA);
            }
#pragma unroll
            for (int n = 0; n < 8; ++n) ckv[n] = *(LAS u32x2*)(lds + CV_BASE + wave * CV_SLOT + CV_KF + 2 * (2044 - 256 * n - 16 * ca - 4 * cg));
            const float Dd = (wave & 1) ? bfhi(sdw) : bflo(sdw);
            const int d = cb * 4 + wave;
            rsrc_t ry = mkrsrc(p.ws + WS_YT, D * S * 2);
            u32x2 o[8];
#pragma unroll
            for (int n = 0; n < 8; ++n) {
                o[n][0] = cvtpk(cacc[n][0] + bfhi(ckv[n][1]) * Dd, cacc[n][1] + bflo(ckv[n][1]) * Dd);
                o[n][1] = cvtpk(cacc[n][2] + bfhi(ckv[n][0]) * Dd, cacc[n][3] + bflo(ckv[n][0]) * Dd);
            }
#pragma unroll
            for (int n = 0; n < 8; n += 2) {
                auto x = __builtin_amdgcn_permlane16_swap(o[n][0], o[n + 1][0], false, false);
                auto y = __builtin_amdgcn_permlane16_swap(o[n][1], o[n + 1][1], false, false);
                const u32x4 val = {x[0], y[0], x[1], y[1]};
                const int s = 256 * (n + (cg & 1)) + 16 * ca + 4 * (cg & 2);
                __builtin_amdgcn_raw_buffer_store_b128(val, ry, (d * S + s) * 2, 0, 16);
            }
        }
        STAMPT(7);
        {
            const int sp = wave & 1, kh = wave >> 1;
            const int s16 = lane & 15, g = lane >> 4, q = s16 >> 2, pp = lane & 3;
            const int srow = 64 * st + 32 * sp + 16 * kh + s16;
            f32x4 acc[2][5];
#pragma unroll
            for (int n = 0; n < 5; ++n) { acc[0][n] = (f32x4){0, 0, 0, 0}; acc[1][n] = (f32x4){0, 0, 0, 0}; }
            const unsigned pw = ldsb + kh * 10240 + lane * 16, ph = ldsb + wave * 4096 + lane * 16;
            struct Fr2 { s16x8 w[5]; u32x4 h[2]; } fa, fb;
            auto rd2 = [&](Fr2& f, int s4, int kk) {
                LAS char* bh = (LAS char*)(size_t)(ph + hslot(s4) + kk * 2048);
                LAS char* bw = (LAS char*)(size_t)(pw + wslot(s4) + kk * 5120);
                f.h[0] = *(LAS u32x4*)bh; f.h[1] = *(LAS u32x4*)(bh + 1024);
#pragma unroll
                for (int n = 0; n < 5; ++n) f.w[n] = *(LAS s16x8*)(bw + n * 1024);
            };
            auto swp = [&](Fr2& f) {
                unsigned a2 = f.h[0][2], a3 = f.h[0][3], b0 = f.h[1][0], b1 = f.h[1][1];
                asm volatile("v_swap_b32 %0, %1\n\tv_swap_b32 %2, %3\n\ts_nop 1" : "+v"(a2), "+v"(b0), "+v"(a3), "+v"(b1));
                f.h[0][2] = a2; f.h[0][3] = a3; f.h[1][0] = b0; f.h[1][1] = b1;
            };
            auto step = [&](const Fr2& f) {
                const s16x8 h0 = __builtin_bit_cast(s16x8, f.h[0]), h1 = __builtin_bit_cast(s16x8, f.h[1]);
#pragma unroll
                for (int n = 0; n < 5; ++n) { acc[0][n] = mfma16(f.w[n], h0, acc[0][n]); acc[1][n] = mfma16(f.w[n], h1, acc[1][n]); }
            };
            auto il2 = [&]() {
#pragma unroll
                for (int i = 0; i < 7; ++i) { SGB(0x008, 1); SGB(0x100, 1); }
                SGB(0x008, 3);
                SB0();
            };
            WAITL0();
            bar();
            STAMPT(8);
            rd2(fa, 0, 0);
            swp(fa);
#pragma unroll 1
            for (int t = 0; t < 12; ++t) {
                rd2(fb, t & 3, 1);
                step(fa);
                il2();
                swp(fb);
                SB0();
                if (t == 3) waitv<0>();
                bar();
                if (t == 3 && has_conv && wave == 0 && lane == 0) st_flag(p.ws, FL_Y + (cb / 20) * 64 + cb % 20, tag);
                rd2(fa, (t + 1) & 3, 0);
                step(fb);
                il2();
                swp(fa);
                SB0();
            }
            STAMPT(10);
#pragma unroll
            for (int n = 0; n < 5; ++n) *(LAS f32x4*)(lds + G2_RED + (wave * 5 + n) * 1024 + lane * 16) = kh ? acc[0][n] : acc[1][n];
            WAITL0();
            bar();
            STAMPT(11);
            const int lrow = 32 * sp + 16 * kh + s16;
            const float gt = *(LAS float*)(lds + G2_G + lrow * 4);
#pragma unroll
            for (int n = 0; n < 5; ++n) {
                const u32x2 qn = *(LAS u32x2*)(lds + G2_Q + lrow * 160 + (16 * n + 4 * g) * 2);
                const f32x4 o4 = (kh ? acc[1][n] : acc[0][n]) + *(LAS f32x4*)(lds + G2_RED + ((wave ^ 2) * 5 + n) * 1024 + lane * 16);
                const u32x2 yv = __builtin_bit_cast(u32x2, trread(lds + G2_Y + (16 * n + 4 * g + q) * 128 + (32 * sp + 16 * kh + 4 * pp) * 2));
                const float o0 = gt * (bflo(qn[0]) * bflo(yv[0])) + (1.f - gt) * o4[0];
                const float o1 = gt * (bfhi(qn[0]) * bfhi(yv[0])) + (1.f - gt) * o4[1];
                const float o2 = gt * (bflo(qn[1]) * bflo(yv[1])) + (1.f - gt) * o4[2];
                const float o3 = gt * (bfhi(qn[1]) * bfhi(yv[1])) + (1.f - gt) * o4[3];
                *(u32x2*)(p.out + (size_t)srow * D + 80 * ds + 16 * n + 4 * g) = (u32x2){cvtpk(o0, o1), cvtpk(o2, o3)};
            }
            STAMPT(12);
        }
    }
}

extern "C" void kernel_launch(void* const* d_in, const int* in_sizes, int n_in,
                              void* d_out, int out_size, void* d_ws, size_t ws_size,
                              hipStream_t stream) {
    Params p;
    p.q = (const u16*)d_in[0]; p.k = (const u16*)d_in[1]; p.v = (const u16*)d_in[2];
    p.sk = (const u16*)d_in[3]; p.ssk = (const u16*)d_in[4]; p.sd = (const u16*)d_in[5];
    p.wg = (const u16*)d_in[7]; p.wu = (const u16*)d_in[8]; p.wd = (const u16*)d_in[9]; p.gw = (const u16*)d_in[10];
    p.out = (u16*)d_out; p.ws = (char*)d_ws;
    hipFuncSetAttribute((const void*)k_fused, hipFuncAttributeMaxDynamicSharedMemorySize, LDS_BYTES);
    k_fused<<<256, 512, LDS_BYTES, stream>>>(p);
}
```

```cpp
#include <hip/hip_runtime.h>
#include <hip/hip_bf16.h>
#include <stdint.h>

typedef unsigned short u16;
typedef short s16x4 __attribute__((ext_vector_type(4)));
typedef short s16x8 __attribute__((ext_vector_type(8)));
typedef __bf16 b16x8 __attribute__((ext_vector_type(8)));
typedef __bf16 b16x2 __attribute__((ext_vector_type(2)));
typedef float f32x2 __attribute__((ext_vector_type(2)));
typedef float f32x4 __attribute__((ext_vector_type(4)));
typedef unsigned u32x2 __attribute__((ext_vector_type(2)));
typedef unsigned u32x4 __attribute__((ext_vector_type(4)));
#define LAS __attribute__((address_space(3)))
#define GAS __attribute__((address_space(1)))
#define DI __device__ __forceinline__

static constexpr int S = 2048, D = 640, F = 1536;
static constexpr int KRS = 2112;
static constexpr unsigned WS_H = 0, WS_KR = 8u << 20, WS_YT = 14u << 20, WS_G = 17u << 20, WS_WT = 18u << 20, WS_STAMP = 20u << 20, WS_FLAG = 24u << 20;
static constexpr unsigned FL_EPOCH = 0, FL_KR = 1024, FL_WT = 2048  , FL_H = 4096, FL_Y = 6144;
static constexpr unsigned OOB = 0xF0000000u;

struct Params {
    const u16 *q, *k, *v, *sk, *ssk, *sd, *wg, *wu, *wd, *gw;
    u16* out; char* ws;
};

DI float bflo(unsigned x) { return __uint_as_float(x << 16); }
DI float bfhi(unsigned x) { return __uint_as_float(x & 0xffff0000u); }
DI unsigned cvtpk(float lo, float hi) { f32x2 v = {lo, hi}; b16x2 b = __builtin_convertvector(v, b16x2); return __builtin_bit_cast(unsigned, b); }
DI unsigned rfl(unsigned x) { return __builtin_amdgcn_readfirstlane(x); }
DI float sigmoidf(float x) { return __builtin_amdgcn_rcpf(1.f + __builtin_amdgcn_exp2f(x * -1.44269504089f)); }
DI f32x4 mfma16(s16x8 a, s16x8 b, f32x4 c) {
    return __builtin_amdgcn_mfma_f32_16x16x32_bf16(__builtin_bit_cast(b16x8, a), __builtin_bit_cast(b16x8, b), c, 0, 0, 0);
}
DI s16x4 trread(LAS char* p) { return __builtin_amdgcn_ds_read_tr16_b64_v4i16((LAS s16x4*)p); }
DI s16x8 cat(s16x4 a, s16x4 b) { return __builtin_shufflevector(a, b, 0, 1, 2, 3, 4, 5, 6, 7); }
typedef __amdgpu_buffer_rsrc_t rsrc_t;
DI rsrc_t mkrsrc(const void* p, unsigned bytes) { return __builtin_amdgcn_make_buffer_rsrc((void*)p, 0, bytes, 0x00020000); }
DI void dma16(unsigned m0v, unsigned voff, rsrc_t r, unsigned soff) {
    asm volatile("s_mov_b32 m0, %0\n\ts_nop 0\n\tbuffer_load_dwordx4 %1, %2, %3 offen lds" ::"s"(m0v), "v"(voff), "s"(r), "s"(soff) : "m0", "memory");
}
DI void dma16_sc1(unsigned m0v, unsigned voff, rsrc_t r, unsigned soff) {
    asm volatile("s_mov_b32 m0, %0\n\ts_nop 0\n\tbuffer_load_dwordx4 %1, %2, %3 offen sc1 lds" ::"s"(m0v), "v"(voff), "s"(r), "s"(soff) : "m0", "memory");
}
DI u32x4 aload(rsrc_t r, unsigned voff) { u32x4 x; asm volatile("buffer_load_dwordx4 %0, %1, %2, 0 offen" : "=v"(x) : "v"(voff), "s"(r) : "memory"); return x; }
DI u32x4 aload_sc1(rsrc_t r, unsigned voff) { u32x4 x; asm volatile("buffer_load_dwordx4 %0, %1, %2, 0 offen sc1" : "=v"(x) : "v"(voff), "s"(r) : "memory"); return x; }
DI void prefetch_lines(unsigned scratch, rsrc_t r, unsigned voff) {
    asm volatile("s_mov_b32 m0, %0\n\ts_nop 0\n\tbuffer_load_dword %1, %2, 0 offen lds" ::"s"(scratch), "v"(voff), "s"(r) : "m0", "memory");
}
template <int N> DI void waitv() { asm volatile("s_waitcnt vmcnt(%0)" ::"n"(N) : "memory"); }
DI float dot2(unsigned a, unsigned b, float c) { return __builtin_amdgcn_fdot2_f32_bf16(__builtin_bit_cast(b16x2, a), __builtin_bit_cast(b16x2, b), c, false); }
template <int CTRL> DI float dpp_add(float x) { return x + __int_as_float(__builtin_amdgcn_update_dpp(0, __float_as_int(x), CTRL, 0xf, 0xf, true)); }
#define TIE(x) asm volatile("" : "+v"(x))
#define WAITL0() asm volatile("s_waitcnt lgkmcnt(0)" ::: "memory")
DI void bar() { asm volatile("" ::: "memory"); __builtin_amdgcn_s_barrier(); asm volatile("" ::: "memory"); }
#define SGB(m, n) __builtin_amdgcn_sched_group_barrier(m, n, 0)
#define SB0() __builtin_amdgcn_sched_barrier(0)
#define RLX_AGENT __ATOMIC_RELAXED, __HIP_MEMORY_SCOPE_AGENT

#define CODE_PF 38912
#define SLEEP_L 0
#define SLEEP_LK 12
#define SLEEP_K 0
#define SLEEP_C 12

DI void st_flag(char* ws, unsigned word, unsigned tag) { __hip_atomic_store((GAS unsigned*)(ws + WS_FLAG) + word, tag, RLX_AGENT); }
DI rsrc_t flag_rsrc(char* ws) { return mkrsrc(ws + WS_FLAG, 32768); }
DI bool all_tag(u32x4 x, unsigned tag) { return x[0] == tag && x[1] == tag && x[2] == tag && x[3] == tag; }
template <int NL>
DI void poll4(char* ws, unsigned word, unsigned tag, int lane, unsigned care = 15u) {
    rsrc_t r = flag_rsrc(ws);
    for (int spins = 0; spins < (1 << 18); ++spins) {
        u32x4 x;
        asm volatile("buffer_load_dwordx4 %0, %1, %2, 0 offen sc1\n\ts_waitcnt vmcnt(0)" : "=&v"(x) : "v"((word + (lane < NL ? lane : 0) * 4) * 4), "s"(r) : "memory");
        const bool ok = (x[0] == tag || !(care & 1)) && (x[1] == tag || !(care & 2)) && (x[2] == tag || !(care & 4)) && (x[3] == tag || !(care & 8));
        if (__all(ok)) return;
        __builtin_amdgcn_s_sleep(4);
    }
}
DI void lds_set(LAS char* lds, int off, unsigned v) { __hip_atomic_store((LAS unsigned*)(lds + off), v, __ATOMIC_RELAXED, __HIP_MEMORY_SCOPE_WORKGROUP); }
DI unsigned lds_get(LAS char* lds, int off) { return rfl(__hip_atomic_load((LAS unsigned*)(lds + off), __ATOMIC_RELAXED, __HIP_MEMORY_SCOPE_WORKGROUP)); }
DI unsigned lds_wait(LAS char* lds, int off) {
    unsigned v = 0;
    for (int spins = 0; spins < (1 << 22); ++spins) {
        v = lds_get(lds, off);
        if (v) break;
        __builtin_amdgcn_s_sleep(8);
    }
    asm volatile("" ::: "memory");
    return v;
}
DI bool lds_last(LAS char* lds, int off, unsigned n, int lane) {
    unsigned old = 0;
    if (lane == 0) old = __hip_atomic_fetch_add((LAS unsigned*)(lds + off), 1u, __ATOMIC_RELAXED, __HIP_MEMORY_SCOPE_WORKGROUP);
    return rfl(old) == n - 1;
}

#define STAMPT(i) do {} while (0)

constexpr int G1_STAGE = 40960;
constexpr int P0_LDS = 122880;
constexpr int TP = 272;
constexpr int P0_T = P0_LDS, P0_GW = P0_LDS + 128 * TP;
constexpr int CV_BASE = G1_STAGE;
constexpr int CV_COPY = 4160, CV_KF = 4608, CV_SLOT = CV_KF + 2 * CV_COPY;
constexpr int G2_WST = 20480, G2_HST = 16384;
constexpr int G2_W0 = 0, G2_W2 = CV_BASE + 4 * CV_SLOT, G2_W3 = CV_BASE;
constexpr int G2_H0 = G2_W2 + G2_WST, G2_H3 = G2_W3 + G2_WST;
constexpr int G2_Y = G2_H3 + G2_HST;
constexpr int G2_Q = G2_H0, G2_G = G2_H0 + 12288;
constexpr int WT_SCR = G2_H0;
constexpr int G2_RED = 0;
constexpr unsigned long long G2_WTAB = (unsigned long long)(G2_W0 >> 8) | (unsigned long long)((G2_W0 + G2_WST) >> 8) << 16 | (unsigned long long)(G2_W2 >> 8) << 32 | (unsigned long long)(G2_W3 >> 8) << 48;
constexpr unsigned long long G2_HTAB = (unsigned long long)(G2_H0 >> 8) | (unsigned long long)((G2_H0 + G2_HST) >> 8) << 16 | (unsigned long long)((G2_H0 + 2 * G2_HST) >> 8) << 32 | (unsigned long long)(G2_H3 >> 8) << 48;
DI int wslot(int s4) { return (int)((G2_WTAB >> (16 * s4)) & 0xFFFF) << 8; }
DI int hslot(int s4) { return (int)((G2_HTAB >> (16 * s4)) & 0xFFFF) << 8; }
constexpr int L_DUMMY = 163328;
constexpr int L_CNT = 163776;
constexpr int L_EPOCH = 163824;
constexpr int LDS_BYTES = 163840;

struct Ctx {
    Params p; LAS char* lds; unsigned ldsb; int lane, wave, xcd, cb, pb, bs, bf; unsigned tag;
};
DI void load_epoch(const Ctx& c) {
    rsrc_t r = mkrsrc(c.p.ws + WS_FLAG, 64);
    if (c.lane == 0) asm volatile("s_mov_b32 m0, %0\n\ts_nop 0\n\tbuffer_load_dword %1, %2, 0 offen sc1 lds" ::"s"(c.ldsb + L_EPOCH), "v"(0u), "s"(r) : "m0", "memory");
}
DI unsigned get_tag(const Ctx& c) { return lds_get(c.lds, L_EPOCH) + 1u; }

DI void kv_prepare(const Ctx& c) {
    const Params& p = c.p;
    LAS char* lds = c.lds;
    const int lane = c.lane, wave = c.wave;
    (void)wave;
    const int kdc = c.pb % 5, s0 = (c.pb / 5) * 128, d0 = kdc * 128, tid = c.wave * 64 + lane, c16 = tid & 15, rg = tid >> 4, s = s0 + 8 * rg;
    u32x4 kr[11], vr[8], w4[4];
    {
        rsrc_t rk = mkrsrc(p.k, S * D * 2), rv = mkrsrc(p.v, S * D * 2), rsk = mkrsrc(p.sk, D * 8);
#pragma unroll
        for (int i = 0; i < 11; ++i) kr[i] = aload(rk, (unsigned)(((s - 3 + i) * D + d0 + 8 * c16) * 2));
#pragma unroll
        for (int i = 0; i < 8; ++i) vr[i] = aload(rv, ((s + i) * D + d0 + 8 * c16) * 2);
#pragma unroll
        for (int i = 0; i < 4; ++i) w4[i] = aload(rsk, (d0 + 8 * c16) * 8 + i * 16);
    }
    waitv<0>();
    STAMPT(2);
#pragma unroll
    for (int i = 0; i < 11; ++i) TIE(kr[i]);
#pragma unroll
    for (int i = 0; i < 8; ++i) TIE(vr[i]);
#pragma unroll
    for (int i = 0; i < 4; ++i) TIE(w4[i]);
    LAS char* tw = lds + P0_T + 8 * c16 * TP + (((15 - rg) ^ c16) * 16);
#pragma unroll
    for (int e = 0; e < 8; ++e) {
        const unsigned sel = (e & 1) ? 0x07060302u : 0x05040100u;
        const unsigned wa = w4[e >> 1][(e & 1) * 2], wb = w4[e >> 1][(e & 1) * 2 + 1];
        unsigned pr[10];
#pragma unroll
        for (int i = 0; i < 10; ++i) pr[i] = __builtin_amdgcn_perm(kr[i][e >> 1], kr[i + 1][e >> 1], sel);
        float o[8];
#pragma unroll
        for (int r = 0; r < 8; ++r) o[r] = dot2(pr[r + 2], wa, dot2(pr[r], wb, 0.f)) * ((e & 1) ? bfhi(vr[r][e >> 1]) : bflo(vr[r][e >> 1]));
        *(LAS u32x4*)(tw + e * TP) = (u32x4){cvtpk(o[7], o[6]), cvtpk(o[5], o[4]), cvtpk(o[3], o[2]), cvtpk(o[1], o[0])};
    }
    STAMPT(13);
    WAITL0(); bar();
    rsrc_t r0 = mkrsrc(p.ws + WS_KR, D * KRS * 2);
#pragma unroll
    for (int it = 0; it < 8; ++it) {
        const int item = tid + it * 256, ch = item >> 4, j = item & 15;
        __builtin_amdgcn_raw_buffer_store_b128(*(LAS u32x4*)(lds + P0_T + ch * TP + ((j ^ ((ch >> 3) & 15)) * 16)), r0, ((d0 + ch) * KRS + (1920 - s0) + 8 * j) * 2, 0, 16);
    }
    STAMPT(14);
    waitv<0>();
    if (lds_last(lds, L_CNT + 16, 4, lane) && lane == 0) st_flag(p.ws, FL_KR + kdc * 64 + c.pb / 5, get_tag(c));
    STAMPT(15);
}

struct Tail { u32x4 wd[4]; bool kr_ready; };
template <int MODE>
DI void loader_gemm1(Ctx& c, Tail& X) {
    const Params& p = c.p;
    const int lane = c.lane, w = c.wave - 4, wave = c.wave;
    (void)wave;
    LAS char* lds = c.lds;
    rsrc_t rq = mkrsrc(p.q, S * D * 2), rg_ = mkrsrc(p.wg, D * F * 2), ru_ = mkrsrc(p.wu, D * F * 2);
    unsigned vx[2], vw[3];
    {
        const int r = lane >> 3, slot = lane & 7;
        vx[0] = r * 1280 + ((slot ^ (r >> 1)) * 16);
        vx[1] = r * 1280 + ((slot ^ (r >> 1) ^ 4) * 16);
#pragma unroll
        for (int j = 0; j < 3; ++j) {
            const int L = 2 * j + (lane >> 5), kbl = L / 3, cc = L % 3, row = (lane & 31) >> 2, ch = (lane & 3) ^ (2 * kbl);
            vw[j] = (kbl * 8 + row) * 3072 + cc * 64 + ch * 16;
        }
    }
    const unsigned sx0 = (c.bs * 128 + 32 * w) * 1280, sw0 = 16 * w * 3072 + c.bf * 192;
    auto issue = [&](int t) {
        const unsigned base = c.ldsb + (int)((0x24924924u >> (2 * t)) & 3) * G1_STAGE;
#pragma unroll
        for (int i = 0; i < 4; ++i) dma16(base + (32 * w + 8 * i) * 128, vx[i & 1], rq, sx0 + i * 8 * 1280 + t * 128);
#pragma unroll
        for (int j = 0; j < 3; ++j) dma16(base + 16384 + w * 3072 + j * 1024, vw[j], rg_, sw0 + t * 64 * 3072);
#pragma unroll
        for (int j = 0; j < 3; ++j) dma16(base + 28672 + w * 3072 + j * 1024, vw[j], ru_, sw0 + t * 64 * 3072);
    };
    if (MODE != 1) issue(0);
    if (w == 0) load_epoch(c);
    if (w < 2) {
        rsrc_t rgw = mkrsrc(p.gw, D * 2);
        if (w == 0 || lane < 16) dma16(c.ldsb + P0_GW + w * 1024, (w * 64 + lane) * 16, rgw, 0);
    }
    if (w == 3 && lane < 8) *(LAS unsigned*)(lds + L_CNT + lane * 4) = 0;
    if (MODE == 1) {
        waitv<0>();
        __builtin_amdgcn_s_sleep(SLEEP_K + SLEEP_LK);
        issue(0); issue(1);
        WAITL0(); bar();
        waitv<10>();
    } else {
        waitv<0>();
    }
    WAITL0(); bar();
    c.tag = get_tag(c);
    STAMPT(3);
    if (MODE != 1) issue(1);
    issue(2);
    const int grw = 8 * c.bf + (lane >> 3), gch = (lane & 7) ^ ((grw >> 1) & 7);
    float gacc = 0.f;
    auto gate_step = [&](int t) {
        const int buf = (int)((0x24924924u >> (2 * t)) & 3);
        const u32x4 xv = *(LAS u32x4*)(lds + buf * G1_STAGE + grw * 128 + (lane & 7) * 16);
        const u32x4 gv = *(LAS u32x4*)(lds + P0_GW + t * 128 + gch * 16);
#pragma unroll
        for (int i = 0; i < 4; ++i) gacc = dot2(xv[i], gv[i], gacc);
    };
    if (w == 3) gate_step(0);
#pragma unroll 1
    for (int t = 0; t < 6; ++t) {
        waitv<10>(); WAITL0(); bar();
        issue(t + 3);
        if (w == 3) gate_step(t + 1);
    }
    u32x4 kf4 = {0, 0, 0, 0};
    waitv<10>(); WAITL0(); bar();
    if (MODE == 0) kf4 = aload_sc1(flag_rsrc(p.ws), (FL_KR + (c.cb >> 5) * 64 + (lane & 3) * 4) * 4);
    issue(9);
    if (w == 3) gate_step(7);
    waitv<10>(); WAITL0(); bar();
    if (MODE == 0) {
        TIE(kf4);
        X.kr_ready = __all(all_tag(kf4, c.tag));
        rsrc_t rwd = mkrsrc(p.wd, F * D * 2);
        const int unit = c.cb * 3 + w, T = unit / 10, dl = unit % 10;
#pragma unroll
        for (int i = 0; i < 4; ++i) X.wd[i] = aload(rwd, w < 3 ? ((32 * T + 8 * i + (lane >> 3)) * D + 64 * dl + (lane & 7) * 8) * 2 : OOB);
    }
    if (w == 3) gate_step(8);
    if (MODE == 0) waitv<4>(); else waitv<0>();
    WAITL0(); bar();
    if (MODE == 0) {
        const int d = c.cb * 4 + w;
        const unsigned sl = c.ldsb + CV_BASE + w * CV_SLOT, vo = lane * 16;
        rsrc_t rf = mkrsrc(p.ssk, D * S * 2), r0 = mkrsrc(p.ws + WS_KR, D * KRS * 2);
#pragma unroll
        for (int i = 0; i < 4; ++i) dma16(sl + 512 + i * 1024, vo, rf, d * (S * 2) + i * 1024);
        if (X.kr_ready) {
#pragma unroll
            for (int i = 0; i < 4; ++i) dma16_sc1(sl + CV_KF + i * 1024, vo, r0, d * (KRS * 2) + i * 1024);
        }
        LAS char* slot = lds + CV_BASE + w * CV_SLOT;
        if (lane < 32) *(LAS u32x4*)(slot + lane * 16) = (u32x4){0, 0, 0, 0};
        else if (lane < 40) *(LAS u32x4*)(slot + CV_KF + ((lane - 32) >> 2) * CV_COPY + 4096 + (lane & 3) * 16) = (u32x4){0, 0, 0, 0};
    }
    if (w == 3) gate_step(9);
    WAITL0(); bar();
    waitv<0>();
    gacc = dpp_add<0x111>(gacc); gacc = dpp_add<0x112>(gacc); gacc = dpp_add<0x114>(gacc);
    if (w == 3) {
        rsrc_t rG = mkrsrc(p.ws + WS_G, S * 4);
        __builtin_amdgcn_raw_buffer_store_b32(__float_as_uint(sigmoidf(gacc)), rG, (lane & 7) == 7 ? (c.bs * 128 + grw) * 4 : OOB, 0, 16);
    }
    if (MODE == 0) {
#pragma unroll
        for (int i = 0; i < 4; ++i) TIE(X.wd[i]);
    }
}

__global__ __launch_bounds__(512, 2) void k_fused(Params p) {
    unsigned long long pc0;
    asm volatile("s_getpc_b64 %0" : "=s"(pc0));
    extern __shared__ __attribute__((aligned(16))) char smem[];
    LAS char* lds = (LAS char*)smem;
    const unsigned ldsb = (unsigned)(size_t)lds;
    const int wave = rfl(threadIdx.x >> 6), lane = threadIdx.x & 63;
    const int b = blockIdx.x, xcd = b & 7, idx = b >> 3;
    const bool has_conv = idx < 20;
    const int cb = xcd * 20 + idx, pb = xcd * 12 + idx - 20;
    const bool kvblock = !has_conv && pb < 80;
    const int bs = (xcd & 1) * 8 + (idx & 7), bf = (xcd >> 1) * 4 + (idx >> 3);
    const int st = xcd * 4 + (idx & 3), ds = idx >> 2;
    STAMPT(0);
    Ctx c; c.p = p; c.lds = lds; c.ldsb = ldsb; c.lane = lane; c.wave = wave; c.xcd = xcd; c.cb = cb; c.pb = pb; c.bs = bs; c.bf = bf; c.tag = 0;

    if (__builtin_expect(wave >= 4, 1)) {
        const int w = wave - 4;
        Tail X;
        X.kr_ready = false;
#pragma unroll
        for (int i = 0; i < 4; ++i) X.wd[i] = (u32x4){0, 0, 0, 0};
        if (has_conv) loader_gemm1<0>(c, X);
        else if (kvblock) loader_gemm1<1>(c, X);
        else loader_gemm1<2>(c, X);
        const unsigned tag = c.tag;
        STAMPT(4);
        const unsigned vo = lane * 16;
        rsrc_t rw = mkrsrc(p.ws + WS_WT, F * D * 2);
        if (has_conv) {
            LAS char* c0 = lds + CV_BASE + w * CV_SLOT + CV_KF;
            if (!X.kr_ready) {
                poll4<4>(p.ws, FL_KR + (cb >> 5) * 64, tag, lane);
                rsrc_t r0 = mkrsrc(p.ws + WS_KR, D * KRS * 2);
#pragma unroll
                for (int i = 0; i < 4; ++i) dma16_sc1(ldsb + CV_BASE + w * CV_SLOT + CV_KF + i * 1024, vo, r0, (cb * 4 + w) * (KRS * 2) + i * 1024);
                waitv<0>();
            }
            STAMPT(5);
            {
                const int unit = cb * 3 + w, Tk = unit / 10, dl = unit % 10, kh = Tk / 24, tt = (Tk % 24) >> 1, kk = Tk & 1;
                const int g4 = lane >> 4, q = (lane & 15) >> 2, pp = lane & 3, row = 4 * g4 + q;
                LAS char* buf = lds + WT_SCR + w * 4096;
#pragma unroll
                for (int i = 0; i < 4; ++i) *(LAS u32x4*)(buf + (8 * i + (lane >> 3)) * 128 + (lane & 7) * 16) = X.wd[i];
#pragma unroll
                for (int i = 0; i < 4; ++i) {
                    const int d16 = 4 * dl + i, dsl = d16 / 5, n = d16 % 5;
                    const s16x8 f = cat(trread(buf + row * 128 + 32 * i + 8 * pp), trread(buf + (row + 16) * 128 + 32 * i + 8 * pp));
                    __builtin_amdgcn_raw_buffer_store_b128(__builtin_bit_cast(u32x4, f), rw, w < 3 ? ((((dsl * 12 + tt) * 4 + kh * 2 + kk) * 5 + n) * 64 + lane) * 16 : OOB, 0, 16);
                }
            }
#pragma unroll
            for (int j = 0; j < 4; ++j) {
                const u32x4 a = *(LAS u32x4*)(c0 + j * 1024 + lane * 16);
                const unsigned nx = *(LAS unsigned*)(c0 + j * 1024 + lane * 16 + 16);
                *(LAS u32x4*)(c0 + CV_COPY + j * 1024 + lane * 16) =
                    (u32x4){__builtin_amdgcn_alignbit(a[1], a[0], 16), __builtin_amdgcn_alignbit(a[2], a[1], 16), __builtin_amdgcn_alignbit(a[3], a[2], 16), __builtin_amdgcn_alignbit(nx, a[3], 16)};
            }
        }
        waitv<0>();
        if (has_conv && lds_last(lds, L_CNT + 4, 4, lane) && lane < 8) st_flag(p.ws, FL_WT + lane * 256 + cb, tag);
        WAITL0();
        bar();
        STAMPT(6);
        const bool hi4 = ((st >> 1) & 7) >= 4;
        const int ne = hi4 ? 6 : 8;
        unsigned long long order;
        {
            const unsigned E = hi4 ? 0x876210u : 0x98763210u, L = hi4 ? 0xBA9543u : 0xBA54u;
            const int nl = 12 - ne, re = ds % ne, rl = ds % nl;
            const unsigned long long Er = (((unsigned long long)E << (4 * ne) | E) >> (4 * re)) & ((1ull << (4 * ne)) - 1);
            const unsigned long long Lr = (((unsigned long long)L << (4 * nl) | L) >> (4 * rl)) & ((1ull << (4 * nl)) - 1);
            order = Er | (Lr << (4 * ne));
        }
        rsrc_t rh = mkrsrc(p.ws + WS_H, S * F * 2);
        const unsigned sw2 = ds * (48 * 5 * 1024) + w * 5120, sh2 = ((st * 2 + (w & 1)) * 96 + (w >> 1) * 48) * 1024;
        auto stage_of = [&](int t) { return (int)((order >> (4 * t)) & 15); };
        auto w_dma = [&](int t) {
            const unsigned dst = ldsb + wslot(t & 3) + w * 5120, src = sw2 + stage_of(t) * G2_WST;
#pragma unroll
            for (int j = 0; j < 5; ++j) dma16_sc1(dst + j * 1024, vo, rw, src + j * 1024);
        };
        auto h_dma = [&](int t) {
            const unsigned dst = ldsb + hslot(t & 3) + w * 4096, src = sh2 + stage_of(t) * 4096;
#pragma unroll
            for (int j = 0; j < 4; ++j) dma16_sc1(dst + j * 1024, vo, rh, src + j * 1024);
        };
        const unsigned late_mask = hi4 ? 12u : 8u, hword = FL_H + (st >> 1) * 64;
        if (w == 0) {
            poll4<4>(p.ws, hword, tag, lane, 15u & ~late_mask);
            if (lane == 0) lds_set(lds, L_CNT + 20, 1u);
        } else {
            lds_wait(lds, L_CNT + 20);
        }
        STAMPT(10);
        h_dma(0); h_dma(1); h_dma(2);
        {
            const int ts = stage_of(lane >> 3 < 3 ? lane >> 3 : 0), T = 24 * ((lane >> 2) & 1) + 2 * ts + ((lane >> 1) & 1), dl = (5 * ds + (lane & 1) * 4) >> 2;
            const unsigned fo = (FL_WT + xcd * 256 + (10 * T + dl) / 3) * 4;
            if (w == 0) {
                rsrc_t r = flag_rsrc(p.ws);
                for (int spins = 0; spins < (1 << 18); ++spins) {
                    unsigned x;
                    asm volatile("buffer_load_dword %0, %1, %2, 0 offen sc1\n\ts_waitcnt vmcnt(0)" : "=&v"(x) : "v"(fo), "s"(r) : "memory");
                    if (__all(x == tag)) break;
                    __builtin_amdgcn_s_sleep(4);
                }
                if (lane == 0) lds_set(lds, L_CNT + 8, 1u);
            } else {
                lds_wait(lds, L_CNT + 8);
            }
        }
        STAMPT(9);
        w_dma(0); w_dma(1); w_dma(2);
        const u32x4 hl = aload_sc1(flag_rsrc(p.ws), (hword + (lane & 3) * 4) * 4);
        const u32x4 wl = aload_sc1(flag_rsrc(p.ws), (FL_WT + xcd * 256 + (lane < 40 ? lane : 0) * 4) * 4);
        waitv<0>();
        u32x4 hl_ = hl, wl_ = wl; TIE(hl_); TIE(wl_);
        const bool late_ok = __all(all_tag(hl_, tag));
        const bool wt_ok = __all(all_tag(wl_, tag));
        bar();
        STAMPT(7);
        if (!wt_ok) poll4<40>(p.ws, FL_WT + xcd * 256, tag, lane);
        h_dma(3); w_dma(3);
        bar();
#pragma unroll 1
        for (int t = 1; t <= 8; ++t) {
            waitv<9>(); bar();
            if (t + 3 == ne && !late_ok) poll4<4>(p.ws, hword, tag, lane);
            h_dma(t + 3); w_dma(t + 3);
        }
        rsrc_t ry = mkrsrc(p.ws + WS_YT, D * S * 2);
        const u32x4 yf = aload_sc1(flag_rsrc(p.ws), (FL_Y + ds * 64 + (lane < 5 ? lane : 0) * 4) * 4);
        waitv<10>(); bar();
        waitv<0>();
        u32x4 yf_ = yf; TIE(yf_);
        if (!__all(all_tag(yf_, tag))) poll4<5>(p.ws, FL_Y + ds * 64, tag, lane);
        u32x4 yv[3];
#pragma unroll
        for (int i = 0; i < 3; ++i) {
            const int it = w + 4 * i, ch = 8 * it + (lane >> 3);
            yv[i] = aload_sc1(ry, it < 10 ? ((80 * ds + ch) * S + 64 * st + (lane & 7) * 8) * 2 : OOB);
        }
        {
            rsrc_t rq = mkrsrc(p.q, S * D * 2), rG = mkrsrc(p.ws + WS_G, S * 4);
#pragma unroll
            for (int i = 0; i < 3; ++i) {
                const int ch = (w + 4 * i) * 64 + lane, row = ch / 10;
                dma16(ldsb + G2_Q + (w + 4 * i) * 1024, ch < 640 ? (64 * st + row) * (D * 2) + 160 * ds + (ch - row * 10) * 16 : OOB, rq, 0);
            }
            if (w == 0) asm volatile("s_mov_b32 m0, %0\n\ts_nop 0\n\tbuffer_load_dword %1, %2, 0 offen sc1 lds" ::"s"(ldsb + G2_G), "v"((64 * st + lane) * 4), "s"(rG) : "m0", "memory");
        }
        bar();
        bar();
        waitv<0>();
#pragma unroll
        for (int i = 0; i < 3; ++i) {
            const int it = w + 4 * i, ch = 8 * it + (lane >> 3);
            TIE(yv[i]);
            if (it < 10) *(LAS u32x4*)(lds + G2_Y + ch * 128 + (lane & 7) * 16) = yv[i];
        }
        WAITL0();
        bar();
        if (b == 0 && w == 0) {
            rsrc_t r = flag_rsrc(p.ws);
            for (int spins = 0; spins < (1 << 18); ++spins) {
                u32x4 x;
                asm volatile("buffer_load_dwordx4 %0, %1, %2, 0 offen sc1\n\ts_waitcnt vmcnt(0)" : "=&v"(x) : "v"((FL_H + (lane >> 2) * 64 + (lane & 3) * 4) * 4), "s"(r) : "memory");
                if (__all(all_tag(x, tag))) break;
                __builtin_amdgcn_s_sleep(4);
            }
            if (lane == 0) st_flag(p.ws, FL_EPOCH, tag);
        }
        STAMPT(8);
    } else {
        unsigned tag = 0;
        {
            const int wsx = wave & 1, wf = wave >> 1;
            const int g = lane >> 4, fr = lane & 15, q = (lane & 15) >> 2, pp = lane & 3;
            int oa[3];
#pragma unroll
            for (int i = 0; i < 3; ++i) {
                const int ft = 3 * wf + i, cc = ft >> 1, half = ft & 1;
                oa[i] = 16384 + (g * 3 + cc) * 512 + q * 64 + 16 * ((2 * half + (pp >> 1)) ^ (2 * (g & 1))) + 8 * (pp & 1);
            }
            const int ob0 = (64 * wsx + fr) * 128 + ((g ^ (fr >> 1)) * 16);
            const int ob1 = (64 * wsx + fr) * 128 + ((g ^ (fr >> 1) ^ 4) * 16);
            f32x4 accg[3][4], accu[3][4];
#pragma unroll
            for (int i = 0; i < 3; ++i)
#pragma unroll
                for (int m = 0; m < 4; ++m) { accg[i][m] = (f32x4){0, 0, 0, 0}; accu[i][m] = (f32x4){0, 0, 0, 0}; }
            struct Fr { s16x8 a[6]; s16x8 b[4]; } fa, fb;
            auto rd = [&](Fr& f, int ks, int sb) {
#pragma unroll
                for (int m = 0; m < 4; ++m) f.b[m] = *(LAS s16x8*)(lds + sb + (ks ? ob1 : ob0) + m * 2048);
#pragma unroll
                for (int i = 0; i < 3; ++i) {
                    f.a[i] = cat(trread(lds + sb + oa[i] + ks * 6144), trread(lds + sb + oa[i] + ks * 6144 + 256));
                    f.a[3 + i] = cat(trread(lds + sb + oa[i] + ks * 6144 + 12288), trread(lds + sb + oa[i] + ks * 6144 + 256 + 12288));
                }
            };
            auto mm = [&](const Fr& f) {
#pragma unroll
                for (int i = 0; i < 3; ++i)
#pragma unroll
                    for (int m = 0; m < 4; ++m) {
                        accg[i][m] = mfma16(f.a[i], f.b[m], accg[i][m]);
                        accu[i][m] = mfma16(f.a[3 + i], f.b[m], accu[i][m]);
                    }
            };
            auto interleave = [&]() {
#pragma unroll
                for (int i = 0; i < 16; ++i) { SGB(0x008, 1); SGB(0x100, 1); }
                SGB(0x008, 8);
                SB0();
            };
            const int pi = kvblock ? pb : (has_conv ? cb : 160 + pb - 80), np = kvblock ? 80 : 176, t0 = kvblock ? 6 : 2;
            if (wave == 3) {
                rsrc_t rc = mkrsrc((const void*)pc0, CODE_PF);
                prefetch_lines(ldsb + L_DUMMY, rc, lane < CODE_PF / 4096 + 1 ? (idx * (CODE_PF / 4096 + 1) + lane) * 128 : OOB);
            }
            if (kvblock) { __builtin_amdgcn_s_sleep(SLEEP_K); kv_prepare(c); } else __builtin_amdgcn_s_sleep(SLEEP_C);
            {
                rsrc_t rpf = mkrsrc(wave == 0 ? p.q : (wave == 1 ? p.wg : p.wu), wave == 0 ? S * D * 2 : D * F * 2);
                const int nl = wave == 0 ? 8192 : 6144;
                for (int l0 = pi * 64; l0 < nl && wave < 3; l0 += np * 64) {
                    const int l = l0 + lane;
                    prefetch_lines(ldsb + L_DUMMY, rpf, wave == 0 ? (l >> 2) * 1280 + (t0 + (l & 3)) * 128 : (t0 * 64 + l / 24) * 3072 + (l % 24) * 128);
                }
                if (kvblock && wave == 3) {
                    rsrc_t rwd = mkrsrc(p.wd, F * D * 2), rf = mkrsrc(p.ssk, D * S * 2);
                    for (int l0 = pb * 64; l0 < 15360; l0 += 80 * 64) prefetch_lines(ldsb + L_DUMMY, rwd, (l0 + lane) * 128);
                    for (int l0 = pb * 64; l0 < 20480; l0 += 80 * 64) prefetch_lines(ldsb + L_DUMMY, rf, (l0 + lane) * 128);
                }
            }
            __builtin_amdgcn_s_setprio(1);
            bar();
            STAMPT(3);
            rd(fa, 0, 0);
            int sb = 0;
#pragma unroll 1
            for (int t = 0; t < 9; ++t) {
                rd(fb, 1, sb); mm(fa); interleave();
                WAITL0();
                bar();
                sb = (sb == 2 * G1_STAGE) ? 0 : sb + G1_STAGE;
                rd(fa, 0, sb); mm(fb); interleave();
            }
            rd(fb, 1, sb); mm(fa); interleave();
            WAITL0();
            bar();
            mm(fb);
            STAMPT(4);
            tag = get_tag(c);
            rsrc_t rh = mkrsrc(p.ws + WS_H, S * F * 2);
#pragma unroll
            for (int mp = 0; mp < 2; ++mp)
#pragma unroll
                for (int i = 0; i < 3; ++i) {
                    float hv[8];
#pragma unroll
                    for (int e = 0; e < 8; ++e) hv[e] = accu[i][2 * mp + (e >> 2)][e & 3] * sigmoidf(accg[i][2 * mp + (e >> 2)][e & 3]);
                    const u32x4 o = {cvtpk(hv[0], hv[1]), cvtpk(hv[2], hv[3]), cvtpk(hv[4], hv[5]), cvtpk(hv[6], hv[7])};
                    const int sp = bs * 4 + wsx * 2 + mp, ft = bf * 6 + wf * 3 + i;
                    __builtin_amdgcn_raw_buffer_store_b128(o, rh, ((sp * 96 + ft) * 64 + lane) * 16, 0, 16);
                }
        }
        STAMPT(5);
        auto publish_h = [&]() {
            waitv<0>();
            if (lds_last(lds, L_CNT, 4, lane) && lane == 0) st_flag(p.ws, FL_H + bs * 64 + bf, tag);
        };
        bar();
        STAMPT(6);
        publish_h();
        if (has_conv) {
            const int ca = lane & 15, cg = lane >> 4;
            f32x4 cacc[8];
            u32x2 ckv[8];
#pragma unroll
            for (int n = 0; n < 8; ++n) cacc[n] = (f32x4){0, 0, 0, 0};
            const unsigned slot = ldsb + CV_BASE + wave * CV_SLOT;
            const unsigned sdw = *(const unsigned*)(p.sd + ((cb * 4 + wave) & ~1));
            unsigned pg = slot + 480 + 32 * ca + 16 * cg;
            unsigned pf[8];
#pragma unroll
            for (int j = 0; j < 8; ++j) pf[j] = slot + CV_KF + (1 - (ca & 1)) * CV_COPY + 476 + 16 * cg - 4 * (ca >> 1) + 512 * (7 - j);
            struct Res { s16x8 G[8]; s16x4 Fl[8], Fh[8]; } RA, RB;
            auto rd1 = [&](Res& R, int k) {
                const int j = k / 3, m = k % 3;
                if (m == 0) asm volatile("ds_read2_b32 %0, %1 offset0:0 offset1:1" : "=v"(R.Fl[j]) : "v"(pf[j]));
                else if (m == 1) asm volatile("ds_read2_b32 %0, %1 offset0:2 offset1:3" : "=v"(R.Fh[j]) : "v"(pf[j]));
                else asm volatile("ds_read_b128 %0, %1 offset:%2" : "=v"(R.G[j]) : "v"(pg), "n"(512 * j));
            };
            auto land = [&](Res& R) {
                asm volatile("s_waitcnt lgkmcnt(0)"
                             : "+v"(R.G[0]), "+v"(R.G[1]), "+v"(R.G[2]), "+v"(R.G[3]), "+v"(R.G[4]), "+v"(R.G[5]), "+v"(R.G[6]), "+v"(R.G[7]),
                               "+v"(R.Fl[0]), "+v"(R.Fl[1]), "+v"(R.Fl[2]), "+v"(R.Fl[3]), "+v"(R.Fl[4]), "+v"(R.Fl[5]), "+v"(R.Fl[6]), "+v"(R.Fl[7]));
                asm volatile(""
                             : "+v"(R.Fh[0]), "+v"(R.Fh[1]), "+v"(R.Fh[2]), "+v"(R.Fh[3]), "+v"(R.Fh[4]), "+v"(R.Fh[5]), "+v"(R.Fh[6]), "+v"(R.Fh[7]));
                SB0();
            };
            auto next_addr = [&](int j) {
                if (j < 8) asm volatile("v_subrev_u32 %0, 64, %0" : "+v"(pf[j]));
                else asm volatile("v_subrev_u32 %0, 64, %0" : "+v"(pg));
            };
            auto residue = [&](Res& R, Res& N) {
                int k = 0;
#pragma unroll
                for (int I = 0; I < 8; ++I) {
                    const s16x8 Fv = cat(R.Fl[I], R.Fh[I]);
#pragma unroll
                    for (int n = I; n < 8; ++n) {
                        cacc[n] = mfma16(Fv, R.G[n - I], cacc[n]);
                        if (k < 24) rd1(N, k);
                        else if (k < 33) next_addr(k - 24);
                        ++k;
                        SB0();
                    }
                }
                land(N);
            };
#pragma unroll
            for (int k = 0; k < 24; ++k) rd1(RA, k);
#pragma unroll
            for (int j = 0; j < 9; ++j) next_addr(j);
            land(RA);
#pragma unroll 1
            for (int r2 = 0; r2 < 4; ++r2) {
                residue(RA, RB);
                residue(RB, RA);
            }
#pragma unroll
            for (int n = 0; n < 8; ++n) ckv[n] = *(LAS u32x2*)(lds + CV_BASE + wave * CV_SLOT + CV_KF + 2 * (2044 - 256 * n - 16 * ca - 4 * cg));
            const float Dd = (wave & 1) ? bfhi(sdw) : bflo(sdw);
            const int d = cb * 4 + wave;
            rsrc_t ry = mkrsrc(p.ws + WS_YT, D * S * 2);
            u32x2 o[8];
#pragma unroll
            for (int n = 0; n < 8; ++n) {
                o[n][0] = cvtpk(cacc[n][0] + bfhi(ckv[n][1]) * Dd, cacc[n][1] + bflo(ckv[n][1]) * Dd);
                o[n][1] = cvtpk(cacc[n][2] + bfhi(ckv[n][0]) * Dd, cacc[n][3] + bflo(ckv[n][0]) * Dd);
            }
#pragma unroll
            for (int n = 0; n < 8; n += 2) {
                auto x = __builtin_amdgcn_permlane16_swap(o[n][0], o[n + 1][0], false, false);
                auto y = __builtin_amdgcn_permlane16_swap(o[n][1], o[n + 1][1], false, false);
                const u32x4 val = {x[0], y[0], x[1], y[1]};
                const int s = 256 * (n + (cg & 1)) + 16 * ca + 4 * (cg & 2);
                __builtin_amdgcn_raw_buffer_store_b128(val, ry, (d * S + s) * 2, 0, 16);
            }
        }
        STAMPT(7);
        {
            const int sp = wave & 1, kh = wave >> 1;
            const int s16 = lane & 15, g = lane >> 4, q = s16 >> 2, pp = lane & 3;
            const int srow = 64 * st + 32 * sp + 16 * kh + s16;
            f32x4 acc[2][5];
#pragma unroll
            for (int n = 0; n < 5; ++n) { acc[0][n] = (f32x4){0, 0, 0, 0}; acc[1][n] = (f32x4){0, 0, 0, 0}; }
            const unsigned pw = ldsb + kh * 10240 + lane * 16, ph = ldsb + wave * 4096 + lane * 16;
            struct Fr2 { s16x8 w[5]; u32x4 h[2]; } fa, fb;
            auto rd2 = [&](Fr2& f, int s4, int kk) {
                LAS char* bh = (LAS char*)(size_t)(ph + hslot(s4) + kk * 2048);
                LAS char* bw = (LAS char*)(size_t)(pw + wslot(s4) + kk * 5120);
                f.h[0] = *(LAS u32x4*)bh; f.h[1] = *(LAS u32x4*)(bh + 1024);
#pragma unroll
                for (int n = 0; n < 5; ++n) f.w[n] = *(LAS s16x8*)(bw + n * 1024);
            };
            auto swp = [&](Fr2& f) {
                unsigned a2 = f.h[0][2], a3 = f.h[0][3], b0 = f.h[1][0], b1 = f.h[1][1];
                asm volatile("v_swap_b32 %0, %1\n\tv_swap_b32 %2, %3\n\ts_nop 1" : "+v"(a2), "+v"(b0), "+v"(a3), "+v"(b1));
                f.h[0][2] = a2; f.h[0][3] = a3; f.h[1][0] = b0; f.h[1][1] = b1;
            };
            auto step = [&](const Fr2& f) {
                const s16x8 h0 = __builtin_bit_cast(s16x8, f.h[0]), h1 = __builtin_bit_cast(s16x8, f.h[1]);
#pragma unroll
                for (int n = 0; n < 5; ++n) { acc[0][n] = mfma16(f.w[n], h0, acc[0][n]); acc[1][n] = mfma16(f.w[n], h1, acc[1][n]); }
            };
            auto il2 = [&]() {
#pragma unroll
                for (int i = 0; i < 7; ++i) { SGB(0x008, 1); SGB(0x100, 1); }
                SGB(0x008, 3);
                SB0();
            };
            WAITL0();
            bar();
            STAMPT(8);
            rd2(fa, 0, 0);
            swp(fa);
#pragma unroll 1
            for (int t = 0; t < 12; ++t) {
                rd2(fb, t & 3, 1);
                step(fa);
                il2();
                swp(fb);
                SB0();
                if (t == 3) waitv<0>();
                bar();
                if (t == 3 && has_conv && wave == 0 && lane == 0) st_flag(p.ws, FL_Y + (cb / 20) * 64 + cb % 20, tag);
                rd2(fa, (t + 1) & 3, 0);
                step(fb);
                il2();
                swp(fa);
                SB0();
            }
            STAMPT(10);
#pragma unroll
            for (int n = 0; n < 5; ++n) *(LAS f32x4*)(lds + G2_RED + (wave * 5 + n) * 1024 + lane * 16) = kh ? acc[0][n] : acc[1][n];
            WAITL0();
            bar();
            STAMPT(11);
            const int lrow = 32 * sp + 16 * kh + s16;
            const float gt = *(LAS float*)(lds + G2_G + lrow * 4);
#pragma unroll
            for (int n = 0; n < 5; ++n) {
                const u32x2 qn = *(LAS u32x2*)(lds + G2_Q + lrow * 160 + (16 * n + 4 * g) * 2);
                const f32x4 o4 = (kh ? acc[1][n] : acc[0][n]) + *(LAS f32x4*)(lds + G2_RED + ((wave ^ 2) * 5 + n) * 1024 + lane * 16);
                const u32x2 yv = __builtin_bit_cast(u32x2, trread(lds + G2_Y + (16 * n + 4 * g + q) * 128 + (32 * sp + 16 * kh + 4 * pp) * 2));
                const float o0 = gt * (bflo(qn[0]) * bflo(yv[0])) + (1.f - gt) * o4[0];
                const float o1 = gt * (bfhi(qn[0]) * bfhi(yv[0])) + (1.f - gt) * o4[1];
                const float o2 = gt * (bflo(qn[1]) * bflo(yv[1])) + (1.f - gt) * o4[2];
                const float o3 = gt * (bfhi(qn[1]) * bfhi(yv[1])) + (1.f - gt) * o4[3];
                *(u32x2*)(p.out + (size_t)srow * D + 80 * ds + 16 * n + 4 * g) = (u32x2){cvtpk(o0, o1), cvtpk(o2, o3)};
            }
            STAMPT(12);
        }
    }
}

extern "C" void kernel_launch(void* const* d_in, const int* in_sizes, int n_in,
                              void* d_out, int out_size, void* d_ws, size_t ws_size,
                              hipStream_t stream) {
    Params p;
    p.q = (const u16*)d_in[0]; p.k = (const u16*)d_in[1]; p.v = (const u16*)d_in[2];
    p.sk = (const u16*)d_in[3]; p.ssk = (const u16*)d_in[4]; p.sd = (const u16*)d_in[5];
    p.wg = (const u16*)d_in[7]; p.wu = (const u16*)d_in[8]; p.wd = (const u16*)d_in[9]; p.gw = (const u16*)d_in[10];
    p.out = (u16*)d_out; p.ws = (char*)d_ws;
    hipFuncSetAttribute((const void*)k_fused, hipFuncAttributeMaxDynamicSharedMemorySize, LDS_BYTES);
    k_fused<<<256, 512, LDS_BYTES, stream>>>(p);
}
```

```cpp
#include <hip/hip_runtime.h>
#include <hip/hip_bf16.h>
#include <stdint.h>

typedef unsigned short u16;
typedef short s16x4 __attribute__((ext_vector_type(4)));
typedef short s16x8 __attribute__((ext_vector_type(8)));
typedef __bf16 b16x8 __attribute__((ext_vector_type(8)));
typedef __bf16 b16x2 __attribute__((ext_vector_type(2)));
typedef float f32x2 __attribute__((ext_vector_type(2)));
typedef float f32x4 __attribute__((ext_vector_type(4)));
typedef unsigned u32x2 __attribute__((ext_vector_type(2)));
typedef unsigned u32x4 __attribute__((ext_vector_type(4)));
#define LAS __attribute__((address_space(3)))
#define GAS __attribute__((address_space(1)))
#define DI __device__ __forceinline__

static constexpr int S = 2048, D = 640, F = 1536;
static constexpr int KRS = 2112;
static constexpr unsigned WS_H = 0, WS_KR = 8u << 20, WS_YT = 14u << 20, WS_G = 17u << 20, WS_WT = 18u << 20, WS_FLAG = 24u << 20;
static constexpr unsigned FL_EPOCH = 0, FL_KR = 1024, FL_WT = 2048  , FL_H = 4096, FL_Y = 6144;
static constexpr unsigned OOB = 0xF0000000u;

struct Params {
    const u16 *q, *k, *v, *sk, *ssk, *sd, *wg, *wu, *wd, *gw;
    u16* out; char* ws;
};

DI float bflo(unsigned x) { return __uint_as_float(x << 16); }
DI float bfhi(unsigned x) { return __uint_as_float(x & 0xffff0000u); }
DI unsigned cvtpk(float lo, float hi) { f32x2 v = {lo, hi}; b16x2 b = __builtin_convertvector(v, b16x2); return __builtin_bit_cast(unsigned, b); }
DI unsigned rfl(unsigned x) { return __builtin_amdgcn_readfirstlane(x); }
DI float sigmoidf(float x) { return __builtin_amdgcn_rcpf(1.f + __builtin_amdgcn_exp2f(x * -1.44269504089f)); }
DI f32x4 mfma16(s16x8 a, s16x8 b, f32x4 c) {
    return __builtin_amdgcn_mfma_f32_16x16x32_bf16(__builtin_bit_cast(b16x8, a), __builtin_bit_cast(b16x8, b), c, 0, 0, 0);
}
DI s16x4 trread(LAS char* p) { return __builtin_amdgcn_ds_read_tr16_b64_v4i16((LAS s16x4*)p); }
DI s16x8 cat(s16x4 a, s16x4 b) { return __builtin_shufflevector(a, b, 0, 1, 2, 3, 4, 5, 6, 7); }
typedef __amdgpu_buffer_rsrc_t rsrc_t;
DI rsrc_t mkrsrc(const void* p, unsigned bytes) { return __builtin_amdgcn_make_buffer_rsrc((void*)p, 0, bytes, 0x00020000); }
DI void dma16(unsigned m0v, unsigned voff, rsrc_t r, unsigned soff) {
    asm volatile("s_mov_b32 m0, %0\n\ts_nop 0\n\tbuffer_load_dwordx4 %1, %2, %3 offen lds" ::"s"(m0v), "v"(voff), "s"(r), "s"(soff) : "m0", "memory");
}
DI void dma16_sc1(unsigned m0v, unsigned voff, rsrc_t r, unsigned soff) {
    asm volatile("s_mov_b32 m0, %0\n\ts_nop 0\n\tbuffer_load_dwordx4 %1, %2, %3 offen sc1 lds" ::"s"(m0v), "v"(voff), "s"(r), "s"(soff) : "m0", "memory");
}
DI u32x4 aload(rsrc_t r, unsigned voff) { u32x4 x; asm volatile("buffer_load_dwordx4 %0, %1, %2, 0 offen" : "=v"(x) : "v"(voff), "s"(r) : "memory"); return x; }
DI u32x4 aload_sc1(rsrc_t r, unsigned voff) { u32x4 x; asm volatile("buffer_load_dwordx4 %0, %1, %2, 0 offen sc1" : "=v"(x) : "v"(voff), "s"(r) : "memory"); return x; }
DI void prefetch_lines(unsigned scratch, rsrc_t r, unsigned voff) {
    asm volatile("s_mov_b32 m0, %0\n\ts_nop 0\n\tbuffer_load_dword %1, %2, 0 offen lds" ::"s"(scratch), "v"(voff), "s"(r) : "m0", "memory");
}
template <int N> DI void waitv() { asm volatile("s_waitcnt vmcnt(%0)" ::"n"(N) : "memory"); }
DI float dot2(unsigned a, unsigned b, float c) { return __builtin_amdgcn_fdot2_f32_bf16(__builtin_bit_cast(b16x2, a), __builtin_bit_cast(b16x2, b), c, false); }
template <int CTRL> DI float dpp_add(float x) { return x + __int_as_float(__builtin_amdgcn_update_dpp(0, __float_as_int(x), CTRL, 0xf, 0xf, true)); }
#define TIE(x) asm volatile("" : "+v"(x))
#define WAITL0() asm volatile("s_waitcnt lgkmcnt(0)" ::: "memory")
DI void bar() { asm volatile("" ::: "memory"); __builtin_amdgcn_s_barrier(); asm volatile("" ::: "memory"); }
#define SGB(m, n) __builtin_amdgcn_sched_group_barrier(m, n, 0)
#define SB0() __builtin_amdgcn_sched_barrier(0)
#define RLX_AGENT __ATOMIC_RELAXED, __HIP_MEMORY_SCOPE_AGENT

constexpr int CODE_PF = 36864;
constexpr int SLEEP_LK = 4;
constexpr int SLEEP_C = 6;

DI void st_flag(char* ws, unsigned word, unsigned tag) { __hip_atomic_store((GAS unsigned*)(ws + WS_FLAG) + word, tag, RLX_AGENT); }
DI rsrc_t flag_rsrc(char* ws) { return mkrsrc(ws + WS_FLAG, 32768); }
DI bool all_tag(u32x4 x, unsigned tag) { return x[0] == tag && x[1] == tag && x[2] == tag && x[3] == tag; }
template <int NL>
DI void poll4(char* ws, unsigned word, unsigned tag, int lane, unsigned care = 15u) {
    rsrc_t r = flag_rsrc(ws);
    for (int spins = 0; spins < (1 << 18); ++spins) {
        u32x4 x;
        asm volatile("buffer_load_dwordx4 %0, %1, %2, 0 offen sc1\n\ts_waitcnt vmcnt(0)" : "=&v"(x) : "v"((word + (lane < NL ? lane : 0) * 4) * 4), "s"(r) : "memory");
        const bool ok = (x[0] == tag || !(care & 1)) && (x[1] == tag || !(care & 2)) && (x[2] == tag || !(care & 4)) && (x[3] == tag || !(care & 8));
        if (__all(ok)) return;
        __builtin_amdgcn_s_sleep(4);
    }
}
DI void lds_set(LAS char* lds, int off, unsigned v) { __hip_atomic_store((LAS unsigned*)(lds + off), v, __ATOMIC_RELAXED, __HIP_MEMORY_SCOPE_WORKGROUP); }
DI unsigned lds_get(LAS char* lds, int off) { return rfl(__hip_atomic_load((LAS unsigned*)(lds + off), __ATOMIC_RELAXED, __HIP_MEMORY_SCOPE_WORKGROUP)); }
DI unsigned lds_wait(LAS char* lds, int off) {
    unsigned v = 0;
    for (int spins = 0; spins < (1 << 22); ++spins) {
        v = lds_get(lds, off);
        if (v) break;
        __builtin_amdgcn_s_sleep(8);
    }
    asm volatile("" ::: "memory");
    return v;
}
DI bool lds_last(LAS char* lds, int off, unsigned n, int lane) {
    unsigned old = 0;
    if (lane == 0) old = __hip_atomic_fetch_add((LAS unsigned*)(lds + off), 1u, __ATOMIC_RELAXED, __HIP_MEMORY_SCOPE_WORKGROUP);
    return rfl(old) == n - 1;
}

constexpr int G1_STAGE = 40960;
constexpr int P0_LDS = 122880;
constexpr int TP = 272;
constexpr int P0_T = P0_LDS, P0_GW = P0_LDS + 128 * TP;
constexpr int CV_BASE = G1_STAGE;
constexpr int CV_COPY = 4160, CV_KF = 4608, CV_SLOT = CV_KF + 2 * CV_COPY;
constexpr int G2_WST = 20480, G2_HST = 16384;
constexpr int G2_W0 = 0, G2_W2 = CV_BASE + 4 * CV_SLOT, G2_W3 = CV_BASE;
constexpr int G2_H0 = G2_W2 + G2_WST, G2_H3 = G2_W3 + G2_WST;
constexpr int G2_Y = G2_H3 + G2_HST;
constexpr int G2_Q = G2_H0, G2_G = G2_H0 + 12288;
constexpr int WT_SCR = G2_H0;
constexpr int G2_RED = 0;
constexpr unsigned long long G2_WTAB = (unsigned long long)(G2_W0 >> 8) | (unsigned long long)((G2_W0 + G2_WST) >> 8) << 16 | (unsigned long long)(G2_W2 >> 8) << 32 | (unsigned long long)(G2_W3 >> 8) << 48;
constexpr unsigned long long G2_HTAB = (unsigned long long)(G2_H0 >> 8) | (unsigned long long)((G2_H0 + G2_HST) >> 8) << 16 | (unsigned long long)((G2_H0 + 2 * G2_HST) >> 8) << 32 | (unsigned long long)(G2_H3 >> 8) << 48;
DI int wslot(int s4) { return (int)((G2_WTAB >> (16 * s4)) & 0xFFFF) << 8; }
DI int hslot(int s4) { return (int)((G2_HTAB >> (16 * s4)) & 0xFFFF) << 8; }
constexpr int L_DUMMY = 163328;
constexpr int L_CNT = 163776;
constexpr int L_EPOCH = 163824;
constexpr int LDS_BYTES = 163840;

struct Ctx {
    Params p; LAS char* lds; unsigned ldsb; int lane, wave, xcd, cb, pb, bs, bf; unsigned tag;
};
DI void load_epoch(const Ctx& c) {
    rsrc_t r = mkrsrc(c.p.ws + WS_FLAG, 64);
    if (c.lane == 0) asm volatile("s_mov_b32 m0, %0\n\ts_nop 0\n\tbuffer_load_dword %1, %2, 0 offen sc1 lds" ::"s"(c.ldsb + L_EPOCH), "v"(0u), "s"(r) : "m0", "memory");
}
DI unsigned get_tag(const Ctx& c) { return lds_get(c.lds, L_EPOCH) + 1u; }

DI void kv_prepare(const Ctx& c) {
    const Params& p = c.p;
    LAS char* lds = c.lds;
    const int lane = c.lane;
    const int kdc = c.pb % 5, s0 = (c.pb / 5) * 128, d0 = kdc * 128, tid = c.wave * 64 + lane, c16 = tid & 15, rg = tid >> 4, s = s0 + 8 * rg;
    u32x4 kr[11], vr[8], w4[4];
    {
        rsrc_t rk = mkrsrc(p.k, S * D * 2), rv = mkrsrc(p.v, S * D * 2), rsk = mkrsrc(p.sk, D * 8);
#pragma unroll
        for (int i = 0; i < 11; ++i) kr[i] = aload(rk, (unsigned)(((s - 3 + i) * D + d0 + 8 * c16) * 2));
#pragma unroll
        for (int i = 0; i < 8; ++i) vr[i] = aload(rv, ((s + i) * D + d0 + 8 * c16) * 2);
#pragma unroll
        for (int i = 0; i < 4; ++i) w4[i] = aload(rsk, (d0 + 8 * c16) * 8 + i * 16);
    }
    waitv<0>();
#pragma unroll
    for (int i = 0; i < 11; ++i) TIE(kr[i]);
#pragma unroll
    for (int i = 0; i < 8; ++i) TIE(vr[i]);
#pragma unroll
    for (int i = 0; i < 4; ++i) TIE(w4[i]);
    LAS char* tw = lds + P0_T + 8 * c16 * TP + (((15 - rg) ^ c16) * 16);
#pragma unroll
    for (int e = 0; e < 8; ++e) {
        const unsigned sel = (e & 1) ? 0x07060302u : 0x05040100u;
        const unsigned wa = w4[e >> 1][(e & 1) * 2], wb = w4[e >> 1][(e & 1) * 2 + 1];
        unsigned pr[10];
#pragma unroll
        for (int i = 0; i < 10; ++i) pr[i] = __builtin_amdgcn_perm(kr[i][e >> 1], kr[i + 1][e >> 1], sel);
        float o[8];
#pragma unroll
        for (int r = 0; r < 8; ++r) o[r] = dot2(pr[r + 2], wa, dot2(pr[r], wb, 0.f)) * ((e & 1) ? bfhi(vr[r][e >> 1]) : bflo(vr[r][e >> 1]));
        *(LAS u32x4*)(tw + e * TP) = (u32x4){cvtpk(o[7], o[6]), cvtpk(o[5], o[4]), cvtpk(o[3], o[2]), cvtpk(o[1], o[0])};
    }
    WAITL0(); bar();
    rsrc_t r0 = mkrsrc(p.ws + WS_KR, D * KRS * 2);
#pragma unroll
    for (int it = 0; it < 8; ++it) {
        const int item = tid + it * 256, ch = item >> 4, j = item & 15;
        __builtin_amdgcn_raw_buffer_store_b128(*(LAS u32x4*)(lds + P0_T + ch * TP + ((j ^ ((ch >> 3) & 15)) * 16)), r0, ((d0 + ch) * KRS + (1920 - s0) + 8 * j) * 2, 0, 16);
    }
    waitv<0>();
    if (lds_last(lds, L_CNT + 16, 4, lane) && lane == 0) st_flag(p.ws, FL_KR + kdc * 64 + c.pb / 5, get_tag(c));
}

struct Tail { u32x4 wd[4]; bool kr_ready; };
template <int MODE>
DI void loader_gemm1(Ctx& c, Tail& X) {
    const Params& p = c.p;
    const int lane = c.lane, w = c.wave - 4;
    LAS char* lds = c.lds;
    rsrc_t rq = mkrsrc(p.q, S * D * 2), rg_ = mkrsrc(p.wg, D * F * 2), ru_ = mkrsrc(p.wu, D * F * 2);
    unsigned vx[2], vw[3];
    {
        const int r = lane >> 3, slot = lane & 7;
        vx[0] = r * 1280 + ((slot ^ (r >> 1)) * 16);
        vx[1] = r * 1280 + ((slot ^ (r >> 1) ^ 4) * 16);
#pragma unroll
        for (int j = 0; j < 3; ++j) {
            const int L = 2 * j + (lane >> 5), kbl = L / 3, cc = L % 3, row = (lane & 31) >> 2, ch = (lane & 3) ^ (2 * kbl);
            vw[j] = (kbl * 8 + row) * 3072 + cc * 64 + ch * 16;
        }
    }
    const unsigned sx0 = (c.bs * 128 + 32 * w) * 1280, sw0 = 16 * w * 3072 + c.bf * 192;
    struct Kt { unsigned base, sx, sw; };
    auto kt_addr = [&](int t) {
        Kt a; a.base = c.ldsb + (int)((0x24924924u >> (2 * t)) & 3) * G1_STAGE; a.sx = sx0 + t * 128; a.sw = sw0 + t * 64 * 3072;
        asm volatile("" : "+s"(a.base), "+s"(a.sx), "+s"(a.sw));
        return a;
    };
    auto kt_go = [&](const Kt& a) {
#pragma unroll
        for (int i = 0; i < 4; ++i) dma16(a.base + (32 * w + 8 * i) * 128, vx[i & 1], rq, a.sx + i * 8 * 1280);
#pragma unroll
        for (int j = 0; j < 3; ++j) dma16(a.base + 16384 + w * 3072 + j * 1024, vw[j], rg_, a.sw);
#pragma unroll
        for (int j = 0; j < 3; ++j) dma16(a.base + 28672 + w * 3072 + j * 1024, vw[j], ru_, a.sw);
    };
    auto issue = [&](int t) { kt_go(kt_addr(t)); };
    if (MODE != 1) issue(0);
    if (w == 0) load_epoch(c);
    if (w < 2) {
        rsrc_t rgw = mkrsrc(p.gw, D * 2);
        if (w == 0 || lane < 16) dma16(c.ldsb + P0_GW + w * 1024, (w * 64 + lane) * 16, rgw, 0);
    }
    if (w == 3 && lane < 8) *(LAS unsigned*)(lds + L_CNT + lane * 4) = 0;
    if (MODE == 1) {
        waitv<0>();
        __builtin_amdgcn_s_sleep(SLEEP_LK);
        issue(0); issue(1);
        WAITL0(); bar();
        waitv<10>();
    } else {
        waitv<0>();
    }
    WAITL0(); bar();
    c.tag = get_tag(c);
    if (MODE != 1) issue(1);
    issue(2);
    const int grw = 8 * c.bf + (lane >> 3), gch = (lane & 7) ^ ((grw >> 1) & 7);
    float gacc = 0.f;
    auto gate_step = [&](int t) {
        const int buf = (int)((0x24924924u >> (2 * t)) & 3);
        const u32x4 xv = *(LAS u32x4*)(lds + buf * G1_STAGE + grw * 128 + (lane & 7) * 16);
        const u32x4 gv = *(LAS u32x4*)(lds + P0_GW + t * 128 + gch * 16);
#pragma unroll
        for (int i = 0; i < 4; ++i) gacc = dot2(xv[i], gv[i], gacc);
    };
    if (w == 3) gate_step(0);
#pragma unroll 1
    for (int t = 0; t < 6; ++t) {
        const Kt a = kt_addr(t + 3);
        waitv<10>(); WAITL0(); bar();
        kt_go(a);
        if (w == 3) gate_step(t + 1);
    }
    u32x4 kf4 = {0, 0, 0, 0};
    waitv<10>(); WAITL0(); bar();
    if (MODE == 0) kf4 = aload_sc1(flag_rsrc(p.ws), (FL_KR + (c.cb >> 5) * 64 + (lane & 3) * 4) * 4);
    issue(9);
    if (w == 3) gate_step(7);
    waitv<10>(); WAITL0(); bar();
    if (MODE == 0) {
        TIE(kf4);
        X.kr_ready = __all(all_tag(kf4, c.tag));
        rsrc_t rwd = mkrsrc(p.wd, F * D * 2);
        const int unit = c.cb * 3 + w, T = unit / 10, dl = unit % 10;
#pragma unroll
        for (int i = 0; i < 4; ++i) X.wd[i] = aload(rwd, w < 3 ? ((32 * T + 8 * i + (lane >> 3)) * D + 64 * dl + (lane & 7) * 8) * 2 : OOB);
    }
    if (w == 3) gate_step(8);
    if (MODE == 0) waitv<4>(); else waitv<0>();
    WAITL0(); bar();
    if (MODE == 0) {
        const int d = c.cb * 4 + w;
        const unsigned sl = c.ldsb + CV_BASE + w * CV_SLOT, vo = lane * 16;
        rsrc_t rf = mkrsrc(p.ssk, D * S * 2), r0 = mkrsrc(p.ws + WS_KR, D * KRS * 2);
#pragma unroll
        for (int i = 0; i < 4; ++i) dma16(sl + 512 + i * 1024, vo, rf, d * (S * 2) + i * 1024);
        if (X.kr_ready) {
#pragma unroll
            for (int i = 0; i < 4; ++i) dma16_sc1(sl + CV_KF + i * 1024, vo, r0, d * (KRS * 2) + i * 1024);
        }
        LAS char* slot = lds + CV_BASE + w * CV_SLOT;
        if (lane < 32) *(LAS u32x4*)(slot + lane * 16) = (u32x4){0, 0, 0, 0};
        else if (lane < 40) *(LAS u32x4*)(slot + CV_KF + ((lane - 32) >> 2) * CV_COPY + 4096 + (lane & 3) * 16) = (u32x4){0, 0, 0, 0};
    }
    if (w == 3) gate_step(9);
    WAITL0(); bar();
    waitv<0>();
    gacc = dpp_add<0x111>(gacc); gacc = dpp_add<0x112>(gacc); gacc = dpp_add<0x114>(gacc);
    if (w == 3) {
        rsrc_t rG = mkrsrc(p.ws + WS_G, S * 4);
        __builtin_amdgcn_raw_buffer_store_b32(__float_as_uint(sigmoidf(gacc)), rG, (lane & 7) == 7 ? (c.bs * 128 + grw) * 4 : OOB, 0, 16);
    }
    if (MODE == 0) {
#pragma unroll
        for (int i = 0; i < 4; ++i) TIE(X.wd[i]);
    }
}

__global__ __launch_bounds__(512, 2) void k_fused(Params p) {
    unsigned long long pc0;
    asm volatile("s_getpc_b64 %0" : "=s"(pc0));
    extern __shared__ __attribute__((aligned(16))) char smem[];
    LAS char* lds = (LAS char*)smem;
    const unsigned ldsb = (unsigned)(size_t)lds;
    const int wave = rfl(threadIdx.x >> 6), lane = threadIdx.x & 63;
    const int b = blockIdx.x, xcd = b & 7, idx = b >> 3;
    const bool has_conv = idx < 20;
    const int cb = xcd * 20 + idx, pb = xcd * 12 + idx - 20;
    const bool kvblock = !has_conv && pb < 80;
    const int bs = (xcd & 1) * 8 + (idx & 7), bf = (xcd >> 1) * 4 + (idx >> 3);
    const int st = xcd * 4 + (idx & 3), ds = idx >> 2;
    Ctx c; c.p = p; c.lds = lds; c.ldsb = ldsb; c.lane = lane; c.wave = wave; c.xcd = xcd; c.cb = cb; c.pb = pb; c.bs = bs; c.bf = bf; c.tag = 0;

    if (__builtin_expect(wave >= 4, 1)) {
        const int w = wave - 4;
        Tail X;
        X.kr_ready = false;
#pragma unroll
        for (int i = 0; i < 4; ++i) X.wd[i] = (u32x4){0, 0, 0, 0};
        if (has_conv) loader_gemm1<0>(c, X);
        else if (kvblock) loader_gemm1<1>(c, X);
        else loader_gemm1<2>(c, X);
        const unsigned tag = c.tag;
        const unsigned vo = lane * 16;
        rsrc_t rw = mkrsrc(p.ws + WS_WT, F * D * 2);
        if (has_conv) {
            LAS char* c0 = lds + CV_BASE + w * CV_SLOT + CV_KF;
            if (!X.kr_ready) {
                poll4<4>(p.ws, FL_KR + (cb >> 5) * 64, tag, lane);
                rsrc_t r0 = mkrsrc(p.ws + WS_KR, D * KRS * 2);
#pragma unroll
                for (int i = 0; i < 4; ++i) dma16_sc1(ldsb + CV_BASE + w * CV_SLOT + CV_KF + i * 1024, vo, r0, (cb * 4 + w) * (KRS * 2) + i * 1024);
                waitv<0>();
            }
            {
                const int unit = cb * 3 + w, Tk = unit / 10, dl = unit % 10, kh = Tk / 24, tt = (Tk % 24) >> 1, kk = Tk & 1;
                const int g4 = lane >> 4, q = (lane & 15) >> 2, pp = lane & 3, row = 4 * g4 + q;
                LAS char* buf = lds + WT_SCR + w * 4096;
#pragma unroll
                for (int i = 0; i < 4; ++i) *(LAS u32x4*)(buf + (8 * i + (lane >> 3)) * 128 + (lane & 7) * 16) = X.wd[i];
#pragma unroll
                for (int i = 0; i < 4; ++i) {
                    const int d16 = 4 * dl + i, dsl = d16 / 5, n = d16 % 5;
                    const s16x8 f = cat(trread(buf + row * 128 + 32 * i + 8 * pp), trread(buf + (row + 16) * 128 + 32 * i + 8 * pp));
                    __builtin_amdgcn_raw_buffer_store_b128(__builtin_bit_cast(u32x4, f), rw, w < 3 ? ((((dsl * 12 + tt) * 4 + kh * 2 + kk) * 5 + n) * 64 + lane) * 16 : OOB, 0, 16);
                }
            }
#pragma unroll
            for (int j = 0; j < 4; ++j) {
                const u32x4 a = *(LAS u32x4*)(c0 + j * 1024 + lane * 16);
                const unsigned nx = *(LAS unsigned*)(c0 + j * 1024 + lane * 16 + 16);
                *(LAS u32x4*)(c0 + CV_COPY + j * 1024 + lane * 16) =
                    (u32x4){__builtin_amdgcn_alignbit(a[1], a[0], 16), __builtin_amdgcn_alignbit(a[2], a[1], 16), __builtin_amdgcn_alignbit(a[3], a[2], 16), __builtin_amdgcn_alignbit(nx, a[3], 16)};
            }
        }
        waitv<0>();
        if (has_conv && lds_last(lds, L_CNT + 4, 4, lane) && lane < 8) st_flag(p.ws, FL_WT + lane * 256 + cb, tag);
        WAITL0();
        bar();
        const bool hi4 = ((st >> 1) & 7) >= 4;
        const int ne = hi4 ? 6 : 8;
        unsigned long long order;
        {
            const unsigned E = hi4 ? 0x876210u : 0x98763210u, L = hi4 ? 0xBA9543u : 0xBA54u;
            const int nl = 12 - ne, re = ds % ne, rl = ds % nl;
            const unsigned long long Er = (((unsigned long long)E << (4 * ne) | E) >> (4 * re)) & ((1ull << (4 * ne)) - 1);
            const unsigned long long Lr = (((unsigned long long)L << (4 * nl) | L) >> (4 * rl)) & ((1ull << (4 * nl)) - 1);
            order = Er | (Lr << (4 * ne));
        }
        rsrc_t rh = mkrsrc(p.ws + WS_H, S * F * 2);
        const unsigned sw2 = ds * (48 * 5 * 1024) + w * 5120, sh2 = ((st * 2 + (w & 1)) * 96 + (w >> 1) * 48) * 1024;
        auto stage_of = [&](int t) { return (int)((order >> (4 * t)) & 15); };
        struct St { unsigned hd, hs, wd, ws; };
        auto st_addr = [&](int t) {
            St a; const int ts = stage_of(t);
            a.hd = ldsb + hslot(t & 3) + w * 4096; a.hs = sh2 + ts * 4096; a.wd = ldsb + wslot(t & 3) + w * 5120; a.ws = sw2 + ts * G2_WST;
            asm volatile("" : "+s"(a.hd), "+s"(a.hs), "+s"(a.wd), "+s"(a.ws));
            return a;
        };
        auto w_go = [&](const St& a) {
#pragma unroll
            for (int j = 0; j < 5; ++j) dma16_sc1(a.wd + j * 1024, vo, rw, a.ws + j * 1024);
        };
        auto h_go = [&](const St& a) {
#pragma unroll
            for (int j = 0; j < 4; ++j) dma16_sc1(a.hd + j * 1024, vo, rh, a.hs + j * 1024);
        };
        auto w_dma = [&](int t) { w_go(st_addr(t)); };
        auto h_dma = [&](int t) { h_go(st_addr(t)); };
        const unsigned late_mask = hi4 ? 12u : 8u, hword = FL_H + (st >> 1) * 64;
        if (w == 0) {
            poll4<4>(p.ws, hword, tag, lane, 15u & ~late_mask);
            if (lane == 0) lds_set(lds, L_CNT + 20, 1u);
        } else {
            lds_wait(lds, L_CNT + 20);
        }
        h_dma(0); h_dma(1); h_dma(2);
        {
            const int ts = stage_of(lane >> 3 < 3 ? lane >> 3 : 0), T = 24 * ((lane >> 2) & 1) + 2 * ts + ((lane >> 1) & 1), dl = (5 * ds + (lane & 1) * 4) >> 2;
            const unsigned fo = (FL_WT + xcd * 256 + (10 * T + dl) / 3) * 4;
            if (w == 0) {
                rsrc_t r = flag_rsrc(p.ws);
                for (int spins = 0; spins < (1 << 18); ++spins) {
                    unsigned x;
                    asm volatile("buffer_load_dword %0, %1, %2, 0 offen sc1\n\ts_waitcnt vmcnt(0)" : "=&v"(x) : "v"(fo), "s"(r) : "memory");
                    if (__all(x == tag)) break;
                    __builtin_amdgcn_s_sleep(4);
                }
                if (lane == 0) lds_set(lds, L_CNT + 8, 1u);
            } else {
                lds_wait(lds, L_CNT + 8);
            }
        }
        w_dma(0); w_dma(1); w_dma(2);
        const u32x4 hl = aload_sc1(flag_rsrc(p.ws), (hword + (lane & 3) * 4) * 4);
        const u32x4 wl = aload_sc1(flag_rsrc(p.ws), (FL_WT + xcd * 256 + (lane < 40 ? lane : 0) * 4) * 4);
        waitv<0>();
        u32x4 hl_ = hl, wl_ = wl; TIE(hl_); TIE(wl_);
        const bool late_ok = __all(all_tag(hl_, tag));
        const bool wt_ok = __all(all_tag(wl_, tag));
        bar();
        if (!wt_ok) poll4<40>(p.ws, FL_WT + xcd * 256, tag, lane);
        h_dma(3); w_dma(3);
        bar();
#pragma unroll 1
        for (int t = 1; t <= 7; ++t) {
            const St a = st_addr(t + 3);
            waitv<9>(); bar();
            if (t + 3 == ne && !late_ok) poll4<4>(p.ws, hword, tag, lane);
            h_go(a); w_go(a);
        }
        rsrc_t ry = mkrsrc(p.ws + WS_YT, D * S * 2);
        waitv<9>(); bar();
        const u32x4 yf = aload_sc1(flag_rsrc(p.ws), (FL_Y + ds * 64 + (lane < 5 ? lane : 0) * 4) * 4);
        h_dma(11); w_dma(11);
        waitv<9>(); bar();
        u32x4 yf_ = yf; TIE(yf_);
        if (!__all(all_tag(yf_, tag))) poll4<5>(p.ws, FL_Y + ds * 64, tag, lane);
        u32x4 yv[3];
#pragma unroll
        for (int i = 0; i < 3; ++i) {
            const int it = w + 4 * i, ch = 8 * it + (lane >> 3);
            yv[i] = aload_sc1(ry, it < 10 ? ((80 * ds + ch) * S + 64 * st + (lane & 7) * 8) * 2 : OOB);
        }
        waitv<3>(); bar();
        {
            rsrc_t rq = mkrsrc(p.q, S * D * 2), rG = mkrsrc(p.ws + WS_G, S * 4);
#pragma unroll
            for (int i = 0; i < 3; ++i) {
                const int ch = (w + 4 * i) * 64 + lane, row = ch / 10;
                dma16(ldsb + G2_Q + (w + 4 * i) * 1024, ch < 640 ? (64 * st + row) * (D * 2) + 160 * ds + (ch - row * 10) * 16 : OOB, rq, 0);
            }
            if (w == 0) asm volatile("s_mov_b32 m0, %0\n\ts_nop 0\n\tbuffer_load_dword %1, %2, 0 offen sc1 lds" ::"s"(ldsb + G2_G), "v"((64 * st + lane) * 4), "s"(rG) : "m0", "memory");
        }
        bar();
        waitv<0>();
#pragma unroll
        for (int i = 0; i < 3; ++i) {
            const int it = w + 4 * i, ch = 8 * it + (lane >> 3);
            TIE(yv[i]);
            if (it < 10) *(LAS u32x4*)(lds + G2_Y + ch * 128 + (lane & 7) * 16) = yv[i];
        }
        WAITL0();
        bar();
        if (b == 0 && w == 0) {
            rsrc_t r = flag_rsrc(p.ws);
            for (int spins = 0; spins < (1 << 18); ++spins) {
                u32x4 x;
                asm volatile("buffer_load_dwordx4 %0, %1, %2, 0 offen sc1\n\ts_waitcnt vmcnt(0)" : "=&v"(x) : "v"((FL_H + (lane >> 2) * 64 + (lane & 3) * 4) * 4), "s"(r) : "memory");
                if (__all(all_tag(x, tag))) break;
                __builtin_amdgcn_s_sleep(4);
            }
            if (lane == 0) st_flag(p.ws, FL_EPOCH, tag);
        }
    } else {
        unsigned tag = 0;
        {
            const int wsx = wave & 1, wf = wave >> 1;
            const int g = lane >> 4, fr = lane & 15, q = (lane & 15) >> 2, pp = lane & 3;
            int oa[3];
#pragma unroll
            for (int i = 0; i < 3; ++i) {
                const int ft = 3 * wf + i, cc = ft >> 1, half = ft & 1;
                oa[i] = 16384 + (g * 3 + cc) * 512 + q * 64 + 16 * ((2 * half + (pp >> 1)) ^ (2 * (g & 1))) + 8 * (pp & 1);
            }
            const int ob0 = (64 * wsx + fr) * 128 + ((g ^ (fr >> 1)) * 16);
            const int ob1 = (64 * wsx + fr) * 128 + ((g ^ (fr >> 1) ^ 4) * 16);
            f32x4 accg[3][4], accu[3][4];
#pragma unroll
            for (int i = 0; i < 3; ++i)
#pragma unroll
                for (int m = 0; m < 4; ++m) { accg[i][m] = (f32x4){0, 0, 0, 0}; accu[i][m] = (f32x4){0, 0, 0, 0}; }
            struct Fr { s16x8 a[6]; s16x8 b[4]; } fa, fb;
            auto rd = [&](Fr& f, int ks, int sb) {
#pragma unroll
                for (int m = 0; m < 4; ++m) f.b[m] = *(LAS s16x8*)(lds + sb + (ks ? ob1 : ob0) + m * 2048);
#pragma unroll
                for (int i = 0; i < 3; ++i) {
                    f.a[i] = cat(trread(lds + sb + oa[i] + ks * 6144), trread(lds + sb + oa[i] + ks * 6144 + 256));
                    f.a[3 + i] = cat(trread(lds + sb + oa[i] + ks * 6144 + 12288), trread(lds + sb + oa[i] + ks * 6144 + 256 + 12288));
                }
            };
            auto mm = [&](const Fr& f) {
#pragma unroll
                for (int i = 0; i < 3; ++i)
#pragma unroll
                    for (int m = 0; m < 4; ++m) {
                        accg[i][m] = mfma16(f.a[i], f.b[m], accg[i][m]);
                        accu[i][m] = mfma16(f.a[3 + i], f.b[m], accu[i][m]);
                    }
            };
            auto interleave = [&]() {
#pragma unroll
                for (int i = 0; i < 16; ++i) { SGB(0x008, 1); SGB(0x100, 1); }
                SGB(0x008, 8);
                SB0();
            };
            const int pi = kvblock ? pb : (has_conv ? cb : 160 + pb - 80), np = kvblock ? 80 : 176, t0 = kvblock ? 6 : 2;
            if (wave == 3) {
                rsrc_t rc = mkrsrc((const void*)pc0, CODE_PF);
                prefetch_lines(ldsb + L_DUMMY, rc, lane < CODE_PF / 4096 + 1 ? (idx * (CODE_PF / 4096 + 1) + lane) * 128 : OOB);
            }
            if (kvblock) kv_prepare(c); else __builtin_amdgcn_s_sleep(SLEEP_C);
            {
                rsrc_t rpf = mkrsrc(wave == 0 ? p.q : (wave == 1 ? p.wg : p.wu), wave == 0 ? S * D * 2 : D * F * 2);
                const int nl = wave == 0 ? 8192 : 6144;
                for (int l0 = pi * 64; l0 < nl && wave < 3; l0 += np * 64) {
                    const int l = l0 + lane;
                    prefetch_lines(ldsb + L_DUMMY, rpf, wave == 0 ? (l >> 2) * 1280 + (t0 + (l & 3)) * 128 : (t0 * 64 + l / 24) * 3072 + (l % 24) * 128);
                }
                if (kvblock && wave == 3) {
                    rsrc_t rwd = mkrsrc(p.wd, F * D * 2), rf = mkrsrc(p.ssk, D * S * 2);
                    for (int l0 = pb * 64; l0 < 15360; l0 += 80 * 64) prefetch_lines(ldsb + L_DUMMY, rwd, (l0 + lane) * 128);
                    for (int l0 = pb * 64; l0 < 20480; l0 += 80 * 64) prefetch_lines(ldsb + L_DUMMY, rf, (l0 + lane) * 128);
                }
            }
            __builtin_amdgcn_s_setprio(1);
            bar();
            rd(fa, 0, 0);
            int sb = 0;
#pragma unroll 1
            for (int t = 0; t < 9; ++t) {
                rd(fb, 1, sb); mm(fa); interleave();
                WAITL0();
                bar();
                sb = (sb == 2 * G1_STAGE) ? 0 : sb + G1_STAGE;
                rd(fa, 0, sb); mm(fb); interleave();
            }
            rd(fb, 1, sb); mm(fa); interleave();
            WAITL0();
            bar();
            mm(fb);
            tag = get_tag(c);
            rsrc_t rh = mkrsrc(p.ws + WS_H, S * F * 2);
#pragma unroll
            for (int mp = 0; mp < 2; ++mp)
#pragma unroll
                for (int i = 0; i < 3; ++i) {
                    u32x4 o;
#pragma unroll
                    for (int e = 0; e < 4; ++e) {
                        const f32x4 gv = accg[i][2 * mp + (e >> 1)], uv = accu[i][2 * mp + (e >> 1)];
                        const f32x2 t = (f32x2){gv[2 * (e & 1)], gv[2 * (e & 1) + 1]} * -1.44269504089f;
                        const f32x2 dn = (f32x2){__builtin_amdgcn_exp2f(t[0]), __builtin_amdgcn_exp2f(t[1])} + 1.f;
                        const f32x2 hv = (f32x2){uv[2 * (e & 1)], uv[2 * (e & 1) + 1]} * (f32x2){__builtin_amdgcn_rcpf(dn[0]), __builtin_amdgcn_rcpf(dn[1])};
                        o[e] = cvtpk(hv[0], hv[1]);
                    }
                    const int sp = bs * 4 + wsx * 2 + mp, ft = bf * 6 + wf * 3 + i;
                    __builtin_amdgcn_raw_buffer_store_b128(o, rh, ((sp * 96 + ft) * 64 + lane) * 16, 0, 16);
                }
        }
        auto publish_h = [&]() {
            waitv<0>();
            if (lds_last(lds, L_CNT, 4, lane) && lane == 0) st_flag(p.ws, FL_H + bs * 64 + bf, tag);
        };
        bar();
        publish_h();
        if (has_conv) {
            const int ca = lane & 15, cg = lane >> 4;
            f32x4 cacc[8];
            u32x2 ckv[8];
#pragma unroll
            for (int n = 0; n < 8; ++n) cacc[n] = (f32x4){0, 0, 0, 0};
            const unsigned slot = ldsb + CV_BASE + wave * CV_SLOT;
            const unsigned sdw = *(const unsigned*)(p.sd + ((cb * 4 + wave) & ~1));
            unsigned pg = slot + 480 + 32 * ca + 16 * cg;
            unsigned pf[8];
#pragma unroll
            for (int j = 0; j < 8; ++j) pf[j] = slot + CV_KF + (1 - (ca & 1)) * CV_COPY + 476 + 16 * cg - 4 * (ca >> 1) + 512 * (7 - j);
            struct Res { s16x8 G[8]; s16x4 Fl[8], Fh[8]; } RA, RB;
            auto rd1 = [&](Res& R, int k) {
                const int j = k / 3, m = k % 3;
                if (m == 0) asm volatile("ds_read2_b32 %0, %1 offset0:0 offset1:1" : "=v"(R.Fl[j]) : "v"(pf[j]));
                else if (m == 1) asm volatile("ds_read2_b32 %0, %1 offset0:2 offset1:3" : "=v"(R.Fh[j]) : "v"(pf[j]));
                else asm volatile("ds_read_b128 %0, %1 offset:%2" : "=v"(R.G[j]) : "v"(pg), "n"(512 * j));
            };
            auto land = [&](Res& R) {
                asm volatile("s_waitcnt lgkmcnt(0)"
                             : "+v"(R.G[0]), "+v"(R.G[1]), "+v"(R.G[2]), "+v"(R.G[3]), "+v"(R.G[4]), "+v"(R.G[5]), "+v"(R.G[6]), "+v"(R.G[7]),
                               "+v"(R.Fl[0]), "+v"(R.Fl[1]), "+v"(R.Fl[2]), "+v"(R.Fl[3]), "+v"(R.Fl[4]), "+v"(R.Fl[5]), "+v"(R.Fl[6]), "+v"(R.Fl[7]));
                asm volatile(""
                             : "+v"(R.Fh[0]), "+v"(R.Fh[1]), "+v"(R.Fh[2]), "+v"(R.Fh[3]), "+v"(R.Fh[4]), "+v"(R.Fh[5]), "+v"(R.Fh[6]), "+v"(R.Fh[7]));
                SB0();
            };
            auto next_addr = [&](int j) {
                if (j < 8) asm volatile("v_subrev_u32 %0, 64, %0" : "+v"(pf[j]));
                else asm volatile("v_subrev_u32 %0, 64, %0" : "+v"(pg));
            };
            auto residue = [&](Res& R, Res& N) {
                int k = 0;
#pragma unroll
                for (int I = 0; I < 8; ++I) {
                    const s16x8 Fv = cat(R.Fl[I], R.Fh[I]);
#pragma unroll
                    for (int n = I; n < 8; ++n) {
                        cacc[n] = mfma16(Fv, R.G[n - I], cacc[n]);
                        if (k < 24) rd1(N, k);
                        else if (k < 33) next_addr(k - 24);
                        ++k;
                        SB0();
                    }
                }
                land(N);
            };
#pragma unroll
            for (int k = 0; k < 24; ++k) rd1(RA, k);
#pragma unroll
            for (int j = 0; j < 9; ++j) next_addr(j);
            land(RA);
#pragma unroll 1
            for (int r2 = 0; r2 < 4; ++r2) {
                residue(RA, RB);
                residue(RB, RA);
            }
#pragma unroll
            for (int n = 0; n < 8; ++n) ckv[n] = *(LAS u32x2*)(lds + CV_BASE + wave * CV_SLOT + CV_KF + 2 * (2044 - 256 * n - 16 * ca - 4 * cg));
            const float Dd = (wave & 1) ? bfhi(sdw) : bflo(sdw);
            const int d = cb * 4 + wave;
            rsrc_t ry = mkrsrc(p.ws + WS_YT, D * S * 2);
            u32x2 o[8];
#pragma unroll
            for (int n = 0; n < 8; ++n) {
                o[n][0] = cvtpk(cacc[n][0] + bfhi(ckv[n][1]) * Dd, cacc[n][1] + bflo(ckv[n][1]) * Dd);
                o[n][1] = cvtpk(cacc[n][2] + bfhi(ckv[n][0]) * Dd, cacc[n][3] + bflo(ckv[n][0]) * Dd);
            }
#pragma unroll
            for (int n = 0; n < 8; n += 2) {
                auto x = __builtin_amdgcn_permlane16_swap(o[n][0], o[n + 1][0], false, false);
                auto y = __builtin_amdgcn_permlane16_swap(o[n][1], o[n + 1][1], false, false);
                const u32x4 val = {x[0], y[0], x[1], y[1]};
                const int s = 256 * (n + (cg & 1)) + 16 * ca + 4 * (cg & 2);
                __builtin_amdgcn_raw_buffer_store_b128(val, ry, (d * S + s) * 2, 0, 16);
            }
        }
        {
            const int sp = wave & 1, kh = wave >> 1;
            const int s16 = lane & 15, g = lane >> 4, q = s16 >> 2, pp = lane & 3;
            const int srow = 64 * st + 32 * sp + 16 * kh + s16;
            f32x4 acc[2][5];
#pragma unroll
            for (int n = 0; n < 5; ++n) { acc[0][n] = (f32x4){0, 0, 0, 0}; acc[1][n] = (f32x4){0, 0, 0, 0}; }
            const unsigned pw = ldsb + kh * 10240 + lane * 16, ph = ldsb + wave * 4096 + lane * 16;
            struct Fr2 { s16x8 w[5]; u32x4 h[2]; } fa, fb;
            auto rd2 = [&](Fr2& f, int s4, int kk) {
                LAS char* bh = (LAS char*)(size_t)(ph + hslot(s4) + kk * 2048);
                LAS char* bw = (LAS char*)(size_t)(pw + wslot(s4) + kk * 5120);
                f.h[0] = *(LAS u32x4*)bh; f.h[1] = *(LAS u32x4*)(bh + 1024);
#pragma unroll
                for (int n = 0; n < 5; ++n) f.w[n] = *(LAS s16x8*)(bw + n * 1024);
            };
            auto swp = [&](Fr2& f) {
                unsigned a2 = f.h[0][2], a3 = f.h[0][3], b0 = f.h[1][0], b1 = f.h[1][1];
                asm volatile("v_swap_b32 %0, %1\n\tv_swap_b32 %2, %3\n\ts_nop 1" : "+v"(a2), "+v"(b0), "+v"(a3), "+v"(b1));
                f.h[0][2] = a2; f.h[0][3] = a3; f.h[1][0] = b0; f.h[1][1] = b1;
            };
            auto step = [&](const Fr2& f) {
                const s16x8 h0 = __builtin_bit_cast(s16x8, f.h[0]), h1 = __builtin_bit_cast(s16x8, f.h[1]);
#pragma unroll
                for (int n = 0; n < 5; ++n) { acc[0][n] = mfma16(f.w[n], h0, acc[0][n]); acc[1][n] = mfma16(f.w[n], h1, acc[1][n]); }
            };
            auto il2 = [&]() {
#pragma unroll
                for (int i = 0; i < 7; ++i) { SGB(0x008, 1); SGB(0x100, 1); }
                SGB(0x008, 3);
                SB0();
            };
            WAITL0();
            bar();
            rd2(fa, 0, 0);
            swp(fa);
#pragma unroll 1
            for (int t = 0; t < 12; ++t) {
                rd2(fb, t & 3, 1);
                step(fa);
                il2();
                swp(fb);
                SB0();
                if (t == 3) waitv<0>();
                bar();
                if (t == 3 && has_conv && wave == 0 && lane == 0) st_flag(p.ws, FL_Y + (cb / 20) * 64 + cb % 20, tag);
                rd2(fa, (t + 1) & 3, 0);
                step(fb);
                il2();
                swp(fa);
                SB0();
            }
#pragma unroll
            for (int n = 0; n < 5; ++n) *(LAS f32x4*)(lds + G2_RED + (wave * 5 + n) * 1024 + lane * 16) = kh ? acc[0][n] : acc[1][n];
            WAITL0();
            bar();
            const int lrow = 32 * sp + 16 * kh + s16;
            const float gt = *(LAS float*)(lds + G2_G + lrow * 4);
#pragma unroll
            for (int n = 0; n < 5; ++n) {
                const u32x2 qn = *(LAS u32x2*)(lds + G2_Q + lrow * 160 + (16 * n + 4 * g) * 2);
                const f32x4 o4 = (kh ? acc[1][n] : acc[0][n]) + *(LAS f32x4*)(lds + G2_RED + ((wave ^ 2) * 5 + n) * 1024 + lane * 16);
                const u32x2 yv = __builtin_bit_cast(u32x2, trread(lds + G2_Y + (16 * n + 4 * g + q) * 128 + (32 * sp + 16 * kh + 4 * pp) * 2));
                const float o0 = gt * (bflo(qn[0]) * bflo(yv[0])) + (1.f - gt) * o4[0];
                const float o1 = gt * (bfhi(qn[0]) * bfhi(yv[0])) + (1.f - gt) * o4[1];
                const float o2 = gt * (bflo(qn[1]) * bflo(yv[1])) + (1.f - gt) * o4[2];
                const float o3 = gt * (bfhi(qn[1]) * bfhi(yv[1])) + (1.f - gt) * o4[3];
                *(u32x2*)(p.out + (size_t)srow * D + 80 * ds + 16 * n + 4 * g) = (u32x2){cvtpk(o0, o1), cvtpk(o2, o3)};
            }
        }
    }
}

extern "C" void kernel_launch(void* const* d_in, const int* in_sizes, int n_in,
                              void* d_out, int out_size, void* d_ws, size_t ws_size,
                              hipStream_t stream) {
    Params p;
    p.q = (const u16*)d_in[0]; p.k = (const u16*)d_in[1]; p.v = (const u16*)d_in[2];
    p.sk = (const u16*)d_in[3]; p.ssk = (const u16*)d_in[4]; p.sd = (const u16*)d_in[5];
    p.wg = (const u16*)d_in[7]; p.wu = (const u16*)d_in[8]; p.wd = (const u16*)d_in[9]; p.gw = (const u16*)d_in[10];
    p.out = (u16*)d_out; p.ws = (char*)d_ws;
    hipFuncSetAttribute((const void*)k_fused, hipFuncAttributeMaxDynamicSharedMemorySize, LDS_BYTES);
    k_fused<<<256, 512, LDS_BYTES, stream>>>(p);
}
```

```cpp
#include <hip/hip_runtime.h>
#include <hip/hip_bf16.h>
#include <stdint.h>

typedef unsigned short u16;
typedef short s16x4 __attribute__((ext_vector_type(4)));
typedef short s16x8 __attribute__((ext_vector_type(8)));
typedef __bf16 b16x8 __attribute__((ext_vector_type(8)));
typedef __bf16 b16x2 __attribute__((ext_vector_type(2)));
typedef float f32x2 __attribute__((ext_vector_type(2)));
typedef float f32x4 __attribute__((ext_vector_type(4)));
typedef unsigned u32x2 __attribute__((ext_vector_type(2)));
typedef unsigned u32x4 __attribute__((ext_vector_type(4)));
#define LAS __attribute__((address_space(3)))
#define GAS __attribute__((address_space(1)))
#define DI __device__ __forceinline__

static constexpr int S = 2048, D = 640, F = 1536;
static constexpr int KRS = 2112;
static constexpr unsigned WS_H = 0, WS_KR = 8u << 20, WS_YT = 14u << 20, WS_G = 17u << 20, WS_WT = 18u << 20, WS_FLAG = 24u << 20;
static constexpr unsigned FL_EPOCH = 0, FL_KR = 1024, FL_WT = 2048  , FL_H = 4096, FL_Y = 6144;
static constexpr unsigned OOB = 0xF0000000u;

struct Params {
    const u16 *q, *k, *v, *sk, *ssk, *sd, *wg, *wu, *wd, *gw;
    u16* out; char* ws;
};

DI float bflo(unsigned x) { return __uint_as_float(x << 16); }
DI float bfhi(unsigned x) { return __uint_as_float(x & 0xffff0000u); }
DI unsigned cvtpk(float lo, float hi) { f32x2 v = {lo, hi}; b16x2 b = __builtin_convertvector(v, b16x2); return __builtin_bit_cast(unsigned, b); }
DI unsigned rfl(unsigned x) { return __builtin_amdgcn_readfirstlane(x); }
DI float sigmoidf(float x) { return __builtin_amdgcn_rcpf(1.f + __builtin_amdgcn_exp2f(x * -1.44269504089f)); }
DI f32x4 mfma16(s16x8 a, s16x8 b, f32x4 c) {
    return __builtin_amdgcn_mfma_f32_16x16x32_bf16(__builtin_bit_cast(b16x8, a), __builtin_bit_cast(b16x8, b), c, 0, 0, 0);
}
DI s16x4 trread(LAS char* p) { return __builtin_amdgcn_ds_read_tr16_b64_v4i16((LAS s16x4*)p); }
DI s16x8 cat(s16x4 a, s16x4 b) { return __builtin_shufflevector(a, b, 0, 1, 2, 3, 4, 5, 6, 7); }
typedef __amdgpu_buffer_rsrc_t rsrc_t;
DI rsrc_t mkrsrc(const void* p, unsigned bytes) { return __builtin_amdgcn_make_buffer_rsrc((void*)p, 0, bytes, 0x00020000); }
DI void dma16(unsigned m0v, unsigned voff, rsrc_t r, unsigned soff) {
    asm volatile("s_mov_b32 m0, %0\n\ts_nop 0\n\tbuffer_load_dwordx4 %1, %2, %3 offen lds" ::"s"(m0v), "v"(voff), "s"(r), "s"(soff) : "m0", "memory");
}
DI void dma16_sc1(unsigned m0v, unsigned voff, rsrc_t r, unsigned soff) {
    asm volatile("s_mov_b32 m0, %0\n\ts_nop 0\n\tbuffer_load_dwordx4 %1, %2, %3 offen sc1 lds" ::"s"(m0v), "v"(voff), "s"(r), "s"(soff) : "m0", "memory");
}
DI u32x4 aload(rsrc_t r, unsigned voff) { u32x4 x; asm volatile("buffer_load_dwordx4 %0, %1, %2, 0 offen" : "=v"(x) : "v"(voff), "s"(r) : "memory"); return x; }
DI u32x4 aload_sc1(rsrc_t r, unsigned voff) { u32x4 x; asm volatile("buffer_load_dwordx4 %0, %1, %2, 0 offen sc1" : "=v"(x) : "v"(voff), "s"(r) : "memory"); return x; }
DI void prefetch_lines(unsigned scratch, rsrc_t r, unsigned voff) {
    asm volatile("s_mov_b32 m0, %0\n\ts_nop 0\n\tbuffer_load_dword %1, %2, 0 offen lds" ::"s"(scratch), "v"(voff), "s"(r) : "m0", "memory");
}
template <int N> DI void waitv() { asm volatile("s_waitcnt vmcnt(%0)" ::"n"(N) : "memory"); }
DI float dot2(unsigned a, unsigned b, float c) { return __builtin_amdgcn_fdot2_f32_bf16(__builtin_bit_cast(b16x2, a), __builtin_bit_cast(b16x2, b), c, false); }
template <int CTRL> DI float dpp_add(float x) { return x + __int_as_float(__builtin_amdgcn_update_dpp(0, __float_as_int(x), CTRL, 0xf, 0xf, true)); }
#define TIE(x) asm volatile("" : "+v"(x))
#define WAITL0() asm volatile("s_waitcnt lgkmcnt(0)" ::: "memory")
DI void bar() { asm volatile("" ::: "memory"); __builtin_amdgcn_s_barrier(); asm volatile("" ::: "memory"); }
#define SGB(m, n) __builtin_amdgcn_sched_group_barrier(m, n, 0)
#define SB0() __builtin_amdgcn_sched_barrier(0)
#define RLX_AGENT __ATOMIC_RELAXED, __HIP_MEMORY_SCOPE_AGENT

constexpr int CODE_PF = 36864;
constexpr int SLEEP_LK = 4;
constexpr int SLEEP_C = 6;

DI void st_flag(char* ws, unsigned word, unsigned tag) { __hip_atomic_store((GAS unsigned*)(ws + WS_FLAG) + word, tag, RLX_AGENT); }
DI rsrc_t flag_rsrc(char* ws) { return mkrsrc(ws + WS_FLAG, 32768); }
DI bool all_tag(u32x4 x, unsigned tag) { return x[0] == tag && x[1] == tag && x[2] == tag && x[3] == tag; }
template <int NL>
DI void poll4(char* ws, unsigned word, unsigned tag, int lane, unsigned care = 15u) {
    rsrc_t r = flag_rsrc(ws);
    for (int spins = 0; spins < (1 << 18); ++spins) {
        u32x4 x;
        asm volatile("buffer_load_dwordx4 %0, %1, %2, 0 offen sc1\n\ts_waitcnt vmcnt(0)" : "=&v"(x) : "v"((word + (lane < NL ? lane : 0) * 4) * 4), "s"(r) : "memory");
        const bool ok = (x[0] == tag || !(care & 1)) && (x[1] == tag || !(care & 2)) && (x[2] == tag || !(care & 4)) && (x[3] == tag || !(care & 8));
        if (__all(ok)) return;
        __builtin_amdgcn_s_sleep(4);
    }
}
DI void lds_set(LAS char* lds, int off, unsigned v) { __hip_atomic_store((LAS unsigned*)(lds + off), v, __ATOMIC_RELAXED, __HIP_MEMORY_SCOPE_WORKGROUP); }
DI unsigned lds_get(LAS char* lds, int off) { return rfl(__hip_atomic_load((LAS unsigned*)(lds + off), __ATOMIC_RELAXED, __HIP_MEMORY_SCOPE_WORKGROUP)); }
DI unsigned lds_wait(LAS char* lds, int off) {
    unsigned v = 0;
    for (int spins = 0; spins < (1 << 22); ++spins) {
        v = lds_get(lds, off);
        if (v) break;
        __builtin_amdgcn_s_sleep(8);
    }
    asm volatile("" ::: "memory");
    return v;
}
DI bool lds_last(LAS char* lds, int off, unsigned n, int lane) {
    unsigned old = 0;
    if (lane == 0) old = __hip_atomic_fetch_add((LAS unsigned*)(lds + off), 1u, __ATOMIC_RELAXED, __HIP_MEMORY_SCOPE_WORKGROUP);
    return rfl(old) == n - 1;
}

constexpr int G1_STAGE = 40960;
constexpr int P0_LDS = 122880;
constexpr int TP = 272;
constexpr int P0_T = P0_LDS, P0_GW = P0_LDS + 128 * TP;
constexpr int CV_BASE = G1_STAGE;
constexpr int CV_COPY = 4160, CV_KF = 4608, CV_SLOT = CV_KF + 2 * CV_COPY;
constexpr int G2_WST = 20480, G2_HST = 16384;
constexpr int G2_W0 = 0, G2_W2 = CV_BASE + 4 * CV_SLOT, G2_W3 = CV_BASE;
constexpr int G2_H0 = G2_W2 + G2_WST, G2_H3 = G2_W3 + G2_WST;
constexpr int G2_Y = G2_H3 + G2_HST;
constexpr int G2_Q = G2_H0, G2_G = G2_H0 + 12288;
constexpr int WT_SCR = G2_H0;
constexpr int G2_RED = 0;
constexpr unsigned long long G2_WTAB = (unsigned long long)(G2_W0 >> 8) | (unsigned long long)((G2_W0 + G2_WST) >> 8) << 16 | (unsigned long long)(G2_W2 >> 8) << 32 | (unsigned long long)(G2_W3 >> 8) << 48;
constexpr unsigned long long G2_HTAB = (unsigned long long)(G2_H0 >> 8) | (unsigned long long)((G2_H0 + G2_HST) >> 8) << 16 | (unsigned long long)((G2_H0 + 2 * G2_HST) >> 8) << 32 | (unsigned long long)(G2_H3 >> 8) << 48;
DI int wslot(int s4) { return (int)((G2_WTAB >> (16 * s4)) & 0xFFFF) << 8; }
DI int hslot(int s4) { return (int)((G2_HTAB >> (16 * s4)) & 0xFFFF) << 8; }
constexpr int L_DUMMY = 163328;
constexpr int L_CNT = 163776;
constexpr int L_EPOCH = 163824;
constexpr int LDS_BYTES = 163840;

struct Ctx {
    Params p; LAS char* lds; unsigned ldsb; int lane, wave, xcd, cb, pb, bs, bf; unsigned tag;
};
DI void load_epoch(const Ctx& c) {
    rsrc_t r = mkrsrc(c.p.ws + WS_FLAG, 64);
    if (c.lane == 0) asm volatile("s_mov_b32 m0, %0\n\ts_nop 0\n\tbuffer_load_dword %1, %2, 0 offen sc1 lds" ::"s"(c.ldsb + L_EPOCH), "v"(0u), "s"(r) : "m0", "memory");
}
DI unsigned get_tag(const Ctx& c) { return lds_get(c.lds, L_EPOCH) + 1u; }

DI void kv_prepare(const Ctx& c) {
    const Params& p = c.p;
    LAS char* lds = c.lds;
    const int lane = c.lane, wave = c.wave;
    const int kdc = c.pb % 5, s0 = (c.pb / 5) * 128, d0 = kdc * 128, tid = c.wave * 64 + lane, c16 = tid & 15, rg = tid >> 4, s = s0 + 8 * rg;
    u32x4 kr[11], vr[8], w4[4];
    {
        rsrc_t rk = mkrsrc(p.k, S * D * 2), rv = mkrsrc(p.v, S * D * 2), rsk = mkrsrc(p.sk, D * 8);
#pragma unroll
        for (int i = 0; i < 11; ++i) kr[i] = aload(rk, (unsigned)(((s - 3 + i) * D + d0 + 8 * c16) * 2));
#pragma unroll
        for (int i = 0; i < 8; ++i) vr[i] = aload(rv, ((s + i) * D + d0 + 8 * c16) * 2);
#pragma unroll
        for (int i = 0; i < 4; ++i) w4[i] = aload(rsk, (d0 + 8 * c16) * 8 + i * 16);
    }
    waitv<0>();
#pragma unroll
    for (int i = 0; i < 11; ++i) TIE(kr[i]);
#pragma unroll
    for (int i = 0; i < 8; ++i) TIE(vr[i]);
#pragma unroll
    for (int i = 0; i < 4; ++i) TIE(w4[i]);
    LAS char* tw = lds + P0_T + 8 * c16 * TP + (((15 - rg) ^ c16) * 16);
#pragma unroll
    for (int e = 0; e < 8; ++e) {
        const unsigned sel = (e & 1) ? 0x07060302u : 0x05040100u;
        const unsigned wa = w4[e >> 1][(e & 1) * 2], wb = w4[e >> 1][(e & 1) * 2 + 1];
        unsigned pr[10];
#pragma unroll
        for (int i = 0; i < 10; ++i) pr[i] = __builtin_amdgcn_perm(kr[i][e >> 1], kr[i + 1][e >> 1], sel);
        float o[8];
#pragma unroll
        for (int r = 0; r < 8; ++r) o[r] = dot2(pr[r + 2], wa, dot2(pr[r], wb, 0.f)) * ((e & 1) ? bfhi(vr[r][e >> 1]) : bflo(vr[r][e >> 1]));
        *(LAS u32x4*)(tw + e * TP) = (u32x4){cvtpk(o[7], o[6]), cvtpk(o[5], o[4]), cvtpk(o[3], o[2]), cvtpk(o[1], o[0])};
    }
    WAITL0(); bar();
    rsrc_t r0 = mkrsrc(p.ws + WS_KR, D * KRS * 2);
#pragma unroll
    for (int it = 0; it < 8; ++it) {
        const int item = tid + it * 256, ch = item >> 4, j = item & 15;
        __builtin_amdgcn_raw_buffer_store_b128(*(LAS u32x4*)(lds + P0_T + ch * TP + ((j ^ ((ch >> 3) & 15)) * 16)), r0, ((d0 + ch) * KRS + (1920 - s0) + 8 * j) * 2, 0, 16);
    }
    waitv<0>();
    if (lds_last(lds, L_CNT + 16, 4, lane) && lane == 0) st_flag(p.ws, FL_KR + kdc * 64 + c.pb / 5, get_tag(c));
}

struct Tail { u32x4 wd[4]; bool kr_ready; };
template <int MODE>
DI void loader_gemm1(Ctx& c, Tail& X) {
    const Params& p = c.p;
    const int lane = c.lane, w = c.wave - 4, wave = c.wave;
    LAS char* lds = c.lds;
    rsrc_t rq = mkrsrc(p.q, S * D * 2), rg_ = mkrsrc(p.wg, D * F * 2), ru_ = mkrsrc(p.wu, D * F * 2);
    unsigned vx[2], vw[3];
    {
        const int r = lane >> 3, slot = lane & 7;
        vx[0] = r * 1280 + ((slot ^ (r >> 1)) * 16);
        vx[1] = r * 1280 + ((slot ^ (r >> 1) ^ 4) * 16);
#pragma unroll
        for (int j = 0; j < 3; ++j) {
            const int L = 2 * j + (lane >> 5), kbl = L / 3, cc = L % 3, row = (lane & 31) >> 2, ch = (lane & 3) ^ (2 * kbl);
            vw[j] = (kbl * 8 + row) * 3072 + cc * 64 + ch * 16;
        }
    }
    const unsigned sx0 = (c.bs * 128 + 32 * w) * 1280, sw0 = 16 * w * 3072 + c.bf * 192;
    auto issue = [&](int t) {
        const unsigned base = c.ldsb + (int)((0x24924924u >> (2 * t)) & 3) * G1_STAGE;
#pragma unroll
        for (int i = 0; i < 4; ++i) dma16(base + (32 * w + 8 * i) * 128, vx[i & 1], rq, sx0 + i * 8 * 1280 + t * 128);
#pragma unroll
        for (int j = 0; j < 3; ++j) dma16(base + 16384 + w * 3072 + j * 1024, vw[j], rg_, sw0 + t * 64 * 3072);
#pragma unroll
        for (int j = 0; j < 3; ++j) dma16(base + 28672 + w * 3072 + j * 1024, vw[j], ru_, sw0 + t * 64 * 3072);
    };
    if (MODE != 1) issue(0);
    if (w == 0) load_epoch(c);
    if (w < 2) {
        rsrc_t rgw = mkrsrc(p.gw, D * 2);
        if (w == 0 || lane < 16) dma16(c.ldsb + P0_GW + w * 1024, (w * 64 + lane) * 16, rgw, 0);
    }
    if (w == 3 && lane < 8) *(LAS unsigned*)(lds + L_CNT + lane * 4) = 0;
    if (MODE == 1) {
        waitv<0>();
        __builtin_amdgcn_s_sleep(SLEEP_LK);
        issue(0); issue(1);
        WAITL0(); bar();
        waitv<10>();
    } else {
        waitv<0>();
    }
    WAITL0(); bar();
    c.tag = get_tag(c);
    if (MODE != 1) issue(1);
    issue(2);
    const int grw = 8 * c.bf + (lane >> 3), gch = (lane & 7) ^ ((grw >> 1) & 7);
    float gacc = 0.f;
    auto gate_step = [&](int t) {
        const int buf = (int)((0x24924924u >> (2 * t)) & 3);
        const u32x4 xv = *(LAS u32x4*)(lds + buf * G1_STAGE + grw * 128 + (lane & 7) * 16);
        const u32x4 gv = *(LAS u32x4*)(lds + P0_GW + t * 128 + gch * 16);
#pragma unroll
        for (int i = 0; i < 4; ++i) gacc = dot2(xv[i], gv[i], gacc);
    };
    if (w == 3) gate_step(0);
#pragma unroll 1
    for (int t = 0; t < 6; ++t) {
        waitv<10>(); WAITL0(); bar();
        issue(t + 3);
        if (w == 3) gate_step(t + 1);
    }
    u32x4 kf4 = {0, 0, 0, 0};
    waitv<10>(); WAITL0(); bar();
    if (MODE == 0) kf4 = aload_sc1(flag_rsrc(p.ws), (FL_KR + (c.cb >> 5) * 64 + (lane & 3) * 4) * 4);
    issue(9);
    if (w == 3) gate_step(7);
    waitv<10>(); WAITL0(); bar();
    if (MODE == 0) {
        TIE(kf4);
        X.kr_ready = __all(all_tag(kf4, c.tag));
        rsrc_t rwd = mkrsrc(p.wd, F * D * 2);
        const int unit = c.cb * 3 + w, T = unit / 10, dl = unit % 10;
#pragma unroll
        for (int i = 0; i < 4; ++i) X.wd[i] = aload(rwd, w < 3 ? ((32 * T + 8 * i + (lane >> 3)) * D + 64 * dl + (lane & 7) * 8) * 2 : OOB);
    }
    if (w == 3) gate_step(8);
    if (MODE == 0) waitv<4>(); else waitv<0>();
    WAITL0(); bar();
    if (MODE == 0) {
        const int d = c.cb * 4 + w;
        const unsigned sl = c.ldsb + CV_BASE + w * CV_SLOT, vo = lane * 16;
        rsrc_t rf = mkrsrc(p.ssk, D * S * 2), r0 = mkrsrc(p.ws + WS_KR, D * KRS * 2);
#pragma unroll
        for (int i = 0; i < 4; ++i) dma16(sl + 512 + i * 1024, vo, rf, d * (S * 2) + i * 1024);
        if (X.kr_ready) {
#pragma unroll
            for (int i = 0; i < 4; ++i) dma16_sc1(sl + CV_KF + i * 1024, vo, r0, d * (KRS * 2) + i * 1024);
        }
        LAS char* slot = lds + CV_BASE + w * CV_SLOT;
        if (lane < 32) *(LAS u32x4*)(slot + lane * 16) = (u32x4){0, 0, 0, 0};
        else if (lane < 40) *(LAS u32x4*)(slot + CV_KF + ((lane - 32) >> 2) * CV_COPY + 4096 + (lane & 3) * 16) = (u32x4){0, 0, 0, 0};
    }
    if (w == 3) gate_step(9);
    WAITL0(); bar();
    waitv<0>();
    gacc = dpp_add<0x111>(gacc); gacc = dpp_add<0x112>(gacc); gacc = dpp_add<0x114>(gacc);
    if (w == 3) {
        rsrc_t rG = mkrsrc(p.ws + WS_G, S * 4);
        __builtin_amdgcn_raw_buffer_store_b32(__float_as_uint(sigmoidf(gacc)), rG, (lane & 7) == 7 ? (c.bs * 128 + grw) * 4 : OOB, 0, 16);
    }
    if (MODE == 0) {
#pragma unroll
        for (int i = 0; i < 4; ++i) TIE(X.wd[i]);
    }
}

__global__ __launch_bounds__(512, 2) void k_fused(Params p) {
    unsigned long long pc0;
    asm volatile("s_getpc_b64 %0" : "=s"(pc0));
    extern __shared__ __attribute__((aligned(16))) char smem[];
    LAS char* lds = (LAS char*)smem;
    const unsigned ldsb = (unsigned)(size_t)lds;
    const int wave = rfl(threadIdx.x >> 6), lane = threadIdx.x & 63;
    const int b = blockIdx.x, xcd = b & 7, idx = b >> 3;
    const bool has_conv = idx < 20;
    const int cb = xcd * 20 + idx, pb = xcd * 12 + idx - 20;
    const bool kvblock = !has_conv && pb < 80;
    const int bs = (xcd & 1) * 8 + (idx & 7), bf = (xcd >> 1) * 4 + (idx >> 3);
    const int st = xcd * 4 + (idx & 3), ds = idx >> 2;
    Ctx c; c.p = p; c.lds = lds; c.ldsb = ldsb; c.lane = lane; c.wave = wave; c.xcd = xcd; c.cb = cb; c.pb = pb; c.bs = bs; c.bf = bf; c.tag = 0;

    if (__builtin_expect(wave >= 4, 1)) {
        const int w = wave - 4;
        Tail X;
        X.kr_ready = false;
#pragma unroll
        for (int i = 0; i < 4; ++i) X.wd[i] = (u32x4){0, 0, 0, 0};
        if (has_conv) loader_gemm1<0>(c, X);
        else if (kvblock) loader_gemm1<1>(c, X);
        else loader_gemm1<2>(c, X);
        const unsigned tag = c.tag;
        const unsigned vo = lane * 16;
        rsrc_t rw = mkrsrc(p.ws + WS_WT, F * D * 2);
        if (has_conv) {
            LAS char* c0 = lds + CV_BASE + w * CV_SLOT + CV_KF;
            if (!X.kr_ready) {
                poll4<4>(p.ws, FL_KR + (cb >> 5) * 64, tag, lane);
                rsrc_t r0 = mkrsrc(p.ws + WS_KR, D * KRS * 2);
#pragma unroll
                for (int i = 0; i < 4; ++i) dma16_sc1(ldsb + CV_BASE + w * CV_SLOT + CV_KF + i * 1024, vo, r0, (cb * 4 + w) * (KRS * 2) + i * 1024);
                waitv<0>();
            }
            {
                const int unit = cb * 3 + w, Tk = unit / 10, dl = unit % 10, kh = Tk / 24, tt = (Tk % 24) >> 1, kk = Tk & 1;
                const int g4 = lane >> 4, q = (lane & 15) >> 2, pp = lane & 3, row = 4 * g4 + q;
                LAS char* buf = lds + WT_SCR + w * 4096;
#pragma unroll
                for (int i = 0; i < 4; ++i) *(LAS u32x4*)(buf + (8 * i + (lane >> 3)) * 128 + (lane & 7) * 16) = X.wd[i];
#pragma unroll
                for (int i = 0; i < 4; ++i) {
                    const int d16 = 4 * dl + i, dsl = d16 / 5, n = d16 % 5;
                    const s16x8 f = cat(trread(buf + row * 128 + 32 * i + 8 * pp), trread(buf + (row + 16) * 128 + 32 * i + 8 * pp));
                    __builtin_amdgcn_raw_buffer_store_b128(__builtin_bit_cast(u32x4, f), rw, w < 3 ? ((((dsl * 12 + tt) * 4 + kh * 2 + kk) * 5 + n) * 64 + lane) * 16 : OOB, 0, 16);
                }
            }
#pragma unroll
            for (int j = 0; j < 4; ++j) {
                const u32x4 a = *(LAS u32x4*)(c0 + j * 1024 + lane * 16);
                const unsigned nx = *(LAS unsigned*)(c0 + j * 1024 + lane * 16 + 16);
                *(LAS u32x4*)(c0 + CV_COPY + j * 1024 + lane * 16) =
                    (u32x4){__builtin_amdgcn_alignbit(a[1], a[0], 16), __builtin_amdgcn_alignbit(a[2], a[1], 16), __builtin_amdgcn_alignbit(a[3], a[2], 16), __builtin_amdgcn_alignbit(nx, a[3], 16)};
            }
        }
        waitv<0>();
        if (has_conv && lds_last(lds, L_CNT + 4, 4, lane) && lane < 8) st_flag(p.ws, FL_WT + lane * 256 + cb, tag);
        WAITL0();
        bar();
        const bool hi4 = ((st >> 1) & 7) >= 4;
        const int ne = hi4 ? 6 : 8;
        unsigned long long order;
        {
            const unsigned E = hi4 ? 0x876210u : 0x98763210u, L = hi4 ? 0xBA9543u : 0xBA54u;
            const int nl = 12 - ne, re = ds % ne, rl = ds % nl;
            const unsigned long long Er = (((unsigned long long)E << (4 * ne) | E) >> (4 * re)) & ((1ull << (4 * ne)) - 1);
            const unsigned long long Lr = (((unsigned long long)L << (4 * nl) | L) >> (4 * rl)) & ((1ull << (4 * nl)) - 1);
            order = Er | (Lr << (4 * ne));
        }
        rsrc_t rh = mkrsrc(p.ws + WS_H, S * F * 2);
        const unsigned sw2 = ds * (48 * 5 * 1024) + w * 5120, sh2 = ((st * 2 + (w & 1)) * 96 + (w >> 1) * 48) * 1024;
        auto stage_of = [&](int t) { return (int)((order >> (4 * t)) & 15); };
        auto w_dma = [&](int t) {
            const unsigned dst = ldsb + wslot(t & 3) + w * 5120, src = sw2 + stage_of(t) * G2_WST;
#pragma unroll
            for (int j = 0; j < 5; ++j) dma16_sc1(dst + j * 1024, vo, rw, src + j * 1024);
        };
        auto h_dma = [&](int t) {
            const unsigned dst = ldsb + hslot(t & 3) + w * 4096, src = sh2 + stage_of(t) * 4096;
#pragma unroll
            for (int j = 0; j < 4; ++j) dma16_sc1(dst + j * 1024, vo, rh, src + j * 1024);
        };
        const unsigned late_mask = hi4 ? 12u : 8u, hword = FL_H + (st >> 1) * 64;
        if (w == 0) {
            poll4<4>(p.ws, hword, tag, lane, 15u & ~late_mask);
            if (lane == 0) lds_set(lds, L_CNT + 20, 1u);
        } else {
            lds_wait(lds, L_CNT + 20);
        }
        h_dma(0); h_dma(1); h_dma(2);
        {
            const int ts = stage_of(lane >> 3 < 3 ? lane >> 3 : 0), T = 24 * ((lane >> 2) & 1) + 2 * ts + ((lane >> 1) & 1), dl = (5 * ds + (lane & 1) * 4) >> 2;
            const unsigned fo = (FL_WT + xcd * 256 + (10 * T + dl) / 3) * 4;
            if (w == 0) {
                rsrc_t r = flag_rsrc(p.ws);
                for (int spins = 0; spins < (1 << 18); ++spins) {
                    unsigned x;
                    asm volatile("buffer_load_dword %0, %1, %2, 0 offen sc1\n\ts_waitcnt vmcnt(0)" : "=&v"(x) : "v"(fo), "s"(r) : "memory");
                    if (__all(x == tag)) break;
                    __builtin_amdgcn_s_sleep(4);
                }
                if (lane == 0) lds_set(lds, L_CNT + 8, 1u);
            } else {
                lds_wait(lds, L_CNT + 8);
            }
        }
        w_dma(0); w_dma(1); w_dma(2);
        const u32x4 hl = aload_sc1(flag_rsrc(p.ws), (hword + (lane & 3) * 4) * 4);
        const u32x4 wl = aload_sc1(flag_rsrc(p.ws), (FL_WT + xcd * 256 + (lane < 40 ? lane : 0) * 4) * 4);
        waitv<0>();
        u32x4 hl_ = hl, wl_ = wl; TIE(hl_); TIE(wl_);
        const bool late_ok = __all(all_tag(hl_, tag));
        const bool wt_ok = __all(all_tag(wl_, tag));
        bar();
        if (!wt_ok) poll4<40>(p.ws, FL_WT + xcd * 256, tag, lane);
        h_dma(3); w_dma(3);
        bar();
#pragma unroll 1
        for (int t = 1; t <= 8; ++t) {
            waitv<9>(); bar();
            if (t + 3 == ne && !late_ok) poll4<4>(p.ws, hword, tag, lane);
            h_dma(t + 3); w_dma(t + 3);
        }
        rsrc_t ry = mkrsrc(p.ws + WS_YT, D * S * 2);
        const u32x4 yf = aload_sc1(flag_rsrc(p.ws), (FL_Y + ds * 64 + (lane < 5 ? lane : 0) * 4) * 4);
        waitv<10>(); bar();
        waitv<0>();
        u32x4 yf_ = yf; TIE(yf_);
        if (!__all(all_tag(yf_, tag))) poll4<5>(p.ws, FL_Y + ds * 64, tag, lane);
        u32x4 yv[3];
#pragma unroll
        for (int i = 0; i < 3; ++i) {
            const int it = w + 4 * i, ch = 8 * it + (lane >> 3);
            yv[i] = aload_sc1(ry, it < 10 ? ((80 * ds + ch) * S + 64 * st + (lane & 7) * 8) * 2 : OOB);
        }
        {
            rsrc_t rq = mkrsrc(p.q, S * D * 2), rG = mkrsrc(p.ws + WS_G, S * 4);
#pragma unroll
            for (int i = 0; i < 3; ++i) {
                const int ch = (w + 4 * i) * 64 + lane, row = ch / 10;
                dma16(ldsb + G2_Q + (w + 4 * i) * 1024, ch < 640 ? (64 * st + row) * (D * 2) + 160 * ds + (ch - row * 10) * 16 : OOB, rq, 0);
            }
            if (w == 0) asm volatile("s_mov_b32 m0, %0\n\ts_nop 0\n\tbuffer_load_dword %1, %2, 0 offen sc1 lds" ::"s"(ldsb + G2_G), "v"((64 * st + lane) * 4), "s"(rG) : "m0", "memory");
        }
        bar();
        bar();
        waitv<0>();
#pragma unroll
        for (int i = 0; i < 3; ++i) {
            const int it = w + 4 * i, ch = 8 * it + (lane >> 3);
            TIE(yv[i]);
            if (it < 10) *(LAS u32x4*)(lds + G2_Y + ch * 128 + (lane & 7) * 16) = yv[i];
        }
        WAITL0();
        bar();
        if (b == 0 && w == 0) {
            rsrc_t r = flag_rsrc(p.ws);
            for (int spins = 0; spins < (1 << 18); ++spins) {
                u32x4 x;
                asm volatile("buffer_load_dwordx4 %0, %1, %2, 0 offen sc1\n\ts_waitcnt vmcnt(0)" : "=&v"(x) : "v"((FL_H + (lane >> 2) * 64 + (lane & 3) * 4) * 4), "s"(r) : "memory");
                if (__all(all_tag(x, tag))) break;
                __builtin_amdgcn_s_sleep(4);
            }
            if (lane == 0) st_flag(p.ws, FL_EPOCH, tag);
        }
    } else {
        unsigned tag = 0;
        {
            const int wsx = wave & 1, wf = wave >> 1;
            const int g = lane >> 4, fr = lane & 15, q = (lane & 15) >> 2, pp = lane & 3;
            int oa[3];
#pragma unroll
            for (int i = 0; i < 3; ++i) {
                const int ft = 3 * wf + i, cc = ft >> 1, half = ft & 1;
                oa[i] = 16384 + (g * 3 + cc) * 512 + q * 64 + 16 * ((2 * half + (pp >> 1)) ^ (2 * (g & 1))) + 8 * (pp & 1);
            }
            const int ob0 = (64 * wsx + fr) * 128 + ((g ^ (fr >> 1)) * 16);
            const int ob1 = (64 * wsx + fr) * 128 + ((g ^ (fr >> 1) ^ 4) * 16);
            f32x4 accg[3][4], accu[3][4];
#pragma unroll
            for (int i = 0; i < 3; ++i)
#pragma unroll
                for (int m = 0; m < 4; ++m) { accg[i][m] = (f32x4){0, 0, 0, 0}; accu[i][m] = (f32x4){0, 0, 0, 0}; }
            struct Fr { s16x8 a[6]; s16x8 b[4]; } fa, fb;
            auto rd = [&](Fr& f, int ks, int sb) {
#pragma unroll
                for (int m = 0; m < 4; ++m) f.b[m] = *(LAS s16x8*)(lds + sb + (ks ? ob1 : ob0) + m * 2048);
#pragma unroll
                for (int i = 0; i < 3; ++i) {
                    f.a[i] = cat(trread(lds + sb + oa[i] + ks * 6144), trread(lds + sb + oa[i] + ks * 6144 + 256));
                    f.a[3 + i] = cat(trread(lds + sb + oa[i] + ks * 6144 + 12288), trread(lds + sb + oa[i] + ks * 6144 + 256 + 12288));
                }
            };
            auto mm = [&](const Fr& f) {
#pragma unroll
                for (int i = 0; i < 3; ++i)
#pragma unroll
                    for (int m = 0; m < 4; ++m) {
                        accg[i][m] = mfma16(f.a[i], f.b[m], accg[i][m]);
                        accu[i][m] = mfma16(f.a[3 + i], f.b[m], accu[i][m]);
                    }
            };
            auto interleave = [&]() {
#pragma unroll
                for (int i = 0; i < 16; ++i) { SGB(0x008, 1); SGB(0x100, 1); }
                SGB(0x008, 8);
                SB0();
            };
            const int pi = kvblock ? pb : (has_conv ? cb : 160 + pb - 80), np = kvblock ? 80 : 176, t0 = kvblock ? 6 : 2;
            if (wave == 3) {
                rsrc_t rc = mkrsrc((const void*)pc0, CODE_PF);
                prefetch_lines(ldsb + L_DUMMY, rc, lane < CODE_PF / 4096 + 1 ? (idx * (CODE_PF / 4096 + 1) + lane) * 128 : OOB);
            }
            if (kvblock) kv_prepare(c); else __builtin_amdgcn_s_sleep(SLEEP_C);
            {
                rsrc_t rpf = mkrsrc(wave == 0 ? p.q : (wave == 1 ? p.wg : p.wu), wave == 0 ? S * D * 2 : D * F * 2);
                const int nl = wave == 0 ? 8192 : 6144;
                for (int l0 = pi * 64; l0 < nl && wave < 3; l0 += np * 64) {
                    const int l = l0 + lane;
                    prefetch_lines(ldsb + L_DUMMY, rpf, wave == 0 ? (l >> 2) * 1280 + (t0 + (l & 3)) * 128 : (t0 * 64 + l / 24) * 3072 + (l % 24) * 128);
                }
                if (kvblock && wave == 3) {
                    rsrc_t rwd = mkrsrc(p.wd, F * D * 2), rf = mkrsrc(p.ssk, D * S * 2);
                    for (int l0 = pb * 64; l0 < 15360; l0 += 80 * 64) prefetch_lines(ldsb + L_DUMMY, rwd, (l0 + lane) * 128);
                    for (int l0 = pb * 64; l0 < 20480; l0 += 80 * 64) prefetch_lines(ldsb + L_DUMMY, rf, (l0 + lane) * 128);
                }
            }
            __builtin_amdgcn_s_setprio(1);
            bar();
            rd(fa, 0, 0);
            int sb = 0;
#pragma unroll 1
            for (int t = 0; t < 9; ++t) {
                rd(fb, 1, sb); mm(fa); interleave();
                WAITL0();
                bar();
                sb = (sb == 2 * G1_STAGE) ? 0 : sb + G1_STAGE;
                rd(fa, 0, sb); mm(fb); interleave();
            }
            rd(fb, 1, sb); mm(fa); interleave();
            WAITL0();
            bar();
            mm(fb);
            tag = get_tag(c);
            rsrc_t rh = mkrsrc(p.ws + WS_H, S * F * 2);
#pragma unroll
            for (int mp = 0; mp < 2; ++mp)
#pragma unroll
                for (int i = 0; i < 3; ++i) {
                    float hv[8];
#pragma unroll
                    for (int e = 0; e < 8; ++e) hv[e] = accu[i][2 * mp + (e >> 2)][e & 3] * sigmoidf(accg[i][2 * mp + (e >> 2)][e & 3]);
                    const u32x4 o = {cvtpk(hv[0], hv[1]), cvtpk(hv[2], hv[3]), cvtpk(hv[4], hv[5]), cvtpk(hv[6], hv[7])};
                    const int sp = bs * 4 + wsx * 2 + mp, ft = bf * 6 + wf * 3 + i;
                    __builtin_amdgcn_raw_buffer_store_b128(o, rh, ((sp * 96 + ft) * 64 + lane) * 16, 0, 16);
                }
        }
        auto publish_h = [&]() {
            waitv<0>();
            if (lds_last(lds, L_CNT, 4, lane) && lane == 0) st_flag(p.ws, FL_H + bs * 64 + bf, tag);
        };
        bar();
        publish_h();
        if (has_conv) {
            const int ca = lane & 15, cg = lane >> 4;
            f32x4 cacc[8];
            u32x2 ckv[8];
#pragma unroll
            for (int n = 0; n < 8; ++n) cacc[n] = (f32x4){0, 0, 0, 0};
            const unsigned slot = ldsb + CV_BASE + wave * CV_SLOT;
            const unsigned sdw = *(const unsigned*)(p.sd + ((cb * 4 + wave) & ~1));
            unsigned pg = slot + 480 + 32 * ca + 16 * cg;
            unsigned pf[8];
#pragma unroll
            for (int j = 0; j < 8; ++j) pf[j] = slot + CV_KF + (1 - (ca & 1)) * CV_COPY + 476 + 16 * cg - 4 * (ca >> 1) + 512 * (7 - j);
            struct Res { s16x8 G[8]; s16x4 Fl[8], Fh[8]; } RA, RB;
            auto rd1 = [&](Res& R, int k) {
                const int j = k / 3, m = k % 3;
                if (m == 0) asm volatile("ds_read2_b32 %0, %1 offset0:0 offset1:1" : "=v"(R.Fl[j]) : "v"(pf[j]));
                else if (m == 1) asm volatile("ds_read2_b32 %0, %1 offset0:2 offset1:3" : "=v"(R.Fh[j]) : "v"(pf[j]));
                else asm volatile("ds_read_b128 %0, %1 offset:%2" : "=v"(R.G[j]) : "v"(pg), "n"(512 * j));
            };
            auto land = [&](Res& R) {
                asm volatile("s_waitcnt lgkmcnt(0)"
                             : "+v"(R.G[0]), "+v"(R.G[1]), "+v"(R.G[2]), "+v"(R.G[3]), "+v"(R.G[4]), "+v"(R.G[5]), "+v"(R.G[6]), "+v"(R.G[7]),
                               "+v"(R.Fl[0]), "+v"(R.Fl[1]), "+v"(R.Fl[2]), "+v"(R.Fl[3]), "+v"(R.Fl[4]), "+v"(R.Fl[5]), "+v"(R.Fl[6]), "+v"(R.Fl[7]));
                asm volatile(""
                             : "+v"(R.Fh[0]), "+v"(R.Fh[1]), "+v"(R.Fh[2]), "+v"(R.Fh[3]), "+v"(R.Fh[4]), "+v"(R.Fh[5]), "+v"(R.Fh[6]), "+v"(R.Fh[7]));
                SB0();
            };
            auto next_addr = [&](int j) {
                if (j < 8) asm volatile("v_subrev_u32 %0, 64, %0" : "+v"(pf[j]));
                else asm volatile("v_subrev_u32 %0, 64, %0" : "+v"(pg));
            };
            auto residue = [&](Res& R, Res& N) {
                int k = 0;
#pragma unroll
                for (int I = 0; I < 8; ++I) {
                    const s16x8 Fv = cat(R.Fl[I], R.Fh[I]);
#pragma unroll
                    for (int n = I; n < 8; ++n) {
                        cacc[n] = mfma16(Fv, R.G[n - I], cacc[n]);
                        if (k < 24) rd1(N, k);
                        else if (k < 33) next_addr(k - 24);
                        ++k;
                        SB0();
                    }
                }
                land(N);
            };
#pragma unroll
            for (int k = 0; k < 24; ++k) rd1(RA, k);
#pragma unroll
            for (int j = 0; j < 9; ++j) next_addr(j);
            land(RA);
#pragma unroll 1
            for (int r2 = 0; r2 < 4; ++r2) {
                residue(RA, RB);
                residue(RB, RA);
            }
#pragma unroll
            for (int n = 0; n < 8; ++n) ckv[n] = *(LAS u32x2*)(lds + CV_BASE + wave * CV_SLOT + CV_KF + 2 * (2044 - 256 * n - 16 * ca - 4 * cg));
            const float Dd = (wave & 1) ? bfhi(sdw) : bflo(sdw);
            const int d = cb * 4 + wave;
            rsrc_t ry = mkrsrc(p.ws + WS_YT, D * S * 2);
            u32x2 o[8];
#pragma unroll
            for (int n = 0; n < 8; ++n) {
                o[n][0] = cvtpk(cacc[n][0] + bfhi(ckv[n][1]) * Dd, cacc[n][1] + bflo(ckv[n][1]) * Dd);
                o[n][1] = cvtpk(cacc[n][2] + bfhi(ckv[n][0]) * Dd, cacc[n][3] + bflo(ckv[n][0]) * Dd);
            }
#pragma unroll
            for (int n = 0; n < 8; n += 2) {
                auto x = __builtin_amdgcn_permlane16_swap(o[n][0], o[n + 1][0], false, false);
                auto y = __builtin_amdgcn_permlane16_swap(o[n][1], o[n + 1][1], false, false);
                const u32x4 val = {x[0], y[0], x[1], y[1]};
                const int s = 256 * (n + (cg & 1)) + 16 * ca + 4 * (cg & 2);
                __builtin_amdgcn_raw_buffer_store_b128(val, ry, (d * S + s) * 2, 0, 16);
            }
        }
        {
            const int sp = wave & 1, kh = wave >> 1;
            const int s16 = lane & 15, g = lane >> 4, q = s16 >> 2, pp = lane & 3;
            const int srow = 64 * st + 32 * sp + 16 * kh + s16;
            f32x4 acc[2][5];
#pragma unroll
            for (int n = 0; n < 5; ++n) { acc[0][n] = (f32x4){0, 0, 0, 0}; acc[1][n] = (f32x4){0, 0, 0, 0}; }
            const unsigned pw = ldsb + kh * 10240 + lane * 16, ph = ldsb + wave * 4096 + lane * 16;
            struct Fr2 { s16x8 w[5]; u32x4 h[2]; } fa, fb;
            auto rd2 = [&](Fr2& f, int s4, int kk) {
                LAS char* bh = (LAS char*)(size_t)(ph + hslot(s4) + kk * 2048);
                LAS char* bw = (LAS char*)(size_t)(pw + wslot(s4) + kk * 5120);
                f.h[0] = *(LAS u32x4*)bh; f.h[1] = *(LAS u32x4*)(bh + 1024);
#pragma unroll
                for (int n = 0; n < 5; ++n) f.w[n] = *(LAS s16x8*)(bw + n * 1024);
            };
            auto swp = [&](Fr2& f) {
                unsigned a2 = f.h[0][2], a3 = f.h[0][3], b0 = f.h[1][0], b1 = f.h[1][1];
                asm volatile("v_swap_b32 %0, %1\n\tv_swap_b32 %2, %3\n\ts_nop 1" : "+v"(a2), "+v"(b0), "+v"(a3), "+v"(b1));
                f.h[0][2] = a2; f.h[0][3] = a3; f.h[1][0] = b0; f.h[1][1] = b1;
            };
            auto step = [&](const Fr2& f) {
                const s16x8 h0 = __builtin_bit_cast(s16x8, f.h[0]), h1 = __builtin_bit_cast(s16x8, f.h[1]);
#pragma unroll
                for (int n = 0; n < 5; ++n) { acc[0][n] = mfma16(f.w[n], h0, acc[0][n]); acc[1][n] = mfma16(f.w[n], h1, acc[1][n]); }
            };
            auto il2 = [&]() {
#pragma unroll
                for (int i = 0; i < 7; ++i) { SGB(0x008, 1); SGB(0x100, 1); }
                SGB(0x008, 3);
                SB0();
            };
            WAITL0();
            bar();
            rd2(fa, 0, 0);
            swp(fa);
#pragma unroll 1
            for (int t = 0; t < 12; ++t) {
                rd2(fb, t & 3, 1);
                step(fa);
                il2();
                swp(fb);
                SB0();
                if (t == 3) waitv<0>();
                bar();
                if (t == 3 && has_conv && wave == 0 && lane == 0) st_flag(p.ws, FL_Y + (cb / 20) * 64 + cb % 20, tag);
                rd2(fa, (t + 1) & 3, 0);
                step(fb);
                il2();
                swp(fa);
                SB0();
            }
#pragma unroll
            for (int n = 0; n < 5; ++n) *(LAS f32x4*)(lds + G2_RED + (wave * 5 + n) * 1024 + lane * 16) = kh ? acc[0][n] : acc[1][n];
            WAITL0();
            bar();
            const int lrow = 32 * sp + 16 * kh + s16;
            const float gt = *(LAS float*)(lds + G2_G + lrow * 4);
#pragma unroll
            for (int n = 0; n < 5; ++n) {
                const u32x2 qn = *(LAS u32x2*)(lds + G2_Q + lrow * 160 + (16 * n + 4 * g) * 2);
                const f32x4 o4 = (kh ? acc[1][n] : acc[0][n]) + *(LAS f32x4*)(lds + G2_RED + ((wave ^ 2) * 5 + n) * 1024 + lane * 16);
                const u32x2 yv = __builtin_bit_cast(u32x2, trread(lds + G2_Y + (16 * n + 4 * g + q) * 128 + (32 * sp + 16 * kh + 4 * pp) * 2));
                const float o0 = gt * (bflo(qn[0]) * bflo(yv[0])) + (1.f - gt) * o4[0];
                const float o1 = gt * (bfhi(qn[0]) * bfhi(yv[0])) + (1.f - gt) * o4[1];
                const float o2 = gt * (bflo(qn[1]) * bflo(yv[1])) + (1.f - gt) * o4[2];
                const float o3 = gt * (bfhi(qn[1]) * bfhi(yv[1])) + (1.f - gt) * o4[3];
                *(u32x2*)(p.out + (size_t)srow * D + 80 * ds + 16 * n + 4 * g) = (u32x2){cvtpk(o0, o1), cvtpk(o2, o3)};
            }
        }
    }
}

extern "C" void kernel_launch(void* const* d_in, const int* in_sizes, int n_in,
                              void* d_out, int out_size, void* d_ws, size_t ws_size,
                              hipStream_t stream) {
    Params p;
    p.q = (const u16*)d_in[0]; p.k = (const u16*)d_in[1]; p.v = (const u16*)d_in[2];
    p.sk = (const u16*)d_in[3]; p.ssk = (const u16*)d_in[4]; p.sd = (const u16*)d_in[5];
    p.wg = (const u16*)d_in[7]; p.wu = (const u16*)d_in[8]; p.wd = (const u16*)d_in[9]; p.gw = (const u16*)d_in[10];
    p.out = (u16*)d_out; p.ws = (char*)d_ws;
    hipFuncSetAttribute((const void*)k_fused, hipFuncAttributeMaxDynamicSharedMemorySize, LDS_BYTES);
    k_fused<<<256, 512, LDS_BYTES, stream>>>(p);
}
```
